```python
import torch

def _softmax(x, axis=-1):
    return torch.softmax(x, dim=axis)
def _gelu(x):
    return torch.nn.functional.gelu(x, approximate="tanh")
def _silu(x):
    return torch.nn.functional.silu(x)
def _sigmoid(x):
    return torch.sigmoid(x)
def _layernorm(x, axis=-1, eps=1e-5):
    m = x.mean(dim=axis, keepdim=True)
    v = x.var(dim=axis, keepdim=True, unbiased=False)
    return (x - m) / torch.sqrt(v + eps)
def _scatter_add(tgt, idx, src, axis=0):
    return tgt.clone().index_add_(axis, idx, src)
def _segment_sum(src, seg, n):
    out = torch.zeros((n,) + src.shape[1:], dtype=src.dtype)
    return out.index_add_(0, seg, src)
def _pad(x, pw):
    flat = tuple(v for pair in reversed(pw) for v in pair)
    return torch.nn.functional.pad(x, flat)

kernel_name = 'interleave_layers__gqa_block_cached__glu_gated_reglu__decode'

def setup_inputs():

    _w = (lambda a: (lambda t: t.to(torch.bfloat16) if t.is_floating_point() else t)(torch.as_tensor(a)))
    def _A_setup():
        B, Sq, D = 1, 1, 512
        Hq, Hkv, Sk, Dh = 32, 8, 65536, 128
        assert Hq % Hkv == 0, (Hq, Hkv)
        return {
            "x": _w((lambda *s: torch.randn(*s, dtype=torch.bfloat16) * 0.05)(B, Sq, D)),
            "k_cache": _w((lambda *s: torch.randn(*s, dtype=torch.bfloat16) * 0.8)(B, Hkv, Sk, Dh)),
            "v_cache": _w((lambda *s: torch.randn(*s, dtype=torch.bfloat16) * 1.6)(B, Hkv, Sk, Dh)),
            "wq": _w((lambda *s: torch.randn(*s, dtype=torch.bfloat16) * 0.8)(D, Hq * Dh) * (D ** -0.5)),
            "wo": _w((lambda *s: torch.randn(*s, dtype=torch.bfloat16) * 1.6)(Hq * Dh, D) * ((Hq * Dh) ** -0.5)),
        }
    _A_in = _A_setup()

    def _B_setup():
        B, S, D, F = 1, 1, 512, 1792
        return {
            "x": _w((lambda *s: torch.randn(*s, dtype=torch.bfloat16) * 0.05)(B, S, D)),
            "W_gate": _w((lambda *s: torch.randn(*s, dtype=torch.bfloat16) * 0.2)(D, F) * (2.0 / D) ** 0.5),
            "W_up": _w((lambda *s: torch.randn(*s, dtype=torch.bfloat16) * 0.2)(D, F) * (2.0 / D) ** 0.5),
            "W_down": _w((lambda *s: torch.randn(*s, dtype=torch.bfloat16) * 0.2)(F, D) * (1.0 / F) ** 0.5),
        }
    _B_in = _B_setup()

    return {
        "A_x": _A_in["x"],
        "A_k_cache": _A_in["k_cache"],
        "A_v_cache": _A_in["v_cache"],
        "A_wq": _A_in["wq"],
        "A_wo": _A_in["wo"],
        "B_x": _B_in["x"],
        "B_W_gate": _B_in["W_gate"],
        "B_W_up": _B_in["W_up"],
        "B_W_down": _B_in["W_down"],
    }

def reference(A_x, A_k_cache, A_v_cache, A_wq, A_wo, B_x, B_W_gate, B_W_up, B_W_down):
    def _A_ref(x, k_cache, v_cache, wq, wo):
        B, Sq, D = x.shape
        Hq, Dh = 32, 128
        Hkv = k_cache.shape[1]
        rep = Hq // Hkv
        q = (lambda x, *d: x.permute(*d))((x @ wq).reshape(B, Sq, Hq, Dh), 0, 2, 1, 3)
        k = (lambda x, n, axis: torch.repeat_interleave(x, n, dim=axis))(k_cache, rep, axis=1)
        v = (lambda x, n, axis: torch.repeat_interleave(x, n, dim=axis))(v_cache, rep, axis=1)
        scores = torch.einsum("bhqd,bhkd->bhqk", q, k) * (Dh ** -0.5)
        p = _softmax(scores, axis=-1)
        o = torch.einsum("bhqk,bhkd->bhqd", p, v)
        return (lambda x, *d: x.permute(*d))(o, 0, 2, 1, 3).reshape(B, Sq, Hq * Dh) @ wo

    def _B_ref(x, W_gate, W_up, W_down):
        gate = torch.einsum("bsd,df->bsf", x, W_gate)
        up = torch.einsum("bsd,df->bsf", x, W_up)
        gate = gate * (lambda x, dt: x.to(dt))(gate > 0, gate.dtype)
        h = gate * up
        return torch.einsum("bsf,fd->bsd", h, W_down)

    x = A_x
    x = x + _A_ref(_layernorm(x, axis=-1), A_k_cache, A_v_cache, A_wq, A_wo)
    x = x + _B_ref(_layernorm(x, axis=-1), B_W_gate, B_W_up, B_W_down)
    x = x + _A_ref(_layernorm(x, axis=-1), A_k_cache, A_v_cache, A_wq, A_wo)
    x = x + _B_ref(_layernorm(x, axis=-1), B_W_gate, B_W_up, B_W_down)
    return x
```

```cpp
#include <hip/hip_runtime.h>
#include <hip/hip_bf16.h>
#include <cstdint>

typedef unsigned short u16;
__device__ __forceinline__ float bf2f(u16 v) { return __uint_as_float(((unsigned)v) << 16); }
__device__ __forceinline__ u16 f2bf(float f) { unsigned u = __float_as_uint(f); return (u16)((u + 0x7fffu + ((u >> 16) & 1u)) >> 16); }
__device__ __forceinline__ float rbf(float f) { return bf2f(f2bf(f)); }

constexpr int D = 512, HQ = 32, HKV = 8, SK = 65536, DH = 128, F = 1792;
constexpr int NSPLIT = 64;

__device__ void ln512(const float* x, float* xn, float* red) {
  int t = threadIdx.x;
  float s = 0.f;
  for (int i = t; i < D; i += blockDim.x) s += x[i];
  red[t] = s; __syncthreads();
  for (int o = blockDim.x / 2; o > 0; o >>= 1) { if (t < o) red[t] += red[t + o]; __syncthreads(); }
  float mean = red[0] / D; __syncthreads();
  float v = 0.f;
  for (int i = t; i < D; i += blockDim.x) { float d = x[i] - mean; v += d * d; }
  red[t] = v; __syncthreads();
  for (int o = blockDim.x / 2; o > 0; o >>= 1) { if (t < o) red[t] += red[t + o]; __syncthreads(); }
  float var = red[0] / D; __syncthreads();
  float rstd = rsqrtf(var + 1e-5f);
  for (int i = t; i < D; i += blockDim.x) xn[i] = rbf((x[i] - mean) * rstd);
  __syncthreads();
}

__global__ void k_init(const u16* x, float* xf) { int t = threadIdx.x + blockIdx.x * blockDim.x; if (t < D) xf[t] = bf2f(x[t]); }

__global__ void k_q(const float* x, const u16* wq, float* q) {
  __shared__ float xn[D]; __shared__ float red[256];
  ln512(x, xn, red);
  int col = blockIdx.x * 256 + threadIdx.x;
  float a = 0.f;
  for (int k = 0; k < D; ++k) a += xn[k] * bf2f(wq[(size_t)k * (HQ * DH) + col]);
  q[col] = rbf(a);
}

__global__ void k_attn(const float* q, const u16* K, const u16* V, float* pm, float* pl, float* po) {
  __shared__ float qs[4][DH];
  __shared__ float sc[4][SK / NSPLIT];
  __shared__ float red[4][256];
  int s = blockIdx.x, g = blockIdx.y, t = threadIdx.x;
  constexpr int NK = SK / NSPLIT;
  for (int i = t; i < 4 * DH; i += 256) qs[i / DH][i % DH] = q[g * 4 * DH + i];
  __syncthreads();
  const u16* Kb = K + ((size_t)g * SK + (size_t)s * NK) * DH;
  const u16* Vb = V + ((size_t)g * SK + (size_t)s * NK) * DH;
  float mx[4] = {-INFINITY, -INFINITY, -INFINITY, -INFINITY};
  for (int k = t; k < NK; k += 256) {
    float a[4] = {0, 0, 0, 0};
    for (int d = 0; d < DH; ++d) { float kv = bf2f(Kb[(size_t)k * DH + d]); for (int h = 0; h < 4; ++h) a[h] += qs[h][d] * kv; }
    for (int h = 0; h < 4; ++h) { a[h] *= 0.08838834764831845f; sc[h][k] = a[h]; mx[h] = fmaxf(mx[h], a[h]); }
  }
  for (int h = 0; h < 4; ++h) red[h][t] = mx[h];
  __syncthreads();
  for (int o = 128; o > 0; o >>= 1) { if (t < o) for (int h = 0; h < 4; ++h) red[h][t] = fmaxf(red[h][t], red[h][t + o]); __syncthreads(); }
  float M[4]; for (int h = 0; h < 4; ++h) M[h] = red[h][0];
  __syncthreads();
  float l[4] = {0, 0, 0, 0};
  for (int k = t; k < NK; k += 256) for (int h = 0; h < 4; ++h) { float p = __expf(sc[h][k] - M[h]); sc[h][k] = p; l[h] += p; }
  for (int h = 0; h < 4; ++h) red[h][t] = l[h];
  __syncthreads();
  for (int o = 128; o > 0; o >>= 1) { if (t < o) for (int h = 0; h < 4; ++h) red[h][t] += red[h][t + o]; __syncthreads(); }
  if (t < 4) { pm[(g * 4 + t) * NSPLIT + s] = M[t]; pl[(g * 4 + t) * NSPLIT + s] = red[t][0]; }
  int d = t & 127, hh = t >> 7;
  float o0 = 0.f, o1 = 0.f;
  for (int k = 0; k < NK; ++k) { float v = bf2f(Vb[(size_t)k * DH + d]); o0 += sc[hh * 2][k] * v; o1 += sc[hh * 2 + 1][k] * v; }
  po[((size_t)(g * 4 + hh * 2) * NSPLIT + s) * DH + d] = o0;
  po[((size_t)(g * 4 + hh * 2 + 1) * NSPLIT + s) * DH + d] = o1;
}

__global__ void k_comb(const float* pm, const float* pl, const float* po, float* o) {
  int h = blockIdx.x, d = threadIdx.x;
  float M = -INFINITY;
  for (int s = 0; s < NSPLIT; ++s) M = fmaxf(M, pm[h * NSPLIT + s]);
  float L = 0.f, a = 0.f;
  for (int s = 0; s < NSPLIT; ++s) { float w = __expf(pm[h * NSPLIT + s] - M); L += w * pl[h * NSPLIT + s]; a += w * po[((size_t)h * NSPLIT + s) * DH + d]; }
  o[h * DH + d] = rbf(a / L);
}

__global__ void k_proj_res(const float* in, int Kd, const u16* W, float* x) {
  __shared__ float red[256];
  int col = blockIdx.x, t = threadIdx.x;
  float a = 0.f;
  for (int k = t; k < Kd; k += 256) a += in[k] * bf2f(W[(size_t)k * D + col]);
  red[t] = a; __syncthreads();
  for (int o = 128; o > 0; o >>= 1) { if (t < o) red[t] += red[t + o]; __syncthreads(); }
  if (t == 0) x[col] = rbf(x[col] + rbf(red[0]));
}

__global__ void k_gu(const float* x, const u16* Wg, const u16* Wu, float* h) {
  __shared__ float xn[D]; __shared__ float red[256];
  ln512(x, xn, red);
  int col = blockIdx.x * 256 + threadIdx.x;
  float g = 0.f, u = 0.f;
  for (int k = 0; k < D; ++k) { g += xn[k] * bf2f(Wg[(size_t)k * F + col]); u += xn[k] * bf2f(Wu[(size_t)k * F + col]); }
  g = rbf(g); u = rbf(u);
  g = g > 0.f ? g : 0.f;
  h[col] = rbf(g * u);
}

__global__ void k_out(const float* x, u16* out) { int t = threadIdx.x + blockIdx.x * blockDim.x; if (t < D) out[t] = f2bf(x[t]); }

extern "C" void kernel_launch(void* const* d_in, const int* in_sizes, int n_in, void* d_out, int out_size, void* d_ws, size_t ws_size, hipStream_t stream) {
  const u16* x = (const u16*)d_in[0]; const u16* K = (const u16*)d_in[1]; const u16* V = (const u16*)d_in[2];
  const u16* wq = (const u16*)d_in[3]; const u16* wo = (const u16*)d_in[4];
  const u16* Wg = (const u16*)d_in[6]; const u16* Wu = (const u16*)d_in[7]; const u16* Wd = (const u16*)d_in[8];
  float* ws = (float*)d_ws;
  float* xf = ws; float* q = ws + 1024; float* o = q + 4096; float* h = o + 4096; float* pm = h + 2048; float* pl = pm + HQ * NSPLIT; float* po = pl + HQ * NSPLIT;
  k_init<<<2, 256, 0, stream>>>(x, xf);
  for (int layer = 0; layer < 2; ++layer) {
    k_q<<<16, 256, 0, stream>>>(xf, wq, q);
    k_attn<<<dim3(NSPLIT, HKV), 256, 0, stream>>>(q, K, V, pm, pl, po);
    k_comb<<<HQ, DH, 0, stream>>>(pm, pl, po, o);
    k_proj_res<<<D, 256, 0, stream>>>(o, HQ * DH, wo, xf);
    k_gu<<<F / 256, 256, 0, stream>>>(xf, Wg, Wu, h);
    k_proj_res<<<D, 256, 0, stream>>>(h, F, Wd, xf);
  }
  k_out<<<2, 256, 0, stream>>>(xf, (u16*)d_out);
}
```

```cpp
#include <hip/hip_runtime.h>
#include <hip/hip_bf16.h>
#include <cstdint>

typedef unsigned u32x4 __attribute__((ext_vector_type(4)));
template <int NT> __device__ __forceinline__ u32x4 ldp(const u32x4* p) { if (NT) return __builtin_nontemporal_load(p); else return *p; }
template <int DEPTH, int NT, int T0, int T1, int REV>
__device__ __forceinline__ u32x4 pstream(const char* K, const char* V, int wg, int wave, int lane, u32x4 acc) {
  auto addr = [&](int i) {
    int t = REV ? (T1 - 1 - (i - T0)) : i;
    const char* base = (t < 128 ? K : V) + (size_t)wg * 524288;
    int tt = t & 127;
    return (const u32x4*)(base + (size_t)(tt * 4 + wave) * 1024 + lane * 16);
  };
  u32x4 buf[DEPTH];
#pragma unroll
  for (int j = 0; j < DEPTH; ++j) buf[j] = ldp<NT>(addr(T0 + j));
#pragma unroll 1
  for (int i = T0 + DEPTH; i < T1; i += DEPTH) {
#pragma unroll
    for (int j = 0; j < DEPTH; ++j) { acc ^= buf[j]; buf[j] = ldp<NT>(addr(i + j)); }
  }
#pragma unroll
  for (int j = 0; j < DEPTH; ++j) acc ^= buf[j];
  return acc;
}
template <int DEPTH, int NT, int T0, int T1, int REV>
__global__ void __launch_bounds__(256, 1) k_probe(const char* K, const char* V, unsigned* sink) {
  int wg = blockIdx.x, wave = __builtin_amdgcn_readfirstlane(threadIdx.x >> 6), lane = threadIdx.x & 63;
  u32x4 acc = {0, 0, 0, 0};
  acc = pstream<DEPTH, NT, T0, T1, REV>(K, V, wg, wave, lane, acc);
  if (acc[0] == 0x12345678u && acc[1] == 0x9abcdef0u) sink[0] = acc[2];
}

typedef unsigned short u16;
__device__ __forceinline__ float bf2f(u16 v) { return __uint_as_float(((unsigned)v) << 16); }
__device__ __forceinline__ u16 f2bf(float f) { unsigned u = __float_as_uint(f); return (u16)((u + 0x7fffu + ((u >> 16) & 1u)) >> 16); }
__device__ __forceinline__ float rbf(float f) { return bf2f(f2bf(f)); }

constexpr int D = 512, HQ = 32, HKV = 8, SK = 65536, DH = 128, F = 1792;
constexpr int NSPLIT = 64;

__device__ void ln512(const float* x, float* xn, float* red) {
  int t = threadIdx.x;
  float s = 0.f;
  for (int i = t; i < D; i += blockDim.x) s += x[i];
  red[t] = s; __syncthreads();
  for (int o = blockDim.x / 2; o > 0; o >>= 1) { if (t < o) red[t] += red[t + o]; __syncthreads(); }
  float mean = red[0] / D; __syncthreads();
  float v = 0.f;
  for (int i = t; i < D; i += blockDim.x) { float d = x[i] - mean; v += d * d; }
  red[t] = v; __syncthreads();
  for (int o = blockDim.x / 2; o > 0; o >>= 1) { if (t < o) red[t] += red[t + o]; __syncthreads(); }
  float var = red[0] / D; __syncthreads();
  float rstd = rsqrtf(var + 1e-5f);
  for (int i = t; i < D; i += blockDim.x) xn[i] = rbf((x[i] - mean) * rstd);
  __syncthreads();
}

__global__ void k_init(const u16* x, float* xf) { int t = threadIdx.x + blockIdx.x * blockDim.x; if (t < D) xf[t] = bf2f(x[t]); }

__global__ void k_q(const float* x, const u16* wq, float* q) {
  __shared__ float xn[D]; __shared__ float red[256];
  ln512(x, xn, red);
  int col = blockIdx.x * 256 + threadIdx.x;
  float a = 0.f;
  for (int k = 0; k < D; ++k) a += xn[k] * bf2f(wq[(size_t)k * (HQ * DH) + col]);
  q[col] = rbf(a);
}

__global__ void k_attn(const float* q, const u16* K, const u16* V, float* pm, float* pl, float* po) {
  __shared__ float qs[4][DH];
  __shared__ float sc[4][SK / NSPLIT];
  __shared__ float red[4][256];
  int s = blockIdx.x, g = blockIdx.y, t = threadIdx.x;
  constexpr int NK = SK / NSPLIT;
  for (int i = t; i < 4 * DH; i += 256) qs[i / DH][i % DH] = q[g * 4 * DH + i];
  __syncthreads();
  const u16* Kb = K + ((size_t)g * SK + (size_t)s * NK) * DH;
  const u16* Vb = V + ((size_t)g * SK + (size_t)s * NK) * DH;
  float mx[4] = {-INFINITY, -INFINITY, -INFINITY, -INFINITY};
  for (int k = t; k < NK; k += 256) {
    float a[4] = {0, 0, 0, 0};
    for (int d = 0; d < DH; ++d) { float kv = bf2f(Kb[(size_t)k * DH + d]); for (int h = 0; h < 4; ++h) a[h] += qs[h][d] * kv; }
    for (int h = 0; h < 4; ++h) { a[h] *= 0.08838834764831845f; sc[h][k] = a[h]; mx[h] = fmaxf(mx[h], a[h]); }
  }
  for (int h = 0; h < 4; ++h) red[h][t] = mx[h];
  __syncthreads();
  for (int o = 128; o > 0; o >>= 1) { if (t < o) for (int h = 0; h < 4; ++h) red[h][t] = fmaxf(red[h][t], red[h][t + o]); __syncthreads(); }
  float M[4]; for (int h = 0; h < 4; ++h) M[h] = red[h][0];
  __syncthreads();
  float l[4] = {0, 0, 0, 0};
  for (int k = t; k < NK; k += 256) for (int h = 0; h < 4; ++h) { float p = __expf(sc[h][k] - M[h]); sc[h][k] = p; l[h] += p; }
  for (int h = 0; h < 4; ++h) red[h][t] = l[h];
  __syncthreads();
  for (int o = 128; o > 0; o >>= 1) { if (t < o) for (int h = 0; h < 4; ++h) red[h][t] += red[h][t + o]; __syncthreads(); }
  if (t < 4) { pm[(g * 4 + t) * NSPLIT + s] = M[t]; pl[(g * 4 + t) * NSPLIT + s] = red[t][0]; }
  int d = t & 127, hh = t >> 7;
  float o0 = 0.f, o1 = 0.f;
  for (int k = 0; k < NK; ++k) { float v = bf2f(Vb[(size_t)k * DH + d]); o0 += sc[hh * 2][k] * v; o1 += sc[hh * 2 + 1][k] * v; }
  po[((size_t)(g * 4 + hh * 2) * NSPLIT + s) * DH + d] = o0;
  po[((size_t)(g * 4 + hh * 2 + 1) * NSPLIT + s) * DH + d] = o1;
}

__global__ void k_comb(const float* pm, const float* pl, const float* po, float* o) {
  int h = blockIdx.x, d = threadIdx.x;
  float M = -INFINITY;
  for (int s = 0; s < NSPLIT; ++s) M = fmaxf(M, pm[h * NSPLIT + s]);
  float L = 0.f, a = 0.f;
  for (int s = 0; s < NSPLIT; ++s) { float w = __expf(pm[h * NSPLIT + s] - M); L += w * pl[h * NSPLIT + s]; a += w * po[((size_t)h * NSPLIT + s) * DH + d]; }
  o[h * DH + d] = rbf(a / L);
}

__global__ void k_proj_res(const float* in, int Kd, const u16* W, float* x) {
  __shared__ float red[256];
  int col = blockIdx.x, t = threadIdx.x;
  float a = 0.f;
  for (int k = t; k < Kd; k += 256) a += in[k] * bf2f(W[(size_t)k * D + col]);
  red[t] = a; __syncthreads();
  for (int o = 128; o > 0; o >>= 1) { if (t < o) red[t] += red[t + o]; __syncthreads(); }
  if (t == 0) x[col] = rbf(x[col] + rbf(red[0]));
}

__global__ void k_gu(const float* x, const u16* Wg, const u16* Wu, float* h) {
  __shared__ float xn[D]; __shared__ float red[256];
  ln512(x, xn, red);
  int col = blockIdx.x * 256 + threadIdx.x;
  float g = 0.f, u = 0.f;
  for (int k = 0; k < D; ++k) { g += xn[k] * bf2f(Wg[(size_t)k * F + col]); u += xn[k] * bf2f(Wu[(size_t)k * F + col]); }
  g = rbf(g); u = rbf(u);
  g = g > 0.f ? g : 0.f;
  h[col] = rbf(g * u);
}

__global__ void k_out(const float* x, u16* out) { int t = threadIdx.x + blockIdx.x * blockDim.x; if (t < D) out[t] = f2bf(x[t]); }

extern "C" void kernel_launch(void* const* d_in, const int* in_sizes, int n_in, void* d_out, int out_size, void* d_ws, size_t ws_size, hipStream_t stream) {
  const u16* x = (const u16*)d_in[0]; const u16* K = (const u16*)d_in[1]; const u16* V = (const u16*)d_in[2];
  const u16* wq = (const u16*)d_in[3]; const u16* wo = (const u16*)d_in[4];
  const u16* Wg = (const u16*)d_in[6]; const u16* Wu = (const u16*)d_in[7]; const u16* Wd = (const u16*)d_in[8];
  float* ws = (float*)d_ws;
  float* xf = ws; float* q = ws + 1024; float* o = q + 4096; float* h = o + 4096; float* pm = h + 2048; float* pl = pm + HQ * NSPLIT; float* po = pl + HQ * NSPLIT;

  k_init<<<2, 256, 0, stream>>>(x, xf);
  for (int layer = 0; layer < 2; ++layer) {
    k_q<<<16, 256, 0, stream>>>(xf, wq, q);
    k_attn<<<dim3(NSPLIT, HKV), 256, 0, stream>>>(q, K, V, pm, pl, po);
    k_comb<<<HQ, DH, 0, stream>>>(pm, pl, po, o);
    k_proj_res<<<D, 256, 0, stream>>>(o, HQ * DH, wo, xf);
    k_gu<<<F / 256, 256, 0, stream>>>(xf, Wg, Wu, h);
    k_proj_res<<<D, 256, 0, stream>>>(h, F, Wd, xf);
  }
  k_out<<<2, 256, 0, stream>>>(xf, (u16*)d_out);
}
```

```cpp
#include <hip/hip_runtime.h>
#include <cstdint>

typedef unsigned u32;
typedef unsigned long long u64;
typedef u32 u32x4 __attribute__((ext_vector_type(4)));
typedef u32 u32x2 __attribute__((ext_vector_type(2)));
typedef float f32x4 __attribute__((ext_vector_type(4)));
typedef float f32x2 __attribute__((ext_vector_type(2)));
typedef __bf16 bf16x2 __attribute__((ext_vector_type(2)));
#define LAS __attribute__((address_space(3)))

struct Params { const char* x; const char* K; const char* V; const char* wq; const char* wo; const char* wg; const char* wu; const char* wd; char* out; char* ws; };

constexpr int NS = 15, ND = 17;
constexpr int XS = 64;
constexpr u32 CODE_BYTES = 78000;
constexpr size_t BLK_STRIDE = 16384;
constexpr u32 OFF_R = 4096, OFF_SX = 12288, OFF_G3 = 32768, OFF_G5 = 49152, OFF_G6 = 65536,
              OFF_G1 = 1u << 20, OFF_G2 = 2u << 20, OFF_G4 = OFF_G2 + (1u << 19), OFF_DBG = 4u << 20;
constexpr int RING_BYTES = 8 * 4096, SCR = 4 * RING_BYTES;
constexpr float CS = 0.08838834764831845f * 1.4426950408889634f;
constexpr u32 SPIN_CAP = 1u << 16;
constexpr int DLY1 = 20, DLY2 = 45, DLY3 = 25, DLY4 = 40, DLY5 = 25, DLY6 = 25;
constexpr int MARGIN = 1;

#define STAMP(i) do {} while (0)
#define STAMP_FLUSH() do {} while (0)

#define ld16(d, voff, sb, OFF) asm volatile("global_load_dwordx4 %0, %1, %2 offset:%3 nt" : "=v"(d) : "v"((u32)(voff)), "s"(sb), "i"(OFF) : "memory")
#define ld16_if(d, voff, sb, ph, K) asm volatile("s_cmp_lg_u32 %3, %4\n\ts_cbranch_scc1 .Lsk%=\n\tglobal_load_dwordx4 %0, %1, %2\n.Lsk%=:" : "+v"(d) : "v"((u32)(voff)), "s"(sb), "s"(ph), "i"(K) : "memory", "scc")
#define ld16_sc1(d, voff, sb, OFF) asm volatile("global_load_dwordx4 %0, %1, %2 offset:%3 sc1" : "=v"(d) : "v"((u32)(voff)), "s"(sb), "i"(OFF) : "memory")
#define ld4(d, voff, sb) asm volatile("global_load_dword %0, %1, %2 nt" : "=v"(d) : "v"((u32)(voff)), "s"(sb) : "memory")
__device__ __forceinline__ void st8_sc1(u32 voff, u32x2 d, char* sb) { asm volatile("global_store_dwordx2 %0, %1, %2 sc1" : : "v"(voff), "v"(d), "s"(sb) : "memory"); }
__device__ __forceinline__ void st16_sc1(u32 voff, u32x4 d, char* sb) { asm volatile("global_store_dwordx4 %0, %1, %2 sc1\n\ts_nop 1" : : "v"(voff), "v"(d), "s"(sb) : "memory"); }

template <int CTRL> __device__ __forceinline__ float dppf(float x) { return __builtin_bit_cast(float, __builtin_amdgcn_mov_dpp(__builtin_bit_cast(int, x), CTRL, 0xf, 0xf, true)); }
constexpr int XOR1 = 0xB1, XOR2 = 0x4E, HMIR = 0x141, ROR8 = 0x128;
__device__ __forceinline__ float swap16_sum(float a, float b) {
  auto s = __builtin_amdgcn_permlane16_swap(__float_as_uint(a), __float_as_uint(b), false, false);
  return __uint_as_float(s[0]) + __uint_as_float(s[1]);
}
__device__ __forceinline__ float swap32_sum(float a, float b) {
  auto s = __builtin_amdgcn_permlane32_swap(__float_as_uint(a), __float_as_uint(b), false, false);
  return __uint_as_float(s[0]) + __uint_as_float(s[1]);
}
__device__ __forceinline__ float rows_max(float x) {
  auto s = __builtin_amdgcn_permlane16_swap(__float_as_uint(x), __float_as_uint(x), false, false);
  x = fmaxf(__uint_as_float(s[0]), __uint_as_float(s[1]));
  auto t = __builtin_amdgcn_permlane32_swap(__float_as_uint(x), __float_as_uint(x), false, false);
  return fmaxf(__uint_as_float(t[0]), __uint_as_float(t[1]));
}
__device__ __forceinline__ float row16_sum(float v) { v += dppf<XOR1>(v); v += dppf<XOR2>(v); v += dppf<HMIR>(v); v += dppf<ROR8>(v); return v; }
__device__ __forceinline__ float row16_max(float v) { v = fmaxf(v, dppf<XOR1>(v)); v = fmaxf(v, dppf<XOR2>(v)); v = fmaxf(v, dppf<HMIR>(v)); v = fmaxf(v, dppf<ROR8>(v)); return v; }
__device__ __forceinline__ u32 pk_bf16(float lo, float hi) { bf16x2 r = {(__bf16)lo, (__bf16)hi}; return __builtin_bit_cast(u32, r); }
__device__ __forceinline__ float bf_lo(u32 v) { return __uint_as_float(v << 16); }
__device__ __forceinline__ float bf_hi(u32 v) { return __uint_as_float(v & 0xffff0000u); }
__device__ __forceinline__ float rbf(float v) { return bf_lo(pk_bf16(v, v)); }
__device__ __forceinline__ float dot2(u32 a, u32 b, float c) { return __builtin_amdgcn_fdot2_f32_bf16(__builtin_bit_cast(bf16x2, a), __builtin_bit_cast(bf16x2, b), c, false); }
__device__ __forceinline__ void lds_barrier() { asm volatile("s_waitcnt lgkmcnt(0)\n\ts_barrier" ::: "memory"); }

__device__ __forceinline__ int lane_id() { int l; asm volatile("v_mbcnt_lo_u32_b32 %0, -1, 0\n\tv_mbcnt_hi_u32_b32 %0, -1, %0" : "=v"(l)); return l; }

__device__ __forceinline__ void delay(int ticks) { if (ticks) { u64 s = __builtin_amdgcn_s_memrealtime(); while (__builtin_amdgcn_s_memrealtime() - s < (u64)ticks) {} } }
__device__ __forceinline__ u32 now32() { return (u32)__builtin_amdgcn_s_memrealtime(); }
__device__ __forceinline__ void pace(u32 t0, int due) { if (due > 0) while ((int)(now32() - t0) < due) {} }
__device__ __forceinline__ void qk_one(float (&X)[1], const u32x4 (&kt)[4], u32x4 q, u64 modd, u64 mhi) {
  asm volatile(
    "s_nop 1\n\t"
    "v_mfma_f32_16x16x32_bf16 v[0:3], %1, %5, 0\n\t"
    "v_mfma_f32_16x16x32_bf16 v[4:7], %2, %5, 0\n\t"
    "v_mfma_f32_16x16x32_bf16 v[8:11], %3, %5, 0\n\t"
    "v_mfma_f32_16x16x32_bf16 v[12:15], %4, %5, 0\n\t"
    "s_nop 4\n\t"
    "v_add_f32_dpp v0, v1, v0 quad_perm:[1,1,1,1] row_mask:0xf bank_mask:0xf\n\t"
    "v_add_f32_dpp v5, v6, v5 quad_perm:[2,2,2,2] row_mask:0xf bank_mask:0xf\n\t"
    "v_add_f32_dpp v10, v11, v10 quad_perm:[3,3,3,3] row_mask:0xf bank_mask:0xf\n\t"
    "v_add_f32_dpp v15, v12, v15 quad_perm:[0,0,0,0] row_mask:0xf bank_mask:0xf\n\t"
    "v_add_f32_dpp v0, v2, v0 quad_perm:[2,2,2,2] row_mask:0xf bank_mask:0xf\n\t"
    "v_add_f32_dpp v5, v7, v5 quad_perm:[3,3,3,3] row_mask:0xf bank_mask:0xf\n\t"
    "v_add_f32_dpp v10, v8, v10 quad_perm:[0,0,0,0] row_mask:0xf bank_mask:0xf\n\t"
    "v_add_f32_dpp v15, v13, v15 quad_perm:[1,1,1,1] row_mask:0xf bank_mask:0xf\n\t"
    "v_add_f32_dpp v0, v3, v0 quad_perm:[3,3,3,3] row_mask:0xf bank_mask:0xf\n\t"
    "v_add_f32_dpp v5, v4, v5 quad_perm:[0,0,0,0] row_mask:0xf bank_mask:0xf\n\t"
    "v_add_f32_dpp v10, v9, v10 quad_perm:[1,1,1,1] row_mask:0xf bank_mask:0xf\n\t"
    "v_add_f32_dpp v15, v14, v15 quad_perm:[2,2,2,2] row_mask:0xf bank_mask:0xf\n\t"
    "v_cndmask_b32_e64 v0, v0, v5, %6\n\t"
    "v_cndmask_b32_e64 v10, v10, v15, %6\n\t"
    "v_cndmask_b32_e64 %0, v0, v10, %7\n\t"
    : "=&v"(X[0])
    : "v"(kt[0]), "v"(kt[1]), "v"(kt[2]), "v"(kt[3]), "v"(q), "s"(modd), "s"(mhi)
    : "v0", "v1", "v2", "v3", "v4", "v5", "v6", "v7", "v8", "v9", "v10", "v11", "v12", "v13", "v14", "v15");
}
__device__ __forceinline__ void pv_one(f32x4& acc0, f32x4& acc1, const u32 (&pp)[1], const u32x4 (&vt)[4], u32 mask0, u32 mask1) {
  asm volatile(
    "v_mov_b32 v0, 0\n\t"
    "v_mov_b32 v1, 0\n\t"
    "v_mov_b32 v4, 0\n\t"
    "v_mov_b32 v5, 0\n\t"
    "v_mov_b32 v8, 0\n\t"
    "v_mov_b32 v9, 0\n\t"
    "v_mov_b32 v12, 0\n\t"
    "v_mov_b32 v13, 0\n\t"
    "v_mov_b32 v16, 0\n\t"
    "v_mov_b32 v17, 0\n\t"
    "v_and_b32_dpp v2, %2, %7 quad_perm:[0,0,0,0] row_mask:0xf bank_mask:0xf\n\t"
    "v_and_b32_dpp v3, %2, %8 quad_perm:[0,0,0,0] row_mask:0xf bank_mask:0xf\n\t"
    "v_and_b32_dpp v6, %2, %7 quad_perm:[1,1,1,1] row_mask:0xf bank_mask:0xf\n\t"
    "s_nop 0\n\t"
    "v_mfma_f32_16x16x32_bf16 %0, v[2:5], %3, %0\n\t"
    "v_and_b32_dpp v7, %2, %8 quad_perm:[1,1,1,1] row_mask:0xf bank_mask:0xf\n\t"
    "v_mfma_f32_16x16x32_bf16 %1, v[0:3], %3, %1\n\t"
    "v_and_b32_dpp v10, %2, %7 quad_perm:[2,2,2,2] row_mask:0xf bank_mask:0xf\n\t"
    "v_mfma_f32_16x16x32_bf16 %0, v[6:9], %4, %0\n\t"
    "v_and_b32_dpp v11, %2, %8 quad_perm:[2,2,2,2] row_mask:0xf bank_mask:0xf\n\t"
    "v_mfma_f32_16x16x32_bf16 %1, v[4:7], %4, %1\n\t"
    "v_and_b32_dpp v14, %2, %7 quad_perm:[3,3,3,3] row_mask:0xf bank_mask:0xf\n\t"
    "v_mfma_f32_16x16x32_bf16 %0, v[10:13], %5, %0\n\t"
    "v_and_b32_dpp v15, %2, %8 quad_perm:[3,3,3,3] row_mask:0xf bank_mask:0xf\n\t"
    "v_mfma_f32_16x16x32_bf16 %1, v[8:11], %5, %1\n\t"
    "s_nop 0\n\t"
    "v_mfma_f32_16x16x32_bf16 %0, v[14:17], %6, %0\n\t"
    "v_mfma_f32_16x16x32_bf16 %1, v[12:15], %6, %1\n\t"
    "s_nop 7\n\t"
    : "+v"(acc0), "+v"(acc1)
    : "v"(pp[0]), "v"(vt[0]), "v"(vt[1]), "v"(vt[2]), "v"(vt[3]), "v"(mask0), "v"(mask1)
    : "v0", "v1", "v2", "v3", "v4", "v5", "v6", "v7", "v8", "v9", "v10", "v11", "v12", "v13", "v14", "v15", "v16", "v17");
}
template <int WAIT, int SLOT> __device__ __forceinline__ void uk(float& X, u32x4 q, u64 modd, u64 mhi, u32 laddr, u32 koff, const char* kb, u32 m0v, u32 boff, u32 mode) {
  asm volatile(
    "s_waitcnt vmcnt(%5)\n\t"
    "ds_read_b128 v[16:19], %4 offset:%6+0\n\t"
    "ds_read_b128 v[20:23], %4 offset:%6+1024\n\t"
    "ds_read_b128 v[24:27], %4 offset:%6+2048\n\t"
    "ds_read_b128 v[28:31], %4 offset:%6+3072\n\t"
    "s_cmp_eq_u32 %11, 0\n\t"
    "s_cbranch_scc1 .Le%=\n\t"
    "v_xor_b32 v15, %10, %7\n\t"
    "s_mov_b32 m0, %9\n\t"
    "s_cmp_eq_u32 %11, 1\n\t"
    "s_cbranch_scc1 .Ln%=\n\t"
    "s_cmp_eq_u32 %11, 2\n\t"
    "s_cbranch_scc0 .Lw%=\n\t"
    "global_load_lds_dwordx4 v15, %8\n\t"
    "global_load_lds_dwordx4 v15, %8 offset:1024\n\t"
    "global_load_lds_dwordx4 v15, %8 offset:2048\n\t"
    "global_load_lds_dwordx4 v15, %8 offset:3072\n\t"
    "s_branch .Le%=\n\t"
    ".Lw%=:\n\t"
    "s_waitcnt lgkmcnt(0)\n\t"
    ".Ln%=:\n\t"
    "global_load_lds_dwordx4 v15, %8 nt\n\t"
    "global_load_lds_dwordx4 v15, %8 offset:1024 nt\n\t"
    "global_load_lds_dwordx4 v15, %8 offset:2048 nt\n\t"
    "global_load_lds_dwordx4 v15, %8 offset:3072 nt\n\t"
    ".Le%=:\n\t"
    "s_waitcnt lgkmcnt(3)\n\t"
    "v_mfma_f32_16x16x32_bf16 v[0:3], v[16:19], %1, 0\n\t"
    "s_waitcnt lgkmcnt(2)\n\t"
    "v_mfma_f32_16x16x32_bf16 v[4:7], v[20:23], %1, 0\n\t"
    "s_waitcnt lgkmcnt(1)\n\t"
    "v_mfma_f32_16x16x32_bf16 v[8:11], v[24:27], %1, 0\n\t"
    "s_waitcnt lgkmcnt(0)\n\t"
    "v_mfma_f32_16x16x32_bf16 v[12:15], v[28:31], %1, 0\n\t"
    "s_nop 1\n\t"
    "v_add_f32_dpp v0, v1, v0 quad_perm:[1,1,1,1] row_mask:0xf bank_mask:0xf\n\t"
    "s_nop 0\n\t"
    "v_add_f32_dpp v5, v6, v5 quad_perm:[2,2,2,2] row_mask:0xf bank_mask:0xf\n\t"
    "s_nop 0\n\t"
    "v_add_f32_dpp v10, v11, v10 quad_perm:[3,3,3,3] row_mask:0xf bank_mask:0xf\n\t"
    "s_nop 0\n\t"
    "v_add_f32_dpp v15, v12, v15 quad_perm:[0,0,0,0] row_mask:0xf bank_mask:0xf\n\t"
    "v_add_f32_dpp v0, v2, v0 quad_perm:[2,2,2,2] row_mask:0xf bank_mask:0xf\n\t"
    "v_add_f32_dpp v5, v7, v5 quad_perm:[3,3,3,3] row_mask:0xf bank_mask:0xf\n\t"
    "v_add_f32_dpp v10, v8, v10 quad_perm:[0,0,0,0] row_mask:0xf bank_mask:0xf\n\t"
    "v_add_f32_dpp v15, v13, v15 quad_perm:[1,1,1,1] row_mask:0xf bank_mask:0xf\n\t"
    "v_add_f32_dpp v0, v3, v0 quad_perm:[3,3,3,3] row_mask:0xf bank_mask:0xf\n\t"
    "v_add_f32_dpp v5, v4, v5 quad_perm:[0,0,0,0] row_mask:0xf bank_mask:0xf\n\t"
    "v_add_f32_dpp v10, v9, v10 quad_perm:[1,1,1,1] row_mask:0xf bank_mask:0xf\n\t"
    "v_add_f32_dpp v15, v14, v15 quad_perm:[2,2,2,2] row_mask:0xf bank_mask:0xf\n\t"
    "v_cndmask_b32_e64 v0, v0, v5, %2\n\t"
    "v_cndmask_b32_e64 v10, v10, v15, %2\n\t"
    "v_cndmask_b32_e64 %0, v0, v10, %3\n\t"
    : "=&v"(X)
    : "v"(q), "s"(modd), "s"(mhi), "v"(laddr), "i"(WAIT), "i"(SLOT * 4096), "v"(koff), "s"(kb), "s"(m0v), "s"(boff), "s"(mode)
    : "memory", "scc", "v0", "v1", "v2", "v3", "v4", "v5", "v6", "v7", "v8", "v9", "v10", "v11", "v12", "v13", "v14", "v15", "v16", "v17", "v18", "v19", "v20", "v21", "v22", "v23", "v24", "v25", "v26", "v27", "v28", "v29", "v30", "v31");
}
template <int WAIT, int SLOT> __device__ __forceinline__ void uv(f32x4& acc0, f32x4& acc1, u32 pp, u32 mask0, u32 mask1, u32 laddr, u32 laddr8, u32 voff, const char* vb, u32 m0v, u32 boff, u32 mode) {
  asm volatile(
    "s_waitcnt vmcnt(%7)\n\t"
    "ds_read2st64_b64 v[16:19], %5 offset0:%8 offset1:%8+2\n\t"
    "ds_read2st64_b64 v[20:23], %6 offset0:%8 offset1:%8+2\n\t"
    "ds_read2st64_b64 v[24:27], %5 offset0:%8+4 offset1:%8+6\n\t"
    "ds_read2st64_b64 v[28:31], %6 offset0:%8+4 offset1:%8+6\n\t"
    "s_cmp_eq_u32 %13, 0\n\t"
    "s_cbranch_scc1 .Le%=\n\t"
    "v_xor_b32 v15, %12, %9\n\t"
    "s_mov_b32 m0, %11\n\t"
    "s_cmp_eq_u32 %13, 1\n\t"
    "s_cbranch_scc1 .Ln%=\n\t"
    "s_cmp_eq_u32 %13, 2\n\t"
    "s_cbranch_scc0 .Lw%=\n\t"
    "global_load_lds_dwordx4 v15, %10\n\t"
    "global_load_lds_dwordx4 v15, %10 offset:1024\n\t"
    "global_load_lds_dwordx4 v15, %10 offset:2048\n\t"
    "global_load_lds_dwordx4 v15, %10 offset:3072\n\t"
    "s_branch .Le%=\n\t"
    ".Lw%=:\n\t"
    "s_waitcnt lgkmcnt(0)\n\t"
    ".Ln%=:\n\t"
    "global_load_lds_dwordx4 v15, %10 nt\n\t"
    "global_load_lds_dwordx4 v15, %10 offset:1024 nt\n\t"
    "global_load_lds_dwordx4 v15, %10 offset:2048 nt\n\t"
    "global_load_lds_dwordx4 v15, %10 offset:3072 nt\n\t"
    ".Le%=:\n\t"
    "v_and_b32_dpp v0, %2, %3 quad_perm:[0,0,0,0] row_mask:0xf bank_mask:0xf\n\t"
    "v_and_b32_dpp v1, %2, %4 quad_perm:[0,0,0,0] row_mask:0xf bank_mask:0xf\n\t"
    "v_and_b32_dpp v2, %2, %3 quad_perm:[1,1,1,1] row_mask:0xf bank_mask:0xf\n\t"
    "v_and_b32_dpp v3, %2, %4 quad_perm:[1,1,1,1] row_mask:0xf bank_mask:0xf\n\t"
    "v_and_b32_dpp v4, %2, %3 quad_perm:[2,2,2,2] row_mask:0xf bank_mask:0xf\n\t"
    "v_and_b32_dpp v5, %2, %4 quad_perm:[2,2,2,2] row_mask:0xf bank_mask:0xf\n\t"
    "v_and_b32_dpp v6, %2, %3 quad_perm:[3,3,3,3] row_mask:0xf bank_mask:0xf\n\t"
    "v_and_b32_dpp v7, %2, %4 quad_perm:[3,3,3,3] row_mask:0xf bank_mask:0xf\n\t"
    "s_waitcnt lgkmcnt(3)\n\t"
    "v_mfma_f32_16x16x32_bf16 %0, v[0:3], v[16:19], %0\n\t"
    "s_waitcnt lgkmcnt(2)\n\t"
    "v_mfma_f32_16x16x32_bf16 %1, v[0:3], v[20:23], %1\n\t"
    "s_waitcnt lgkmcnt(1)\n\t"
    "v_mfma_f32_16x16x32_bf16 %0, v[4:7], v[24:27], %0\n\t"
    "s_waitcnt lgkmcnt(0)\n\t"
    "v_mfma_f32_16x16x32_bf16 %1, v[4:7], v[28:31], %1\n\t"
    : "+v"(acc0), "+v"(acc1)
    : "v"(pp), "v"(mask0), "v"(mask1), "v"(laddr), "v"(laddr8), "i"(WAIT), "i"(SLOT * 8), "v"(voff), "s"(vb), "s"(m0v), "s"(boff), "s"(mode)
    : "memory", "scc", "v0", "v1", "v2", "v3", "v4", "v5", "v6", "v7", "v8", "v9", "v10", "v11", "v12", "v13", "v14", "v15", "v16", "v17", "v18", "v19", "v20", "v21", "v22", "v23", "v24", "v25", "v26", "v27", "v28", "v29", "v30", "v31");
}
__device__ __forceinline__ void sk0(float& X, const u32 (&pk)[30][13], u32x4 q, u64 modd, u64 mhi) {
  asm volatile(
    "v_accvgpr_read_b32 v38, %13\n\t"
    "v_accvgpr_read_b32 v26, %3\n\t"
    "v_accvgpr_read_b32 v33, %6\n\t"
    "v_accvgpr_read_b32 v24, %1\n\t"
    "v_accvgpr_read_b32 v31, %4\n\t"
    "v_accvgpr_read_b32 v25, %2\n\t"
    "v_accvgpr_read_b32 v32, %5\n\t"
    "v_lshlrev_b32 v29, 7, v38\n\t"
    "v_lshlrev_b32 v36, 5, v38\n\t"
    "v_and_b32 v27, %17, v26\n\t"
    "v_and_b32 v34, %17, v33\n\t"
    "v_lshrrev_b32 v28, 4, v26\n\t"
    "v_lshrrev_b32 v35, 4, v33\n\t"
    "v_lshlrev_b32 v30, 6, v38\n\t"
    "v_lshlrev_b32 v37, 4, v38\n\t"
    "v_and_or_b32 v27, v29, %18, v27\n\t"
    "v_and_or_b32 v34, v36, %18, v34\n\t"
    "v_and_b32 v28, %17, v28\n\t"
    "v_and_b32 v35, %17, v35\n\t"
    "v_perm_b32 v16, v27, v24, %19\n\t"
    "v_perm_b32 v20, v34, v31, %19\n\t"
    "v_and_or_b32 v28, v30, %18, v28\n\t"
    "v_and_or_b32 v35, v37, %18, v35\n\t"
    "v_perm_b32 v17, v27, v24, %20\n\t"
    "v_perm_b32 v21, v34, v31, %20\n\t"
    "v_perm_b32 v18, v28, v25, %19\n\t"
    "v_perm_b32 v22, v35, v32, %19\n\t"
    "v_perm_b32 v19, v28, v25, %20\n\t"
    "v_perm_b32 v23, v35, v32, %20\n\t"
    "v_accvgpr_read_b32 v26, %9\n\t"
    "v_mfma_f32_16x16x32_bf16 v[0:3], v[16:19], %14, 0\n\t"
    "v_accvgpr_read_b32 v33, %12\n\t"
    "v_accvgpr_read_b32 v24, %7\n\t"
    "v_mfma_f32_16x16x32_bf16 v[4:7], v[20:23], %14, 0\n\t"
    "v_accvgpr_read_b32 v31, %10\n\t"
    "v_accvgpr_read_b32 v25, %8\n\t"
    "v_accvgpr_read_b32 v32, %11\n\t"
    "v_lshlrev_b32 v29, 3, v38\n\t"
    "v_lshlrev_b32 v36, 1, v38\n\t"
    "v_and_b32 v27, %17, v26\n\t"
    "v_and_b32 v34, %17, v33\n\t"
    "v_lshrrev_b32 v28, 4, v26\n\t"
    "v_lshrrev_b32 v35, 4, v33\n\t"
    "v_lshlrev_b32 v30, 2, v38\n\t"
    "v_and_or_b32 v34, v36, %18, v34\n\t"
    "v_and_or_b32 v27, v29, %18, v27\n\t"
    "v_and_b32 v35, %17, v35\n\t"
    "v_and_b32 v28, %17, v28\n\t"
    "v_perm_b32 v20, v34, v31, %19\n\t"
    "v_perm_b32 v16, v27, v24, %19\n\t"
    "v_and_or_b32 v35, v38, %18, v35\n\t"
    "v_and_or_b32 v28, v30, %18, v28\n\t"
    "v_perm_b32 v21, v34, v31, %20\n\t"
    "v_perm_b32 v17, v27, v24, %20\n\t"
    "v_perm_b32 v22, v35, v32, %19\n\t"
    "v_perm_b32 v18, v28, v25, %19\n\t"
    "v_perm_b32 v23, v35, v32, %20\n\t"
    "v_perm_b32 v19, v28, v25, %20\n\t"
    "v_add_f32_dpp v0, v1, v0 quad_perm:[1,1,1,1] row_mask:0xf bank_mask:0xf\n\t"
    "v_add_f32_dpp v5, v6, v5 quad_perm:[2,2,2,2] row_mask:0xf bank_mask:0xf\n\t"
    "v_mfma_f32_16x16x32_bf16 v[8:11], v[16:19], %14, 0\n\t"
    "v_add_f32_dpp v0, v2, v0 quad_perm:[2,2,2,2] row_mask:0xf bank_mask:0xf\n\t"
    "v_mfma_f32_16x16x32_bf16 v[12:15], v[20:23], %14, 0\n\t"
    "v_add_f32_dpp v5, v7, v5 quad_perm:[3,3,3,3] row_mask:0xf bank_mask:0xf\n\t"
    "v_add_f32_dpp v0, v3, v0 quad_perm:[3,3,3,3] row_mask:0xf bank_mask:0xf\n\t"
    "v_add_f32_dpp v5, v4, v5 quad_perm:[0,0,0,0] row_mask:0xf bank_mask:0xf\n\t"
    "v_cndmask_b32_e64 v0, v0, v5, %15\n\t"
    "s_nop 1\n\t"
    "v_add_f32_dpp v10, v11, v10 quad_perm:[3,3,3,3] row_mask:0xf bank_mask:0xf\n\t"
    "s_nop 0\n\t"
    "v_add_f32_dpp v15, v12, v15 quad_perm:[0,0,0,0] row_mask:0xf bank_mask:0xf\n\t"
    "v_add_f32_dpp v10, v8, v10 quad_perm:[0,0,0,0] row_mask:0xf bank_mask:0xf\n\t"
    "v_add_f32_dpp v15, v13, v15 quad_perm:[1,1,1,1] row_mask:0xf bank_mask:0xf\n\t"
    "v_add_f32_dpp v10, v9, v10 quad_perm:[1,1,1,1] row_mask:0xf bank_mask:0xf\n\t"
    "v_add_f32_dpp v15, v14, v15 quad_perm:[2,2,2,2] row_mask:0xf bank_mask:0xf\n\t"
    "v_cndmask_b32_e64 v10, v10, v15, %15\n\t"
    "v_cndmask_b32_e64 %0, v0, v10, %16\n\t"
    : "=&v"(X)
    : "a"(pk[0][0]), "a"(pk[0][1]), "a"(pk[0][2]), "a"(pk[0][3]), "a"(pk[0][4]), "a"(pk[0][5]), "a"(pk[0][6]), "a"(pk[0][7]), "a"(pk[0][8]), "a"(pk[0][9]), "a"(pk[0][10]), "a"(pk[0][11]), "a"(pk[0][12]), "v"(q), "s"(modd), "s"(mhi), "s"(0x0f0f0f0fu), "s"(0x80808080u), "s"(0x05010400u), "s"(0x07030602u)
    : "v0", "v1", "v2", "v3", "v4", "v5", "v6", "v7", "v8", "v9", "v10", "v11", "v12", "v13", "v14", "v15", "v16", "v17", "v18", "v19", "v20", "v21", "v22", "v23", "v24", "v25", "v26", "v27", "v28", "v29", "v30", "v31", "v32", "v33", "v34", "v35", "v36", "v37", "v38", "v39");
}
__device__ __forceinline__ void sv0(f32x4& acc0, f32x4& acc1, const u32 (&pk)[30][13], u32 pp, u32 mask0, u32 mask1) {
  asm volatile(
    "v_accvgpr_read_b32 v38, %14\n\t"
    "v_accvgpr_read_b32 v26, %4\n\t"
    "v_accvgpr_read_b32 v33, %7\n\t"
    "v_accvgpr_read_b32 v24, %2\n\t"
    "v_accvgpr_read_b32 v31, %5\n\t"
    "v_accvgpr_read_b32 v25, %3\n\t"
    "v_accvgpr_read_b32 v32, %6\n\t"
    "v_lshlrev_b32 v29, 7, v38\n\t"
    "v_lshlrev_b32 v36, 5, v38\n\t"
    "v_and_b32 v27, %18, v26\n\t"
    "v_and_b32 v34, %18, v33\n\t"
    "v_lshrrev_b32 v28, 4, v26\n\t"
    "v_lshrrev_b32 v35, 4, v33\n\t"
    "v_lshlrev_b32 v30, 6, v38\n\t"
    "v_lshlrev_b32 v37, 4, v38\n\t"
    "v_and_or_b32 v27, v29, %19, v27\n\t"
    "v_and_or_b32 v34, v36, %19, v34\n\t"
    "v_and_b32 v28, %18, v28\n\t"
    "v_and_b32 v35, %18, v35\n\t"
    "v_perm_b32 v16, v27, v24, %20\n\t"
    "v_perm_b32 v18, v34, v31, %20\n\t"
    "v_and_or_b32 v28, v30, %19, v28\n\t"
    "v_and_or_b32 v35, v37, %19, v35\n\t"
    "v_perm_b32 v17, v27, v24, %21\n\t"
    "v_perm_b32 v19, v34, v31, %21\n\t"
    "v_perm_b32 v20, v28, v25, %20\n\t"
    "v_perm_b32 v22, v35, v32, %20\n\t"
    "v_perm_b32 v21, v28, v25, %21\n\t"
    "v_perm_b32 v23, v35, v32, %21\n\t"
    "v_and_b32_dpp v0, %15, %16 quad_perm:[0,0,0,0] row_mask:0xf bank_mask:0xf\n\t"
    "v_and_b32_dpp v1, %15, %17 quad_perm:[0,0,0,0] row_mask:0xf bank_mask:0xf\n\t"
    "v_and_b32_dpp v2, %15, %16 quad_perm:[1,1,1,1] row_mask:0xf bank_mask:0xf\n\t"
    "v_and_b32_dpp v3, %15, %17 quad_perm:[1,1,1,1] row_mask:0xf bank_mask:0xf\n\t"
    "v_accvgpr_read_b32 v26, %10\n\t"
    "v_accvgpr_read_b32 v33, %13\n\t"
    "v_mfma_f32_16x16x32_bf16 %0, v[0:3], v[16:19], %0\n\t"
    "v_accvgpr_read_b32 v24, %8\n\t"
    "v_accvgpr_read_b32 v31, %11\n\t"
    "v_accvgpr_read_b32 v25, %9\n\t"
    "v_mfma_f32_16x16x32_bf16 %1, v[0:3], v[20:23], %1\n\t"
    "v_accvgpr_read_b32 v32, %12\n\t"
    "v_lshlrev_b32 v29, 3, v38\n\t"
    "v_lshlrev_b32 v36, 1, v38\n\t"
    "v_and_b32 v27, %18, v26\n\t"
    "v_and_b32 v34, %18, v33\n\t"
    "v_lshrrev_b32 v28, 4, v26\n\t"
    "v_lshrrev_b32 v35, 4, v33\n\t"
    "v_lshlrev_b32 v30, 2, v38\n\t"
    "v_and_or_b32 v34, v36, %19, v34\n\t"
    "v_and_or_b32 v27, v29, %19, v27\n\t"
    "v_and_b32 v35, %18, v35\n\t"
    "v_and_b32 v28, %18, v28\n\t"
    "v_perm_b32 v18, v34, v31, %20\n\t"
    "v_perm_b32 v16, v27, v24, %20\n\t"
    "v_and_or_b32 v35, v38, %19, v35\n\t"
    "v_and_or_b32 v28, v30, %19, v28\n\t"
    "v_perm_b32 v19, v34, v31, %21\n\t"
    "v_perm_b32 v17, v27, v24, %21\n\t"
    "v_perm_b32 v22, v35, v32, %20\n\t"
    "v_perm_b32 v20, v28, v25, %20\n\t"
    "v_perm_b32 v23, v35, v32, %21\n\t"
    "v_perm_b32 v21, v28, v25, %21\n\t"
    "v_and_b32_dpp v4, %15, %16 quad_perm:[2,2,2,2] row_mask:0xf bank_mask:0xf\n\t"
    "v_and_b32_dpp v5, %15, %17 quad_perm:[2,2,2,2] row_mask:0xf bank_mask:0xf\n\t"
    "v_and_b32_dpp v6, %15, %16 quad_perm:[3,3,3,3] row_mask:0xf bank_mask:0xf\n\t"
    "v_and_b32_dpp v7, %15, %17 quad_perm:[3,3,3,3] row_mask:0xf bank_mask:0xf\n\t"
    "s_nop 1\n\t"
    "v_mfma_f32_16x16x32_bf16 %0, v[4:7], v[16:19], %0\n\t"
    "v_mfma_f32_16x16x32_bf16 %1, v[4:7], v[20:23], %1\n\t"
    : "+v"(acc0), "+v"(acc1)
    : "a"(pk[1][0]), "a"(pk[1][1]), "a"(pk[1][2]), "a"(pk[1][3]), "a"(pk[1][4]), "a"(pk[1][5]), "a"(pk[1][6]), "a"(pk[1][7]), "a"(pk[1][8]), "a"(pk[1][9]), "a"(pk[1][10]), "a"(pk[1][11]), "a"(pk[1][12]), "v"(pp), "v"(mask0), "v"(mask1), "s"(0x0f0f0f0fu), "s"(0x80808080u), "s"(0x05010400u), "s"(0x07030602u)
    : "v0", "v1", "v2", "v3", "v4", "v5", "v6", "v7", "v8", "v9", "v10", "v11", "v12", "v13", "v14", "v15", "v16", "v17", "v18", "v19", "v20", "v21", "v22", "v23", "v24", "v25", "v26", "v27", "v28", "v29", "v30", "v31", "v32", "v33", "v34", "v35", "v36", "v37", "v38", "v39");
}
__device__ __forceinline__ void sk1(float& X, const u32 (&pk)[30][13], u32x4 q, u64 modd, u64 mhi) {
  asm volatile(
    "v_accvgpr_read_b32 v38, %13\n\t"
    "v_accvgpr_read_b32 v26, %3\n\t"
    "v_accvgpr_read_b32 v33, %6\n\t"
    "v_accvgpr_read_b32 v24, %1\n\t"
    "v_accvgpr_read_b32 v31, %4\n\t"
    "v_accvgpr_read_b32 v25, %2\n\t"
    "v_accvgpr_read_b32 v32, %5\n\t"
    "v_lshlrev_b32 v29, 7, v38\n\t"
    "v_lshlrev_b32 v36, 5, v38\n\t"
    "v_and_b32 v27, %17, v26\n\t"
    "v_and_b32 v34, %17, v33\n\t"
    "v_lshrrev_b32 v28, 4, v26\n\t"
    "v_lshrrev_b32 v35, 4, v33\n\t"
    "v_lshlrev_b32 v30, 6, v38\n\t"
    "v_lshlrev_b32 v37, 4, v38\n\t"
    "v_and_or_b32 v27, v29, %18, v27\n\t"
    "v_and_or_b32 v34, v36, %18, v34\n\t"
    "v_and_b32 v28, %17, v28\n\t"
    "v_and_b32 v35, %17, v35\n\t"
    "v_perm_b32 v16, v27, v24, %19\n\t"
    "v_perm_b32 v20, v34, v31, %19\n\t"
    "v_and_or_b32 v28, v30, %18, v28\n\t"
    "v_and_or_b32 v35, v37, %18, v35\n\t"
    "v_perm_b32 v17, v27, v24, %20\n\t"
    "v_perm_b32 v21, v34, v31, %20\n\t"
    "v_perm_b32 v18, v28, v25, %19\n\t"
    "v_perm_b32 v22, v35, v32, %19\n\t"
    "v_perm_b32 v19, v28, v25, %20\n\t"
    "v_perm_b32 v23, v35, v32, %20\n\t"
    "v_accvgpr_read_b32 v26, %9\n\t"
    "v_mfma_f32_16x16x32_bf16 v[0:3], v[16:19], %14, 0\n\t"
    "v_accvgpr_read_b32 v33, %12\n\t"
    "v_accvgpr_read_b32 v24, %7\n\t"
    "v_mfma_f32_16x16x32_bf16 v[4:7], v[20:23], %14, 0\n\t"
    "v_accvgpr_read_b32 v31, %10\n\t"
    "v_accvgpr_read_b32 v25, %8\n\t"
    "v_accvgpr_read_b32 v32, %11\n\t"
    "v_lshlrev_b32 v29, 3, v38\n\t"
    "v_lshlrev_b32 v36, 1, v38\n\t"
    "v_and_b32 v27, %17, v26\n\t"
    "v_and_b32 v34, %17, v33\n\t"
    "v_lshrrev_b32 v28, 4, v26\n\t"
    "v_lshrrev_b32 v35, 4, v33\n\t"
    "v_lshlrev_b32 v30, 2, v38\n\t"
    "v_and_or_b32 v34, v36, %18, v34\n\t"
    "v_and_or_b32 v27, v29, %18, v27\n\t"
    "v_and_b32 v35, %17, v35\n\t"
    "v_and_b32 v28, %17, v28\n\t"
    "v_perm_b32 v20, v34, v31, %19\n\t"
    "v_perm_b32 v16, v27, v24, %19\n\t"
    "v_and_or_b32 v35, v38, %18, v35\n\t"
    "v_and_or_b32 v28, v30, %18, v28\n\t"
    "v_perm_b32 v21, v34, v31, %20\n\t"
    "v_perm_b32 v17, v27, v24, %20\n\t"
    "v_perm_b32 v22, v35, v32, %19\n\t"
    "v_perm_b32 v18, v28, v25, %19\n\t"
    "v_perm_b32 v23, v35, v32, %20\n\t"
    "v_perm_b32 v19, v28, v25, %20\n\t"
    "v_add_f32_dpp v0, v1, v0 quad_perm:[1,1,1,1] row_mask:0xf bank_mask:0xf\n\t"
    "v_add_f32_dpp v5, v6, v5 quad_perm:[2,2,2,2] row_mask:0xf bank_mask:0xf\n\t"
    "v_mfma_f32_16x16x32_bf16 v[8:11], v[16:19], %14, 0\n\t"
    "v_add_f32_dpp v0, v2, v0 quad_perm:[2,2,2,2] row_mask:0xf bank_mask:0xf\n\t"
    "v_mfma_f32_16x16x32_bf16 v[12:15], v[20:23], %14, 0\n\t"
    "v_add_f32_dpp v5, v7, v5 quad_perm:[3,3,3,3] row_mask:0xf bank_mask:0xf\n\t"
    "v_add_f32_dpp v0, v3, v0 quad_perm:[3,3,3,3] row_mask:0xf bank_mask:0xf\n\t"
    "v_add_f32_dpp v5, v4, v5 quad_perm:[0,0,0,0] row_mask:0xf bank_mask:0xf\n\t"
    "v_cndmask_b32_e64 v0, v0, v5, %15\n\t"
    "s_nop 1\n\t"
    "v_add_f32_dpp v10, v11, v10 quad_perm:[3,3,3,3] row_mask:0xf bank_mask:0xf\n\t"
    "s_nop 0\n\t"
    "v_add_f32_dpp v15, v12, v15 quad_perm:[0,0,0,0] row_mask:0xf bank_mask:0xf\n\t"
    "v_add_f32_dpp v10, v8, v10 quad_perm:[0,0,0,0] row_mask:0xf bank_mask:0xf\n\t"
    "v_add_f32_dpp v15, v13, v15 quad_perm:[1,1,1,1] row_mask:0xf bank_mask:0xf\n\t"
    "v_add_f32_dpp v10, v9, v10 quad_perm:[1,1,1,1] row_mask:0xf bank_mask:0xf\n\t"
    "v_add_f32_dpp v15, v14, v15 quad_perm:[2,2,2,2] row_mask:0xf bank_mask:0xf\n\t"
    "v_cndmask_b32_e64 v10, v10, v15, %15\n\t"
    "v_cndmask_b32_e64 %0, v0, v10, %16\n\t"
    : "=&v"(X)
    : "a"(pk[2][0]), "a"(pk[2][1]), "a"(pk[2][2]), "a"(pk[2][3]), "a"(pk[2][4]), "a"(pk[2][5]), "a"(pk[2][6]), "a"(pk[2][7]), "a"(pk[2][8]), "a"(pk[2][9]), "a"(pk[2][10]), "a"(pk[2][11]), "a"(pk[2][12]), "v"(q), "s"(modd), "s"(mhi), "s"(0x0f0f0f0fu), "s"(0x80808080u), "s"(0x05010400u), "s"(0x07030602u)
    : "v0", "v1", "v2", "v3", "v4", "v5", "v6", "v7", "v8", "v9", "v10", "v11", "v12", "v13", "v14", "v15", "v16", "v17", "v18", "v19", "v20", "v21", "v22", "v23", "v24", "v25", "v26", "v27", "v28", "v29", "v30", "v31", "v32", "v33", "v34", "v35", "v36", "v37", "v38", "v39");
}
__device__ __forceinline__ void sv1(f32x4& acc0, f32x4& acc1, const u32 (&pk)[30][13], u32 pp, u32 mask0, u32 mask1) {
  asm volatile(
    "v_accvgpr_read_b32 v38, %14\n\t"
    "v_accvgpr_read_b32 v26, %4\n\t"
    "v_accvgpr_read_b32 v33, %7\n\t"
    "v_accvgpr_read_b32 v24, %2\n\t"
    "v_accvgpr_read_b32 v31, %5\n\t"
    "v_accvgpr_read_b32 v25, %3\n\t"
    "v_accvgpr_read_b32 v32, %6\n\t"
    "v_lshlrev_b32 v29, 7, v38\n\t"
    "v_lshlrev_b32 v36, 5, v38\n\t"
    "v_and_b32 v27, %18, v26\n\t"
    "v_and_b32 v34, %18, v33\n\t"
    "v_lshrrev_b32 v28, 4, v26\n\t"
    "v_lshrrev_b32 v35, 4, v33\n\t"
    "v_lshlrev_b32 v30, 6, v38\n\t"
    "v_lshlrev_b32 v37, 4, v38\n\t"
    "v_and_or_b32 v27, v29, %19, v27\n\t"
    "v_and_or_b32 v34, v36, %19, v34\n\t"
    "v_and_b32 v28, %18, v28\n\t"
    "v_and_b32 v35, %18, v35\n\t"
    "v_perm_b32 v16, v27, v24, %20\n\t"
    "v_perm_b32 v18, v34, v31, %20\n\t"
    "v_and_or_b32 v28, v30, %19, v28\n\t"
    "v_and_or_b32 v35, v37, %19, v35\n\t"
    "v_perm_b32 v17, v27, v24, %21\n\t"
    "v_perm_b32 v19, v34, v31, %21\n\t"
    "v_perm_b32 v20, v28, v25, %20\n\t"
    "v_perm_b32 v22, v35, v32, %20\n\t"
    "v_perm_b32 v21, v28, v25, %21\n\t"
    "v_perm_b32 v23, v35, v32, %21\n\t"
    "v_and_b32_dpp v0, %15, %16 quad_perm:[0,0,0,0] row_mask:0xf bank_mask:0xf\n\t"
    "v_and_b32_dpp v1, %15, %17 quad_perm:[0,0,0,0] row_mask:0xf bank_mask:0xf\n\t"
    "v_and_b32_dpp v2, %15, %16 quad_perm:[1,1,1,1] row_mask:0xf bank_mask:0xf\n\t"
    "v_and_b32_dpp v3, %15, %17 quad_perm:[1,1,1,1] row_mask:0xf bank_mask:0xf\n\t"
    "v_accvgpr_read_b32 v26, %10\n\t"
    "v_accvgpr_read_b32 v33, %13\n\t"
    "v_mfma_f32_16x16x32_bf16 %0, v[0:3], v[16:19], %0\n\t"
    "v_accvgpr_read_b32 v24, %8\n\t"
    "v_accvgpr_read_b32 v31, %11\n\t"
    "v_accvgpr_read_b32 v25, %9\n\t"
    "v_mfma_f32_16x16x32_bf16 %1, v[0:3], v[20:23], %1\n\t"
    "v_accvgpr_read_b32 v32, %12\n\t"
    "v_lshlrev_b32 v29, 3, v38\n\t"
    "v_lshlrev_b32 v36, 1, v38\n\t"
    "v_and_b32 v27, %18, v26\n\t"
    "v_and_b32 v34, %18, v33\n\t"
    "v_lshrrev_b32 v28, 4, v26\n\t"
    "v_lshrrev_b32 v35, 4, v33\n\t"
    "v_lshlrev_b32 v30, 2, v38\n\t"
    "v_and_or_b32 v34, v36, %19, v34\n\t"
    "v_and_or_b32 v27, v29, %19, v27\n\t"
    "v_and_b32 v35, %18, v35\n\t"
    "v_and_b32 v28, %18, v28\n\t"
    "v_perm_b32 v18, v34, v31, %20\n\t"
    "v_perm_b32 v16, v27, v24, %20\n\t"
    "v_and_or_b32 v35, v38, %19, v35\n\t"
    "v_and_or_b32 v28, v30, %19, v28\n\t"
    "v_perm_b32 v19, v34, v31, %21\n\t"
    "v_perm_b32 v17, v27, v24, %21\n\t"
    "v_perm_b32 v22, v35, v32, %20\n\t"
    "v_perm_b32 v20, v28, v25, %20\n\t"
    "v_perm_b32 v23, v35, v32, %21\n\t"
    "v_perm_b32 v21, v28, v25, %21\n\t"
    "v_and_b32_dpp v4, %15, %16 quad_perm:[2,2,2,2] row_mask:0xf bank_mask:0xf\n\t"
    "v_and_b32_dpp v5, %15, %17 quad_perm:[2,2,2,2] row_mask:0xf bank_mask:0xf\n\t"
    "v_and_b32_dpp v6, %15, %16 quad_perm:[3,3,3,3] row_mask:0xf bank_mask:0xf\n\t"
    "v_and_b32_dpp v7, %15, %17 quad_perm:[3,3,3,3] row_mask:0xf bank_mask:0xf\n\t"
    "s_nop 1\n\t"
    "v_mfma_f32_16x16x32_bf16 %0, v[4:7], v[16:19], %0\n\t"
    "v_mfma_f32_16x16x32_bf16 %1, v[4:7], v[20:23], %1\n\t"
    : "+v"(acc0), "+v"(acc1)
    : "a"(pk[3][0]), "a"(pk[3][1]), "a"(pk[3][2]), "a"(pk[3][3]), "a"(pk[3][4]), "a"(pk[3][5]), "a"(pk[3][6]), "a"(pk[3][7]), "a"(pk[3][8]), "a"(pk[3][9]), "a"(pk[3][10]), "a"(pk[3][11]), "a"(pk[3][12]), "v"(pp), "v"(mask0), "v"(mask1), "s"(0x0f0f0f0fu), "s"(0x80808080u), "s"(0x05010400u), "s"(0x07030602u)
    : "v0", "v1", "v2", "v3", "v4", "v5", "v6", "v7", "v8", "v9", "v10", "v11", "v12", "v13", "v14", "v15", "v16", "v17", "v18", "v19", "v20", "v21", "v22", "v23", "v24", "v25", "v26", "v27", "v28", "v29", "v30", "v31", "v32", "v33", "v34", "v35", "v36", "v37", "v38", "v39");
}
__device__ __forceinline__ void sk2(float& X, const u32 (&pk)[30][13], u32x4 q, u64 modd, u64 mhi) {
  asm volatile(
    "v_accvgpr_read_b32 v38, %13\n\t"
    "v_accvgpr_read_b32 v26, %3\n\t"
    "v_accvgpr_read_b32 v33, %6\n\t"
    "v_accvgpr_read_b32 v24, %1\n\t"
    "v_accvgpr_read_b32 v31, %4\n\t"
    "v_accvgpr_read_b32 v25, %2\n\t"
    "v_accvgpr_read_b32 v32, %5\n\t"
    "v_lshlrev_b32 v29, 7, v38\n\t"
    "v_lshlrev_b32 v36, 5, v38\n\t"
    "v_and_b32 v27, %17, v26\n\t"
    "v_and_b32 v34, %17, v33\n\t"
    "v_lshrrev_b32 v28, 4, v26\n\t"
    "v_lshrrev_b32 v35, 4, v33\n\t"
    "v_lshlrev_b32 v30, 6, v38\n\t"
    "v_lshlrev_b32 v37, 4, v38\n\t"
    "v_and_or_b32 v27, v29, %18, v27\n\t"
    "v_and_or_b32 v34, v36, %18, v34\n\t"
    "v_and_b32 v28, %17, v28\n\t"
    "v_and_b32 v35, %17, v35\n\t"
    "v_perm_b32 v16, v27, v24, %19\n\t"
    "v_perm_b32 v20, v34, v31, %19\n\t"
    "v_and_or_b32 v28, v30, %18, v28\n\t"
    "v_and_or_b32 v35, v37, %18, v35\n\t"
    "v_perm_b32 v17, v27, v24, %20\n\t"
    "v_perm_b32 v21, v34, v31, %20\n\t"
    "v_perm_b32 v18, v28, v25, %19\n\t"
    "v_perm_b32 v22, v35, v32, %19\n\t"
    "v_perm_b32 v19, v28, v25, %20\n\t"
    "v_perm_b32 v23, v35, v32, %20\n\t"
    "v_accvgpr_read_b32 v26, %9\n\t"
    "v_mfma_f32_16x16x32_bf16 v[0:3], v[16:19], %14, 0\n\t"
    "v_accvgpr_read_b32 v33, %12\n\t"
    "v_accvgpr_read_b32 v24, %7\n\t"
    "v_mfma_f32_16x16x32_bf16 v[4:7], v[20:23], %14, 0\n\t"
    "v_accvgpr_read_b32 v31, %10\n\t"
    "v_accvgpr_read_b32 v25, %8\n\t"
    "v_accvgpr_read_b32 v32, %11\n\t"
    "v_lshlrev_b32 v29, 3, v38\n\t"
    "v_lshlrev_b32 v36, 1, v38\n\t"
    "v_and_b32 v27, %17, v26\n\t"
    "v_and_b32 v34, %17, v33\n\t"
    "v_lshrrev_b32 v28, 4, v26\n\t"
    "v_lshrrev_b32 v35, 4, v33\n\t"
    "v_lshlrev_b32 v30, 2, v38\n\t"
    "v_and_or_b32 v34, v36, %18, v34\n\t"
    "v_and_or_b32 v27, v29, %18, v27\n\t"
    "v_and_b32 v35, %17, v35\n\t"
    "v_and_b32 v28, %17, v28\n\t"
    "v_perm_b32 v20, v34, v31, %19\n\t"
    "v_perm_b32 v16, v27, v24, %19\n\t"
    "v_and_or_b32 v35, v38, %18, v35\n\t"
    "v_and_or_b32 v28, v30, %18, v28\n\t"
    "v_perm_b32 v21, v34, v31, %20\n\t"
    "v_perm_b32 v17, v27, v24, %20\n\t"
    "v_perm_b32 v22, v35, v32, %19\n\t"
    "v_perm_b32 v18, v28, v25, %19\n\t"
    "v_perm_b32 v23, v35, v32, %20\n\t"
    "v_perm_b32 v19, v28, v25, %20\n\t"
    "v_add_f32_dpp v0, v1, v0 quad_perm:[1,1,1,1] row_mask:0xf bank_mask:0xf\n\t"
    "v_add_f32_dpp v5, v6, v5 quad_perm:[2,2,2,2] row_mask:0xf bank_mask:0xf\n\t"
    "v_mfma_f32_16x16x32_bf16 v[8:11], v[16:19], %14, 0\n\t"
    "v_add_f32_dpp v0, v2, v0 quad_perm:[2,2,2,2] row_mask:0xf bank_mask:0xf\n\t"
    "v_mfma_f32_16x16x32_bf16 v[12:15], v[20:23], %14, 0\n\t"
    "v_add_f32_dpp v5, v7, v5 quad_perm:[3,3,3,3] row_mask:0xf bank_mask:0xf\n\t"
    "v_add_f32_dpp v0, v3, v0 quad_perm:[3,3,3,3] row_mask:0xf bank_mask:0xf\n\t"
    "v_add_f32_dpp v5, v4, v5 quad_perm:[0,0,0,0] row_mask:0xf bank_mask:0xf\n\t"
    "v_cndmask_b32_e64 v0, v0, v5, %15\n\t"
    "s_nop 1\n\t"
    "v_add_f32_dpp v10, v11, v10 quad_perm:[3,3,3,3] row_mask:0xf bank_mask:0xf\n\t"
    "s_nop 0\n\t"
    "v_add_f32_dpp v15, v12, v15 quad_perm:[0,0,0,0] row_mask:0xf bank_mask:0xf\n\t"
    "v_add_f32_dpp v10, v8, v10 quad_perm:[0,0,0,0] row_mask:0xf bank_mask:0xf\n\t"
    "v_add_f32_dpp v15, v13, v15 quad_perm:[1,1,1,1] row_mask:0xf bank_mask:0xf\n\t"
    "v_add_f32_dpp v10, v9, v10 quad_perm:[1,1,1,1] row_mask:0xf bank_mask:0xf\n\t"
    "v_add_f32_dpp v15, v14, v15 quad_perm:[2,2,2,2] row_mask:0xf bank_mask:0xf\n\t"
    "v_cndmask_b32_e64 v10, v10, v15, %15\n\t"
    "v_cndmask_b32_e64 %0, v0, v10, %16\n\t"
    : "=&v"(X)
    : "a"(pk[4][0]), "a"(pk[4][1]), "a"(pk[4][2]), "a"(pk[4][3]), "a"(pk[4][4]), "a"(pk[4][5]), "a"(pk[4][6]), "a"(pk[4][7]), "a"(pk[4][8]), "a"(pk[4][9]), "a"(pk[4][10]), "a"(pk[4][11]), "a"(pk[4][12]), "v"(q), "s"(modd), "s"(mhi), "s"(0x0f0f0f0fu), "s"(0x80808080u), "s"(0x05010400u), "s"(0x07030602u)
    : "v0", "v1", "v2", "v3", "v4", "v5", "v6", "v7", "v8", "v9", "v10", "v11", "v12", "v13", "v14", "v15", "v16", "v17", "v18", "v19", "v20", "v21", "v22", "v23", "v24", "v25", "v26", "v27", "v28", "v29", "v30", "v31", "v32", "v33", "v34", "v35", "v36", "v37", "v38", "v39");
}
__device__ __forceinline__ void sv2(f32x4& acc0, f32x4& acc1, const u32 (&pk)[30][13], u32 pp, u32 mask0, u32 mask1) {
  asm volatile(
    "v_accvgpr_read_b32 v38, %14\n\t"
    "v_accvgpr_read_b32 v26, %4\n\t"
    "v_accvgpr_read_b32 v33, %7\n\t"
    "v_accvgpr_read_b32 v24, %2\n\t"
    "v_accvgpr_read_b32 v31, %5\n\t"
    "v_accvgpr_read_b32 v25, %3\n\t"
    "v_accvgpr_read_b32 v32, %6\n\t"
    "v_lshlrev_b32 v29, 7, v38\n\t"
    "v_lshlrev_b32 v36, 5, v38\n\t"
    "v_and_b32 v27, %18, v26\n\t"
    "v_and_b32 v34, %18, v33\n\t"
    "v_lshrrev_b32 v28, 4, v26\n\t"
    "v_lshrrev_b32 v35, 4, v33\n\t"
    "v_lshlrev_b32 v30, 6, v38\n\t"
    "v_lshlrev_b32 v37, 4, v38\n\t"
    "v_and_or_b32 v27, v29, %19, v27\n\t"
    "v_and_or_b32 v34, v36, %19, v34\n\t"
    "v_and_b32 v28, %18, v28\n\t"
    "v_and_b32 v35, %18, v35\n\t"
    "v_perm_b32 v16, v27, v24, %20\n\t"
    "v_perm_b32 v18, v34, v31, %20\n\t"
    "v_and_or_b32 v28, v30, %19, v28\n\t"
    "v_and_or_b32 v35, v37, %19, v35\n\t"
    "v_perm_b32 v17, v27, v24, %21\n\t"
    "v_perm_b32 v19, v34, v31, %21\n\t"
    "v_perm_b32 v20, v28, v25, %20\n\t"
    "v_perm_b32 v22, v35, v32, %20\n\t"
    "v_perm_b32 v21, v28, v25, %21\n\t"
    "v_perm_b32 v23, v35, v32, %21\n\t"
    "v_and_b32_dpp v0, %15, %16 quad_perm:[0,0,0,0] row_mask:0xf bank_mask:0xf\n\t"
    "v_and_b32_dpp v1, %15, %17 quad_perm:[0,0,0,0] row_mask:0xf bank_mask:0xf\n\t"
    "v_and_b32_dpp v2, %15, %16 quad_perm:[1,1,1,1] row_mask:0xf bank_mask:0xf\n\t"
    "v_and_b32_dpp v3, %15, %17 quad_perm:[1,1,1,1] row_mask:0xf bank_mask:0xf\n\t"
    "v_accvgpr_read_b32 v26, %10\n\t"
    "v_accvgpr_read_b32 v33, %13\n\t"
    "v_mfma_f32_16x16x32_bf16 %0, v[0:3], v[16:19], %0\n\t"
    "v_accvgpr_read_b32 v24, %8\n\t"
    "v_accvgpr_read_b32 v31, %11\n\t"
    "v_accvgpr_read_b32 v25, %9\n\t"
    "v_mfma_f32_16x16x32_bf16 %1, v[0:3], v[20:23], %1\n\t"
    "v_accvgpr_read_b32 v32, %12\n\t"
    "v_lshlrev_b32 v29, 3, v38\n\t"
    "v_lshlrev_b32 v36, 1, v38\n\t"
    "v_and_b32 v27, %18, v26\n\t"
    "v_and_b32 v34, %18, v33\n\t"
    "v_lshrrev_b32 v28, 4, v26\n\t"
    "v_lshrrev_b32 v35, 4, v33\n\t"
    "v_lshlrev_b32 v30, 2, v38\n\t"
    "v_and_or_b32 v34, v36, %19, v34\n\t"
    "v_and_or_b32 v27, v29, %19, v27\n\t"
    "v_and_b32 v35, %18, v35\n\t"
    "v_and_b32 v28, %18, v28\n\t"
    "v_perm_b32 v18, v34, v31, %20\n\t"
    "v_perm_b32 v16, v27, v24, %20\n\t"
    "v_and_or_b32 v35, v38, %19, v35\n\t"
    "v_and_or_b32 v28, v30, %19, v28\n\t"
    "v_perm_b32 v19, v34, v31, %21\n\t"
    "v_perm_b32 v17, v27, v24, %21\n\t"
    "v_perm_b32 v22, v35, v32, %20\n\t"
    "v_perm_b32 v20, v28, v25, %20\n\t"
    "v_perm_b32 v23, v35, v32, %21\n\t"
    "v_perm_b32 v21, v28, v25, %21\n\t"
    "v_and_b32_dpp v4, %15, %16 quad_perm:[2,2,2,2] row_mask:0xf bank_mask:0xf\n\t"
    "v_and_b32_dpp v5, %15, %17 quad_perm:[2,2,2,2] row_mask:0xf bank_mask:0xf\n\t"
    "v_and_b32_dpp v6, %15, %16 quad_perm:[3,3,3,3] row_mask:0xf bank_mask:0xf\n\t"
    "v_and_b32_dpp v7, %15, %17 quad_perm:[3,3,3,3] row_mask:0xf bank_mask:0xf\n\t"
    "s_nop 1\n\t"
    "v_mfma_f32_16x16x32_bf16 %0, v[4:7], v[16:19], %0\n\t"
    "v_mfma_f32_16x16x32_bf16 %1, v[4:7], v[20:23], %1\n\t"
    : "+v"(acc0), "+v"(acc1)
    : "a"(pk[5][0]), "a"(pk[5][1]), "a"(pk[5][2]), "a"(pk[5][3]), "a"(pk[5][4]), "a"(pk[5][5]), "a"(pk[5][6]), "a"(pk[5][7]), "a"(pk[5][8]), "a"(pk[5][9]), "a"(pk[5][10]), "a"(pk[5][11]), "a"(pk[5][12]), "v"(pp), "v"(mask0), "v"(mask1), "s"(0x0f0f0f0fu), "s"(0x80808080u), "s"(0x05010400u), "s"(0x07030602u)
    : "v0", "v1", "v2", "v3", "v4", "v5", "v6", "v7", "v8", "v9", "v10", "v11", "v12", "v13", "v14", "v15", "v16", "v17", "v18", "v19", "v20", "v21", "v22", "v23", "v24", "v25", "v26", "v27", "v28", "v29", "v30", "v31", "v32", "v33", "v34", "v35", "v36", "v37", "v38", "v39");
}
__device__ __forceinline__ void sk3(float& X, const u32 (&pk)[30][13], u32x4 q, u64 modd, u64 mhi) {
  asm volatile(
    "v_accvgpr_read_b32 v38, %13\n\t"
    "v_accvgpr_read_b32 v26, %3\n\t"
    "v_accvgpr_read_b32 v33, %6\n\t"
    "v_accvgpr_read_b32 v24, %1\n\t"
    "v_accvgpr_read_b32 v31, %4\n\t"
    "v_accvgpr_read_b32 v25, %2\n\t"
    "v_accvgpr_read_b32 v32, %5\n\t"
    "v_lshlrev_b32 v29, 7, v38\n\t"
    "v_lshlrev_b32 v36, 5, v38\n\t"
    "v_and_b32 v27, %17, v26\n\t"
    "v_and_b32 v34, %17, v33\n\t"
    "v_lshrrev_b32 v28, 4, v26\n\t"
    "v_lshrrev_b32 v35, 4, v33\n\t"
    "v_lshlrev_b32 v30, 6, v38\n\t"
    "v_lshlrev_b32 v37, 4, v38\n\t"
    "v_and_or_b32 v27, v29, %18, v27\n\t"
    "v_and_or_b32 v34, v36, %18, v34\n\t"
    "v_and_b32 v28, %17, v28\n\t"
    "v_and_b32 v35, %17, v35\n\t"
    "v_perm_b32 v16, v27, v24, %19\n\t"
    "v_perm_b32 v20, v34, v31, %19\n\t"
    "v_and_or_b32 v28, v30, %18, v28\n\t"
    "v_and_or_b32 v35, v37, %18, v35\n\t"
    "v_perm_b32 v17, v27, v24, %20\n\t"
    "v_perm_b32 v21, v34, v31, %20\n\t"
    "v_perm_b32 v18, v28, v25, %19\n\t"
    "v_perm_b32 v22, v35, v32, %19\n\t"
    "v_perm_b32 v19, v28, v25, %20\n\t"
    "v_perm_b32 v23, v35, v32, %20\n\t"
    "v_accvgpr_read_b32 v26, %9\n\t"
    "v_mfma_f32_16x16x32_bf16 v[0:3], v[16:19], %14, 0\n\t"
    "v_accvgpr_read_b32 v33, %12\n\t"
    "v_accvgpr_read_b32 v24, %7\n\t"
    "v_mfma_f32_16x16x32_bf16 v[4:7], v[20:23], %14, 0\n\t"
    "v_accvgpr_read_b32 v31, %10\n\t"
    "v_accvgpr_read_b32 v25, %8\n\t"
    "v_accvgpr_read_b32 v32, %11\n\t"
    "v_lshlrev_b32 v29, 3, v38\n\t"
    "v_lshlrev_b32 v36, 1, v38\n\t"
    "v_and_b32 v27, %17, v26\n\t"
    "v_and_b32 v34, %17, v33\n\t"
    "v_lshrrev_b32 v28, 4, v26\n\t"
    "v_lshrrev_b32 v35, 4, v33\n\t"
    "v_lshlrev_b32 v30, 2, v38\n\t"
    "v_and_or_b32 v34, v36, %18, v34\n\t"
    "v_and_or_b32 v27, v29, %18, v27\n\t"
    "v_and_b32 v35, %17, v35\n\t"
    "v_and_b32 v28, %17, v28\n\t"
    "v_perm_b32 v20, v34, v31, %19\n\t"
    "v_perm_b32 v16, v27, v24, %19\n\t"
    "v_and_or_b32 v35, v38, %18, v35\n\t"
    "v_and_or_b32 v28, v30, %18, v28\n\t"
    "v_perm_b32 v21, v34, v31, %20\n\t"
    "v_perm_b32 v17, v27, v24, %20\n\t"
    "v_perm_b32 v22, v35, v32, %19\n\t"
    "v_perm_b32 v18, v28, v25, %19\n\t"
    "v_perm_b32 v23, v35, v32, %20\n\t"
    "v_perm_b32 v19, v28, v25, %20\n\t"
    "v_add_f32_dpp v0, v1, v0 quad_perm:[1,1,1,1] row_mask:0xf bank_mask:0xf\n\t"
    "v_add_f32_dpp v5, v6, v5 quad_perm:[2,2,2,2] row_mask:0xf bank_mask:0xf\n\t"
    "v_mfma_f32_16x16x32_bf16 v[8:11], v[16:19], %14, 0\n\t"
    "v_add_f32_dpp v0, v2, v0 quad_perm:[2,2,2,2] row_mask:0xf bank_mask:0xf\n\t"
    "v_mfma_f32_16x16x32_bf16 v[12:15], v[20:23], %14, 0\n\t"
    "v_add_f32_dpp v5, v7, v5 quad_perm:[3,3,3,3] row_mask:0xf bank_mask:0xf\n\t"
    "v_add_f32_dpp v0, v3, v0 quad_perm:[3,3,3,3] row_mask:0xf bank_mask:0xf\n\t"
    "v_add_f32_dpp v5, v4, v5 quad_perm:[0,0,0,0] row_mask:0xf bank_mask:0xf\n\t"
    "v_cndmask_b32_e64 v0, v0, v5, %15\n\t"
    "s_nop 1\n\t"
    "v_add_f32_dpp v10, v11, v10 quad_perm:[3,3,3,3] row_mask:0xf bank_mask:0xf\n\t"
    "s_nop 0\n\t"
    "v_add_f32_dpp v15, v12, v15 quad_perm:[0,0,0,0] row_mask:0xf bank_mask:0xf\n\t"
    "v_add_f32_dpp v10, v8, v10 quad_perm:[0,0,0,0] row_mask:0xf bank_mask:0xf\n\t"
    "v_add_f32_dpp v15, v13, v15 quad_perm:[1,1,1,1] row_mask:0xf bank_mask:0xf\n\t"
    "v_add_f32_dpp v10, v9, v10 quad_perm:[1,1,1,1] row_mask:0xf bank_mask:0xf\n\t"
    "v_add_f32_dpp v15, v14, v15 quad_perm:[2,2,2,2] row_mask:0xf bank_mask:0xf\n\t"
    "v_cndmask_b32_e64 v10, v10, v15, %15\n\t"
    "v_cndmask_b32_e64 %0, v0, v10, %16\n\t"
    : "=&v"(X)
    : "a"(pk[6][0]), "a"(pk[6][1]), "a"(pk[6][2]), "a"(pk[6][3]), "a"(pk[6][4]), "a"(pk[6][5]), "a"(pk[6][6]), "a"(pk[6][7]), "a"(pk[6][8]), "a"(pk[6][9]), "a"(pk[6][10]), "a"(pk[6][11]), "a"(pk[6][12]), "v"(q), "s"(modd), "s"(mhi), "s"(0x0f0f0f0fu), "s"(0x80808080u), "s"(0x05010400u), "s"(0x07030602u)
    : "v0", "v1", "v2", "v3", "v4", "v5", "v6", "v7", "v8", "v9", "v10", "v11", "v12", "v13", "v14", "v15", "v16", "v17", "v18", "v19", "v20", "v21", "v22", "v23", "v24", "v25", "v26", "v27", "v28", "v29", "v30", "v31", "v32", "v33", "v34", "v35", "v36", "v37", "v38", "v39");
}
__device__ __forceinline__ void sv3(f32x4& acc0, f32x4& acc1, const u32 (&pk)[30][13], u32 pp, u32 mask0, u32 mask1) {
  asm volatile(
    "v_accvgpr_read_b32 v38, %14\n\t"
    "v_accvgpr_read_b32 v26, %4\n\t"
    "v_accvgpr_read_b32 v33, %7\n\t"
    "v_accvgpr_read_b32 v24, %2\n\t"
    "v_accvgpr_read_b32 v31, %5\n\t"
    "v_accvgpr_read_b32 v25, %3\n\t"
    "v_accvgpr_read_b32 v32, %6\n\t"
    "v_lshlrev_b32 v29, 7, v38\n\t"
    "v_lshlrev_b32 v36, 5, v38\n\t"
    "v_and_b32 v27, %18, v26\n\t"
    "v_and_b32 v34, %18, v33\n\t"
    "v_lshrrev_b32 v28, 4, v26\n\t"
    "v_lshrrev_b32 v35, 4, v33\n\t"
    "v_lshlrev_b32 v30, 6, v38\n\t"
    "v_lshlrev_b32 v37, 4, v38\n\t"
    "v_and_or_b32 v27, v29, %19, v27\n\t"
    "v_and_or_b32 v34, v36, %19, v34\n\t"
    "v_and_b32 v28, %18, v28\n\t"
    "v_and_b32 v35, %18, v35\n\t"
    "v_perm_b32 v16, v27, v24, %20\n\t"
    "v_perm_b32 v18, v34, v31, %20\n\t"
    "v_and_or_b32 v28, v30, %19, v28\n\t"
    "v_and_or_b32 v35, v37, %19, v35\n\t"
    "v_perm_b32 v17, v27, v24, %21\n\t"
    "v_perm_b32 v19, v34, v31, %21\n\t"
    "v_perm_b32 v20, v28, v25, %20\n\t"
    "v_perm_b32 v22, v35, v32, %20\n\t"
    "v_perm_b32 v21, v28, v25, %21\n\t"
    "v_perm_b32 v23, v35, v32, %21\n\t"
    "v_and_b32_dpp v0, %15, %16 quad_perm:[0,0,0,0] row_mask:0xf bank_mask:0xf\n\t"
    "v_and_b32_dpp v1, %15, %17 quad_perm:[0,0,0,0] row_mask:0xf bank_mask:0xf\n\t"
    "v_and_b32_dpp v2, %15, %16 quad_perm:[1,1,1,1] row_mask:0xf bank_mask:0xf\n\t"
    "v_and_b32_dpp v3, %15, %17 quad_perm:[1,1,1,1] row_mask:0xf bank_mask:0xf\n\t"
    "v_accvgpr_read_b32 v26, %10\n\t"
    "v_accvgpr_read_b32 v33, %13\n\t"
    "v_mfma_f32_16x16x32_bf16 %0, v[0:3], v[16:19], %0\n\t"
    "v_accvgpr_read_b32 v24, %8\n\t"
    "v_accvgpr_read_b32 v31, %11\n\t"
    "v_accvgpr_read_b32 v25, %9\n\t"
    "v_mfma_f32_16x16x32_bf16 %1, v[0:3], v[20:23], %1\n\t"
    "v_accvgpr_read_b32 v32, %12\n\t"
    "v_lshlrev_b32 v29, 3, v38\n\t"
    "v_lshlrev_b32 v36, 1, v38\n\t"
    "v_and_b32 v27, %18, v26\n\t"
    "v_and_b32 v34, %18, v33\n\t"
    "v_lshrrev_b32 v28, 4, v26\n\t"
    "v_lshrrev_b32 v35, 4, v33\n\t"
    "v_lshlrev_b32 v30, 2, v38\n\t"
    "v_and_or_b32 v34, v36, %19, v34\n\t"
    "v_and_or_b32 v27, v29, %19, v27\n\t"
    "v_and_b32 v35, %18, v35\n\t"
    "v_and_b32 v28, %18, v28\n\t"
    "v_perm_b32 v18, v34, v31, %20\n\t"
    "v_perm_b32 v16, v27, v24, %20\n\t"
    "v_and_or_b32 v35, v38, %19, v35\n\t"
    "v_and_or_b32 v28, v30, %19, v28\n\t"
    "v_perm_b32 v19, v34, v31, %21\n\t"
    "v_perm_b32 v17, v27, v24, %21\n\t"
    "v_perm_b32 v22, v35, v32, %20\n\t"
    "v_perm_b32 v20, v28, v25, %20\n\t"
    "v_perm_b32 v23, v35, v32, %21\n\t"
    "v_perm_b32 v21, v28, v25, %21\n\t"
    "v_and_b32_dpp v4, %15, %16 quad_perm:[2,2,2,2] row_mask:0xf bank_mask:0xf\n\t"
    "v_and_b32_dpp v5, %15, %17 quad_perm:[2,2,2,2] row_mask:0xf bank_mask:0xf\n\t"
    "v_and_b32_dpp v6, %15, %16 quad_perm:[3,3,3,3] row_mask:0xf bank_mask:0xf\n\t"
    "v_and_b32_dpp v7, %15, %17 quad_perm:[3,3,3,3] row_mask:0xf bank_mask:0xf\n\t"
    "s_nop 1\n\t"
    "v_mfma_f32_16x16x32_bf16 %0, v[4:7], v[16:19], %0\n\t"
    "v_mfma_f32_16x16x32_bf16 %1, v[4:7], v[20:23], %1\n\t"
    : "+v"(acc0), "+v"(acc1)
    : "a"(pk[7][0]), "a"(pk[7][1]), "a"(pk[7][2]), "a"(pk[7][3]), "a"(pk[7][4]), "a"(pk[7][5]), "a"(pk[7][6]), "a"(pk[7][7]), "a"(pk[7][8]), "a"(pk[7][9]), "a"(pk[7][10]), "a"(pk[7][11]), "a"(pk[7][12]), "v"(pp), "v"(mask0), "v"(mask1), "s"(0x0f0f0f0fu), "s"(0x80808080u), "s"(0x05010400u), "s"(0x07030602u)
    : "v0", "v1", "v2", "v3", "v4", "v5", "v6", "v7", "v8", "v9", "v10", "v11", "v12", "v13", "v14", "v15", "v16", "v17", "v18", "v19", "v20", "v21", "v22", "v23", "v24", "v25", "v26", "v27", "v28", "v29", "v30", "v31", "v32", "v33", "v34", "v35", "v36", "v37", "v38", "v39");
}
__device__ __forceinline__ void sk4(float& X, const u32 (&pk)[30][13], u32x4 q, u64 modd, u64 mhi) {
  asm volatile(
    "v_accvgpr_read_b32 v38, %13\n\t"
    "v_accvgpr_read_b32 v26, %3\n\t"
    "v_accvgpr_read_b32 v33, %6\n\t"
    "v_accvgpr_read_b32 v24, %1\n\t"
    "v_accvgpr_read_b32 v31, %4\n\t"
    "v_accvgpr_read_b32 v25, %2\n\t"
    "v_accvgpr_read_b32 v32, %5\n\t"
    "v_lshlrev_b32 v29, 7, v38\n\t"
    "v_lshlrev_b32 v36, 5, v38\n\t"
    "v_and_b32 v27, %17, v26\n\t"
    "v_and_b32 v34, %17, v33\n\t"
    "v_lshrrev_b32 v28, 4, v26\n\t"
    "v_lshrrev_b32 v35, 4, v33\n\t"
    "v_lshlrev_b32 v30, 6, v38\n\t"
    "v_lshlrev_b32 v37, 4, v38\n\t"
    "v_and_or_b32 v27, v29, %18, v27\n\t"
    "v_and_or_b32 v34, v36, %18, v34\n\t"
    "v_and_b32 v28, %17, v28\n\t"
    "v_and_b32 v35, %17, v35\n\t"
    "v_perm_b32 v16, v27, v24, %19\n\t"
    "v_perm_b32 v20, v34, v31, %19\n\t"
    "v_and_or_b32 v28, v30, %18, v28\n\t"
    "v_and_or_b32 v35, v37, %18, v35\n\t"
    "v_perm_b32 v17, v27, v24, %20\n\t"
    "v_perm_b32 v21, v34, v31, %20\n\t"
    "v_perm_b32 v18, v28, v25, %19\n\t"
    "v_perm_b32 v22, v35, v32, %19\n\t"
    "v_perm_b32 v19, v28, v25, %20\n\t"
    "v_perm_b32 v23, v35, v32, %20\n\t"
    "v_accvgpr_read_b32 v26, %9\n\t"
    "v_mfma_f32_16x16x32_bf16 v[0:3], v[16:19], %14, 0\n\t"
    "v_accvgpr_read_b32 v33, %12\n\t"
    "v_accvgpr_read_b32 v24, %7\n\t"
    "v_mfma_f32_16x16x32_bf16 v[4:7], v[20:23], %14, 0\n\t"
    "v_accvgpr_read_b32 v31, %10\n\t"
    "v_accvgpr_read_b32 v25, %8\n\t"
    "v_accvgpr_read_b32 v32, %11\n\t"
    "v_lshlrev_b32 v29, 3, v38\n\t"
    "v_lshlrev_b32 v36, 1, v38\n\t"
    "v_and_b32 v27, %17, v26\n\t"
    "v_and_b32 v34, %17, v33\n\t"
    "v_lshrrev_b32 v28, 4, v26\n\t"
    "v_lshrrev_b32 v35, 4, v33\n\t"
    "v_lshlrev_b32 v30, 2, v38\n\t"
    "v_and_or_b32 v34, v36, %18, v34\n\t"
    "v_and_or_b32 v27, v29, %18, v27\n\t"
    "v_and_b32 v35, %17, v35\n\t"
    "v_and_b32 v28, %17, v28\n\t"
    "v_perm_b32 v20, v34, v31, %19\n\t"
    "v_perm_b32 v16, v27, v24, %19\n\t"
    "v_and_or_b32 v35, v38, %18, v35\n\t"
    "v_and_or_b32 v28, v30, %18, v28\n\t"
    "v_perm_b32 v21, v34, v31, %20\n\t"
    "v_perm_b32 v17, v27, v24, %20\n\t"
    "v_perm_b32 v22, v35, v32, %19\n\t"
    "v_perm_b32 v18, v28, v25, %19\n\t"
    "v_perm_b32 v23, v35, v32, %20\n\t"
    "v_perm_b32 v19, v28, v25, %20\n\t"
    "v_add_f32_dpp v0, v1, v0 quad_perm:[1,1,1,1] row_mask:0xf bank_mask:0xf\n\t"
    "v_add_f32_dpp v5, v6, v5 quad_perm:[2,2,2,2] row_mask:0xf bank_mask:0xf\n\t"
    "v_mfma_f32_16x16x32_bf16 v[8:11], v[16:19], %14, 0\n\t"
    "v_add_f32_dpp v0, v2, v0 quad_perm:[2,2,2,2] row_mask:0xf bank_mask:0xf\n\t"
    "v_mfma_f32_16x16x32_bf16 v[12:15], v[20:23], %14, 0\n\t"
    "v_add_f32_dpp v5, v7, v5 quad_perm:[3,3,3,3] row_mask:0xf bank_mask:0xf\n\t"
    "v_add_f32_dpp v0, v3, v0 quad_perm:[3,3,3,3] row_mask:0xf bank_mask:0xf\n\t"
    "v_add_f32_dpp v5, v4, v5 quad_perm:[0,0,0,0] row_mask:0xf bank_mask:0xf\n\t"
    "v_cndmask_b32_e64 v0, v0, v5, %15\n\t"
    "s_nop 1\n\t"
    "v_add_f32_dpp v10, v11, v10 quad_perm:[3,3,3,3] row_mask:0xf bank_mask:0xf\n\t"
    "s_nop 0\n\t"
    "v_add_f32_dpp v15, v12, v15 quad_perm:[0,0,0,0] row_mask:0xf bank_mask:0xf\n\t"
    "v_add_f32_dpp v10, v8, v10 quad_perm:[0,0,0,0] row_mask:0xf bank_mask:0xf\n\t"
    "v_add_f32_dpp v15, v13, v15 quad_perm:[1,1,1,1] row_mask:0xf bank_mask:0xf\n\t"
    "v_add_f32_dpp v10, v9, v10 quad_perm:[1,1,1,1] row_mask:0xf bank_mask:0xf\n\t"
    "v_add_f32_dpp v15, v14, v15 quad_perm:[2,2,2,2] row_mask:0xf bank_mask:0xf\n\t"
    "v_cndmask_b32_e64 v10, v10, v15, %15\n\t"
    "v_cndmask_b32_e64 %0, v0, v10, %16\n\t"
    : "=&v"(X)
    : "a"(pk[8][0]), "a"(pk[8][1]), "a"(pk[8][2]), "a"(pk[8][3]), "a"(pk[8][4]), "a"(pk[8][5]), "a"(pk[8][6]), "a"(pk[8][7]), "a"(pk[8][8]), "a"(pk[8][9]), "a"(pk[8][10]), "a"(pk[8][11]), "a"(pk[8][12]), "v"(q), "s"(modd), "s"(mhi), "s"(0x0f0f0f0fu), "s"(0x80808080u), "s"(0x05010400u), "s"(0x07030602u)
    : "v0", "v1", "v2", "v3", "v4", "v5", "v6", "v7", "v8", "v9", "v10", "v11", "v12", "v13", "v14", "v15", "v16", "v17", "v18", "v19", "v20", "v21", "v22", "v23", "v24", "v25", "v26", "v27", "v28", "v29", "v30", "v31", "v32", "v33", "v34", "v35", "v36", "v37", "v38", "v39");
}
__device__ __forceinline__ void sv4(f32x4& acc0, f32x4& acc1, const u32 (&pk)[30][13], u32 pp, u32 mask0, u32 mask1) {
  asm volatile(
    "v_accvgpr_read_b32 v38, %14\n\t"
    "v_accvgpr_read_b32 v26, %4\n\t"
    "v_accvgpr_read_b32 v33, %7\n\t"
    "v_accvgpr_read_b32 v24, %2\n\t"
    "v_accvgpr_read_b32 v31, %5\n\t"
    "v_accvgpr_read_b32 v25, %3\n\t"
    "v_accvgpr_read_b32 v32, %6\n\t"
    "v_lshlrev_b32 v29, 7, v38\n\t"
    "v_lshlrev_b32 v36, 5, v38\n\t"
    "v_and_b32 v27, %18, v26\n\t"
    "v_and_b32 v34, %18, v33\n\t"
    "v_lshrrev_b32 v28, 4, v26\n\t"
    "v_lshrrev_b32 v35, 4, v33\n\t"
    "v_lshlrev_b32 v30, 6, v38\n\t"
    "v_lshlrev_b32 v37, 4, v38\n\t"
    "v_and_or_b32 v27, v29, %19, v27\n\t"
    "v_and_or_b32 v34, v36, %19, v34\n\t"
    "v_and_b32 v28, %18, v28\n\t"
    "v_and_b32 v35, %18, v35\n\t"
    "v_perm_b32 v16, v27, v24, %20\n\t"
    "v_perm_b32 v18, v34, v31, %20\n\t"
    "v_and_or_b32 v28, v30, %19, v28\n\t"
    "v_and_or_b32 v35, v37, %19, v35\n\t"
    "v_perm_b32 v17, v27, v24, %21\n\t"
    "v_perm_b32 v19, v34, v31, %21\n\t"
    "v_perm_b32 v20, v28, v25, %20\n\t"
    "v_perm_b32 v22, v35, v32, %20\n\t"
    "v_perm_b32 v21, v28, v25, %21\n\t"
    "v_perm_b32 v23, v35, v32, %21\n\t"
    "v_and_b32_dpp v0, %15, %16 quad_perm:[0,0,0,0] row_mask:0xf bank_mask:0xf\n\t"
    "v_and_b32_dpp v1, %15, %17 quad_perm:[0,0,0,0] row_mask:0xf bank_mask:0xf\n\t"
    "v_and_b32_dpp v2, %15, %16 quad_perm:[1,1,1,1] row_mask:0xf bank_mask:0xf\n\t"
    "v_and_b32_dpp v3, %15, %17 quad_perm:[1,1,1,1] row_mask:0xf bank_mask:0xf\n\t"
    "v_accvgpr_read_b32 v26, %10\n\t"
    "v_accvgpr_read_b32 v33, %13\n\t"
    "v_mfma_f32_16x16x32_bf16 %0, v[0:3], v[16:19], %0\n\t"
    "v_accvgpr_read_b32 v24, %8\n\t"
    "v_accvgpr_read_b32 v31, %11\n\t"
    "v_accvgpr_read_b32 v25, %9\n\t"
    "v_mfma_f32_16x16x32_bf16 %1, v[0:3], v[20:23], %1\n\t"
    "v_accvgpr_read_b32 v32, %12\n\t"
    "v_lshlrev_b32 v29, 3, v38\n\t"
    "v_lshlrev_b32 v36, 1, v38\n\t"
    "v_and_b32 v27, %18, v26\n\t"
    "v_and_b32 v34, %18, v33\n\t"
    "v_lshrrev_b32 v28, 4, v26\n\t"
    "v_lshrrev_b32 v35, 4, v33\n\t"
    "v_lshlrev_b32 v30, 2, v38\n\t"
    "v_and_or_b32 v34, v36, %19, v34\n\t"
    "v_and_or_b32 v27, v29, %19, v27\n\t"
    "v_and_b32 v35, %18, v35\n\t"
    "v_and_b32 v28, %18, v28\n\t"
    "v_perm_b32 v18, v34, v31, %20\n\t"
    "v_perm_b32 v16, v27, v24, %20\n\t"
    "v_and_or_b32 v35, v38, %19, v35\n\t"
    "v_and_or_b32 v28, v30, %19, v28\n\t"
    "v_perm_b32 v19, v34, v31, %21\n\t"
    "v_perm_b32 v17, v27, v24, %21\n\t"
    "v_perm_b32 v22, v35, v32, %20\n\t"
    "v_perm_b32 v20, v28, v25, %20\n\t"
    "v_perm_b32 v23, v35, v32, %21\n\t"
    "v_perm_b32 v21, v28, v25, %21\n\t"
    "v_and_b32_dpp v4, %15, %16 quad_perm:[2,2,2,2] row_mask:0xf bank_mask:0xf\n\t"
    "v_and_b32_dpp v5, %15, %17 quad_perm:[2,2,2,2] row_mask:0xf bank_mask:0xf\n\t"
    "v_and_b32_dpp v6, %15, %16 quad_perm:[3,3,3,3] row_mask:0xf bank_mask:0xf\n\t"
    "v_and_b32_dpp v7, %15, %17 quad_perm:[3,3,3,3] row_mask:0xf bank_mask:0xf\n\t"
    "s_nop 1\n\t"
    "v_mfma_f32_16x16x32_bf16 %0, v[4:7], v[16:19], %0\n\t"
    "v_mfma_f32_16x16x32_bf16 %1, v[4:7], v[20:23], %1\n\t"
    : "+v"(acc0), "+v"(acc1)
    : "a"(pk[9][0]), "a"(pk[9][1]), "a"(pk[9][2]), "a"(pk[9][3]), "a"(pk[9][4]), "a"(pk[9][5]), "a"(pk[9][6]), "a"(pk[9][7]), "a"(pk[9][8]), "a"(pk[9][9]), "a"(pk[9][10]), "a"(pk[9][11]), "a"(pk[9][12]), "v"(pp), "v"(mask0), "v"(mask1), "s"(0x0f0f0f0fu), "s"(0x80808080u), "s"(0x05010400u), "s"(0x07030602u)
    : "v0", "v1", "v2", "v3", "v4", "v5", "v6", "v7", "v8", "v9", "v10", "v11", "v12", "v13", "v14", "v15", "v16", "v17", "v18", "v19", "v20", "v21", "v22", "v23", "v24", "v25", "v26", "v27", "v28", "v29", "v30", "v31", "v32", "v33", "v34", "v35", "v36", "v37", "v38", "v39");
}
__device__ __forceinline__ void sk5(float& X, const u32 (&pk)[30][13], u32x4 q, u64 modd, u64 mhi) {
  asm volatile(
    "v_accvgpr_read_b32 v38, %13\n\t"
    "v_accvgpr_read_b32 v26, %3\n\t"
    "v_accvgpr_read_b32 v33, %6\n\t"
    "v_accvgpr_read_b32 v24, %1\n\t"
    "v_accvgpr_read_b32 v31, %4\n\t"
    "v_accvgpr_read_b32 v25, %2\n\t"
    "v_accvgpr_read_b32 v32, %5\n\t"
    "v_lshlrev_b32 v29, 7, v38\n\t"
    "v_lshlrev_b32 v36, 5, v38\n\t"
    "v_and_b32 v27, %17, v26\n\t"
    "v_and_b32 v34, %17, v33\n\t"
    "v_lshrrev_b32 v28, 4, v26\n\t"
    "v_lshrrev_b32 v35, 4, v33\n\t"
    "v_lshlrev_b32 v30, 6, v38\n\t"
    "v_lshlrev_b32 v37, 4, v38\n\t"
    "v_and_or_b32 v27, v29, %18, v27\n\t"
    "v_and_or_b32 v34, v36, %18, v34\n\t"
    "v_and_b32 v28, %17, v28\n\t"
    "v_and_b32 v35, %17, v35\n\t"
    "v_perm_b32 v16, v27, v24, %19\n\t"
    "v_perm_b32 v20, v34, v31, %19\n\t"
    "v_and_or_b32 v28, v30, %18, v28\n\t"
    "v_and_or_b32 v35, v37, %18, v35\n\t"
    "v_perm_b32 v17, v27, v24, %20\n\t"
    "v_perm_b32 v21, v34, v31, %20\n\t"
    "v_perm_b32 v18, v28, v25, %19\n\t"
    "v_perm_b32 v22, v35, v32, %19\n\t"
    "v_perm_b32 v19, v28, v25, %20\n\t"
    "v_perm_b32 v23, v35, v32, %20\n\t"
    "v_accvgpr_read_b32 v26, %9\n\t"
    "v_mfma_f32_16x16x32_bf16 v[0:3], v[16:19], %14, 0\n\t"
    "v_accvgpr_read_b32 v33, %12\n\t"
    "v_accvgpr_read_b32 v24, %7\n\t"
    "v_mfma_f32_16x16x32_bf16 v[4:7], v[20:23], %14, 0\n\t"
    "v_accvgpr_read_b32 v31, %10\n\t"
    "v_accvgpr_read_b32 v25, %8\n\t"
    "v_accvgpr_read_b32 v32, %11\n\t"
    "v_lshlrev_b32 v29, 3, v38\n\t"
    "v_lshlrev_b32 v36, 1, v38\n\t"
    "v_and_b32 v27, %17, v26\n\t"
    "v_and_b32 v34, %17, v33\n\t"
    "v_lshrrev_b32 v28, 4, v26\n\t"
    "v_lshrrev_b32 v35, 4, v33\n\t"
    "v_lshlrev_b32 v30, 2, v38\n\t"
    "v_and_or_b32 v34, v36, %18, v34\n\t"
    "v_and_or_b32 v27, v29, %18, v27\n\t"
    "v_and_b32 v35, %17, v35\n\t"
    "v_and_b32 v28, %17, v28\n\t"
    "v_perm_b32 v20, v34, v31, %19\n\t"
    "v_perm_b32 v16, v27, v24, %19\n\t"
    "v_and_or_b32 v35, v38, %18, v35\n\t"
    "v_and_or_b32 v28, v30, %18, v28\n\t"
    "v_perm_b32 v21, v34, v31, %20\n\t"
    "v_perm_b32 v17, v27, v24, %20\n\t"
    "v_perm_b32 v22, v35, v32, %19\n\t"
    "v_perm_b32 v18, v28, v25, %19\n\t"
    "v_perm_b32 v23, v35, v32, %20\n\t"
    "v_perm_b32 v19, v28, v25, %20\n\t"
    "v_add_f32_dpp v0, v1, v0 quad_perm:[1,1,1,1] row_mask:0xf bank_mask:0xf\n\t"
    "v_add_f32_dpp v5, v6, v5 quad_perm:[2,2,2,2] row_mask:0xf bank_mask:0xf\n\t"
    "v_mfma_f32_16x16x32_bf16 v[8:11], v[16:19], %14, 0\n\t"
    "v_add_f32_dpp v0, v2, v0 quad_perm:[2,2,2,2] row_mask:0xf bank_mask:0xf\n\t"
    "v_mfma_f32_16x16x32_bf16 v[12:15], v[20:23], %14, 0\n\t"
    "v_add_f32_dpp v5, v7, v5 quad_perm:[3,3,3,3] row_mask:0xf bank_mask:0xf\n\t"
    "v_add_f32_dpp v0, v3, v0 quad_perm:[3,3,3,3] row_mask:0xf bank_mask:0xf\n\t"
    "v_add_f32_dpp v5, v4, v5 quad_perm:[0,0,0,0] row_mask:0xf bank_mask:0xf\n\t"
    "v_cndmask_b32_e64 v0, v0, v5, %15\n\t"
    "s_nop 1\n\t"
    "v_add_f32_dpp v10, v11, v10 quad_perm:[3,3,3,3] row_mask:0xf bank_mask:0xf\n\t"
    "s_nop 0\n\t"
    "v_add_f32_dpp v15, v12, v15 quad_perm:[0,0,0,0] row_mask:0xf bank_mask:0xf\n\t"
    "v_add_f32_dpp v10, v8, v10 quad_perm:[0,0,0,0] row_mask:0xf bank_mask:0xf\n\t"
    "v_add_f32_dpp v15, v13, v15 quad_perm:[1,1,1,1] row_mask:0xf bank_mask:0xf\n\t"
    "v_add_f32_dpp v10, v9, v10 quad_perm:[1,1,1,1] row_mask:0xf bank_mask:0xf\n\t"
    "v_add_f32_dpp v15, v14, v15 quad_perm:[2,2,2,2] row_mask:0xf bank_mask:0xf\n\t"
    "v_cndmask_b32_e64 v10, v10, v15, %15\n\t"
    "v_cndmask_b32_e64 %0, v0, v10, %16\n\t"
    : "=&v"(X)
    : "a"(pk[10][0]), "a"(pk[10][1]), "a"(pk[10][2]), "a"(pk[10][3]), "a"(pk[10][4]), "a"(pk[10][5]), "a"(pk[10][6]), "a"(pk[10][7]), "a"(pk[10][8]), "a"(pk[10][9]), "a"(pk[10][10]), "a"(pk[10][11]), "a"(pk[10][12]), "v"(q), "s"(modd), "s"(mhi), "s"(0x0f0f0f0fu), "s"(0x80808080u), "s"(0x05010400u), "s"(0x07030602u)
    : "v0", "v1", "v2", "v3", "v4", "v5", "v6", "v7", "v8", "v9", "v10", "v11", "v12", "v13", "v14", "v15", "v16", "v17", "v18", "v19", "v20", "v21", "v22", "v23", "v24", "v25", "v26", "v27", "v28", "v29", "v30", "v31", "v32", "v33", "v34", "v35", "v36", "v37", "v38", "v39");
}
__device__ __forceinline__ void sv5(f32x4& acc0, f32x4& acc1, const u32 (&pk)[30][13], u32 pp, u32 mask0, u32 mask1) {
  asm volatile(
    "v_accvgpr_read_b32 v38, %14\n\t"
    "v_accvgpr_read_b32 v26, %4\n\t"
    "v_accvgpr_read_b32 v33, %7\n\t"
    "v_accvgpr_read_b32 v24, %2\n\t"
    "v_accvgpr_read_b32 v31, %5\n\t"
    "v_accvgpr_read_b32 v25, %3\n\t"
    "v_accvgpr_read_b32 v32, %6\n\t"
    "v_lshlrev_b32 v29, 7, v38\n\t"
    "v_lshlrev_b32 v36, 5, v38\n\t"
    "v_and_b32 v27, %18, v26\n\t"
    "v_and_b32 v34, %18, v33\n\t"
    "v_lshrrev_b32 v28, 4, v26\n\t"
    "v_lshrrev_b32 v35, 4, v33\n\t"
    "v_lshlrev_b32 v30, 6, v38\n\t"
    "v_lshlrev_b32 v37, 4, v38\n\t"
    "v_and_or_b32 v27, v29, %19, v27\n\t"
    "v_and_or_b32 v34, v36, %19, v34\n\t"
    "v_and_b32 v28, %18, v28\n\t"
    "v_and_b32 v35, %18, v35\n\t"
    "v_perm_b32 v16, v27, v24, %20\n\t"
    "v_perm_b32 v18, v34, v31, %20\n\t"
    "v_and_or_b32 v28, v30, %19, v28\n\t"
    "v_and_or_b32 v35, v37, %19, v35\n\t"
    "v_perm_b32 v17, v27, v24, %21\n\t"
    "v_perm_b32 v19, v34, v31, %21\n\t"
    "v_perm_b32 v20, v28, v25, %20\n\t"
    "v_perm_b32 v22, v35, v32, %20\n\t"
    "v_perm_b32 v21, v28, v25, %21\n\t"
    "v_perm_b32 v23, v35, v32, %21\n\t"
    "v_and_b32_dpp v0, %15, %16 quad_perm:[0,0,0,0] row_mask:0xf bank_mask:0xf\n\t"
    "v_and_b32_dpp v1, %15, %17 quad_perm:[0,0,0,0] row_mask:0xf bank_mask:0xf\n\t"
    "v_and_b32_dpp v2, %15, %16 quad_perm:[1,1,1,1] row_mask:0xf bank_mask:0xf\n\t"
    "v_and_b32_dpp v3, %15, %17 quad_perm:[1,1,1,1] row_mask:0xf bank_mask:0xf\n\t"
    "v_accvgpr_read_b32 v26, %10\n\t"
    "v_accvgpr_read_b32 v33, %13\n\t"
    "v_mfma_f32_16x16x32_bf16 %0, v[0:3], v[16:19], %0\n\t"
    "v_accvgpr_read_b32 v24, %8\n\t"
    "v_accvgpr_read_b32 v31, %11\n\t"
    "v_accvgpr_read_b32 v25, %9\n\t"
    "v_mfma_f32_16x16x32_bf16 %1, v[0:3], v[20:23], %1\n\t"
    "v_accvgpr_read_b32 v32, %12\n\t"
    "v_lshlrev_b32 v29, 3, v38\n\t"
    "v_lshlrev_b32 v36, 1, v38\n\t"
    "v_and_b32 v27, %18, v26\n\t"
    "v_and_b32 v34, %18, v33\n\t"
    "v_lshrrev_b32 v28, 4, v26\n\t"
    "v_lshrrev_b32 v35, 4, v33\n\t"
    "v_lshlrev_b32 v30, 2, v38\n\t"
    "v_and_or_b32 v34, v36, %19, v34\n\t"
    "v_and_or_b32 v27, v29, %19, v27\n\t"
    "v_and_b32 v35, %18, v35\n\t"
    "v_and_b32 v28, %18, v28\n\t"
    "v_perm_b32 v18, v34, v31, %20\n\t"
    "v_perm_b32 v16, v27, v24, %20\n\t"
    "v_and_or_b32 v35, v38, %19, v35\n\t"
    "v_and_or_b32 v28, v30, %19, v28\n\t"
    "v_perm_b32 v19, v34, v31, %21\n\t"
    "v_perm_b32 v17, v27, v24, %21\n\t"
    "v_perm_b32 v22, v35, v32, %20\n\t"
    "v_perm_b32 v20, v28, v25, %20\n\t"
    "v_perm_b32 v23, v35, v32, %21\n\t"
    "v_perm_b32 v21, v28, v25, %21\n\t"
    "v_and_b32_dpp v4, %15, %16 quad_perm:[2,2,2,2] row_mask:0xf bank_mask:0xf\n\t"
    "v_and_b32_dpp v5, %15, %17 quad_perm:[2,2,2,2] row_mask:0xf bank_mask:0xf\n\t"
    "v_and_b32_dpp v6, %15, %16 quad_perm:[3,3,3,3] row_mask:0xf bank_mask:0xf\n\t"
    "v_and_b32_dpp v7, %15, %17 quad_perm:[3,3,3,3] row_mask:0xf bank_mask:0xf\n\t"
    "s_nop 1\n\t"
    "v_mfma_f32_16x16x32_bf16 %0, v[4:7], v[16:19], %0\n\t"
    "v_mfma_f32_16x16x32_bf16 %1, v[4:7], v[20:23], %1\n\t"
    : "+v"(acc0), "+v"(acc1)
    : "a"(pk[11][0]), "a"(pk[11][1]), "a"(pk[11][2]), "a"(pk[11][3]), "a"(pk[11][4]), "a"(pk[11][5]), "a"(pk[11][6]), "a"(pk[11][7]), "a"(pk[11][8]), "a"(pk[11][9]), "a"(pk[11][10]), "a"(pk[11][11]), "a"(pk[11][12]), "v"(pp), "v"(mask0), "v"(mask1), "s"(0x0f0f0f0fu), "s"(0x80808080u), "s"(0x05010400u), "s"(0x07030602u)
    : "v0", "v1", "v2", "v3", "v4", "v5", "v6", "v7", "v8", "v9", "v10", "v11", "v12", "v13", "v14", "v15", "v16", "v17", "v18", "v19", "v20", "v21", "v22", "v23", "v24", "v25", "v26", "v27", "v28", "v29", "v30", "v31", "v32", "v33", "v34", "v35", "v36", "v37", "v38", "v39");
}
__device__ __forceinline__ void sk6(float& X, const u32 (&pk)[30][13], u32x4 q, u64 modd, u64 mhi) {
  asm volatile(
    "v_accvgpr_read_b32 v38, %13\n\t"
    "v_accvgpr_read_b32 v26, %3\n\t"
    "v_accvgpr_read_b32 v33, %6\n\t"
    "v_accvgpr_read_b32 v24, %1\n\t"
    "v_accvgpr_read_b32 v31, %4\n\t"
    "v_accvgpr_read_b32 v25, %2\n\t"
    "v_accvgpr_read_b32 v32, %5\n\t"
    "v_lshlrev_b32 v29, 7, v38\n\t"
    "v_lshlrev_b32 v36, 5, v38\n\t"
    "v_and_b32 v27, %17, v26\n\t"
    "v_and_b32 v34, %17, v33\n\t"
    "v_lshrrev_b32 v28, 4, v26\n\t"
    "v_lshrrev_b32 v35, 4, v33\n\t"
    "v_lshlrev_b32 v30, 6, v38\n\t"
    "v_lshlrev_b32 v37, 4, v38\n\t"
    "v_and_or_b32 v27, v29, %18, v27\n\t"
    "v_and_or_b32 v34, v36, %18, v34\n\t"
    "v_and_b32 v28, %17, v28\n\t"
    "v_and_b32 v35, %17, v35\n\t"
    "v_perm_b32 v16, v27, v24, %19\n\t"
    "v_perm_b32 v20, v34, v31, %19\n\t"
    "v_and_or_b32 v28, v30, %18, v28\n\t"
    "v_and_or_b32 v35, v37, %18, v35\n\t"
    "v_perm_b32 v17, v27, v24, %20\n\t"
    "v_perm_b32 v21, v34, v31, %20\n\t"
    "v_perm_b32 v18, v28, v25, %19\n\t"
    "v_perm_b32 v22, v35, v32, %19\n\t"
    "v_perm_b32 v19, v28, v25, %20\n\t"
    "v_perm_b32 v23, v35, v32, %20\n\t"
    "v_accvgpr_read_b32 v26, %9\n\t"
    "v_mfma_f32_16x16x32_bf16 v[0:3], v[16:19], %14, 0\n\t"
    "v_accvgpr_read_b32 v33, %12\n\t"
    "v_accvgpr_read_b32 v24, %7\n\t"
    "v_mfma_f32_16x16x32_bf16 v[4:7], v[20:23], %14, 0\n\t"
    "v_accvgpr_read_b32 v31, %10\n\t"
    "v_accvgpr_read_b32 v25, %8\n\t"
    "v_accvgpr_read_b32 v32, %11\n\t"
    "v_lshlrev_b32 v29, 3, v38\n\t"
    "v_lshlrev_b32 v36, 1, v38\n\t"
    "v_and_b32 v27, %17, v26\n\t"
    "v_and_b32 v34, %17, v33\n\t"
    "v_lshrrev_b32 v28, 4, v26\n\t"
    "v_lshrrev_b32 v35, 4, v33\n\t"
    "v_lshlrev_b32 v30, 2, v38\n\t"
    "v_and_or_b32 v34, v36, %18, v34\n\t"
    "v_and_or_b32 v27, v29, %18, v27\n\t"
    "v_and_b32 v35, %17, v35\n\t"
    "v_and_b32 v28, %17, v28\n\t"
    "v_perm_b32 v20, v34, v31, %19\n\t"
    "v_perm_b32 v16, v27, v24, %19\n\t"
    "v_and_or_b32 v35, v38, %18, v35\n\t"
    "v_and_or_b32 v28, v30, %18, v28\n\t"
    "v_perm_b32 v21, v34, v31, %20\n\t"
    "v_perm_b32 v17, v27, v24, %20\n\t"
    "v_perm_b32 v22, v35, v32, %19\n\t"
    "v_perm_b32 v18, v28, v25, %19\n\t"
    "v_perm_b32 v23, v35, v32, %20\n\t"
    "v_perm_b32 v19, v28, v25, %20\n\t"
    "v_add_f32_dpp v0, v1, v0 quad_perm:[1,1,1,1] row_mask:0xf bank_mask:0xf\n\t"
    "v_add_f32_dpp v5, v6, v5 quad_perm:[2,2,2,2] row_mask:0xf bank_mask:0xf\n\t"
    "v_mfma_f32_16x16x32_bf16 v[8:11], v[16:19], %14, 0\n\t"
    "v_add_f32_dpp v0, v2, v0 quad_perm:[2,2,2,2] row_mask:0xf bank_mask:0xf\n\t"
    "v_mfma_f32_16x16x32_bf16 v[12:15], v[20:23], %14, 0\n\t"
    "v_add_f32_dpp v5, v7, v5 quad_perm:[3,3,3,3] row_mask:0xf bank_mask:0xf\n\t"
    "v_add_f32_dpp v0, v3, v0 quad_perm:[3,3,3,3] row_mask:0xf bank_mask:0xf\n\t"
    "v_add_f32_dpp v5, v4, v5 quad_perm:[0,0,0,0] row_mask:0xf bank_mask:0xf\n\t"
    "v_cndmask_b32_e64 v0, v0, v5, %15\n\t"
    "s_nop 1\n\t"
    "v_add_f32_dpp v10, v11, v10 quad_perm:[3,3,3,3] row_mask:0xf bank_mask:0xf\n\t"
    "s_nop 0\n\t"
    "v_add_f32_dpp v15, v12, v15 quad_perm:[0,0,0,0] row_mask:0xf bank_mask:0xf\n\t"
    "v_add_f32_dpp v10, v8, v10 quad_perm:[0,0,0,0] row_mask:0xf bank_mask:0xf\n\t"
    "v_add_f32_dpp v15, v13, v15 quad_perm:[1,1,1,1] row_mask:0xf bank_mask:0xf\n\t"
    "v_add_f32_dpp v10, v9, v10 quad_perm:[1,1,1,1] row_mask:0xf bank_mask:0xf\n\t"
    "v_add_f32_dpp v15, v14, v15 quad_perm:[2,2,2,2] row_mask:0xf bank_mask:0xf\n\t"
    "v_cndmask_b32_e64 v10, v10, v15, %15\n\t"
    "v_cndmask_b32_e64 %0, v0, v10, %16\n\t"
    : "=&v"(X)
    : "a"(pk[12][0]), "a"(pk[12][1]), "a"(pk[12][2]), "a"(pk[12][3]), "a"(pk[12][4]), "a"(pk[12][5]), "a"(pk[12][6]), "a"(pk[12][7]), "a"(pk[12][8]), "a"(pk[12][9]), "a"(pk[12][10]), "a"(pk[12][11]), "a"(pk[12][12]), "v"(q), "s"(modd), "s"(mhi), "s"(0x0f0f0f0fu), "s"(0x80808080u), "s"(0x05010400u), "s"(0x07030602u)
    : "v0", "v1", "v2", "v3", "v4", "v5", "v6", "v7", "v8", "v9", "v10", "v11", "v12", "v13", "v14", "v15", "v16", "v17", "v18", "v19", "v20", "v21", "v22", "v23", "v24", "v25", "v26", "v27", "v28", "v29", "v30", "v31", "v32", "v33", "v34", "v35", "v36", "v37", "v38", "v39");
}
__device__ __forceinline__ void sv6(f32x4& acc0, f32x4& acc1, const u32 (&pk)[30][13], u32 pp, u32 mask0, u32 mask1) {
  asm volatile(
    "v_accvgpr_read_b32 v38, %14\n\t"
    "v_accvgpr_read_b32 v26, %4\n\t"
    "v_accvgpr_read_b32 v33, %7\n\t"
    "v_accvgpr_read_b32 v24, %2\n\t"
    "v_accvgpr_read_b32 v31, %5\n\t"
    "v_accvgpr_read_b32 v25, %3\n\t"
    "v_accvgpr_read_b32 v32, %6\n\t"
    "v_lshlrev_b32 v29, 7, v38\n\t"
    "v_lshlrev_b32 v36, 5, v38\n\t"
    "v_and_b32 v27, %18, v26\n\t"
    "v_and_b32 v34, %18, v33\n\t"
    "v_lshrrev_b32 v28, 4, v26\n\t"
    "v_lshrrev_b32 v35, 4, v33\n\t"
    "v_lshlrev_b32 v30, 6, v38\n\t"
    "v_lshlrev_b32 v37, 4, v38\n\t"
    "v_and_or_b32 v27, v29, %19, v27\n\t"
    "v_and_or_b32 v34, v36, %19, v34\n\t"
    "v_and_b32 v28, %18, v28\n\t"
    "v_and_b32 v35, %18, v35\n\t"
    "v_perm_b32 v16, v27, v24, %20\n\t"
    "v_perm_b32 v18, v34, v31, %20\n\t"
    "v_and_or_b32 v28, v30, %19, v28\n\t"
    "v_and_or_b32 v35, v37, %19, v35\n\t"
    "v_perm_b32 v17, v27, v24, %21\n\t"
    "v_perm_b32 v19, v34, v31, %21\n\t"
    "v_perm_b32 v20, v28, v25, %20\n\t"
    "v_perm_b32 v22, v35, v32, %20\n\t"
    "v_perm_b32 v21, v28, v25, %21\n\t"
    "v_perm_b32 v23, v35, v32, %21\n\t"
    "v_and_b32_dpp v0, %15, %16 quad_perm:[0,0,0,0] row_mask:0xf bank_mask:0xf\n\t"
    "v_and_b32_dpp v1, %15, %17 quad_perm:[0,0,0,0] row_mask:0xf bank_mask:0xf\n\t"
    "v_and_b32_dpp v2, %15, %16 quad_perm:[1,1,1,1] row_mask:0xf bank_mask:0xf\n\t"
    "v_and_b32_dpp v3, %15, %17 quad_perm:[1,1,1,1] row_mask:0xf bank_mask:0xf\n\t"
    "v_accvgpr_read_b32 v26, %10\n\t"
    "v_accvgpr_read_b32 v33, %13\n\t"
    "v_mfma_f32_16x16x32_bf16 %0, v[0:3], v[16:19], %0\n\t"
    "v_accvgpr_read_b32 v24, %8\n\t"
    "v_accvgpr_read_b32 v31, %11\n\t"
    "v_accvgpr_read_b32 v25, %9\n\t"
    "v_mfma_f32_16x16x32_bf16 %1, v[0:3], v[20:23], %1\n\t"
    "v_accvgpr_read_b32 v32, %12\n\t"
    "v_lshlrev_b32 v29, 3, v38\n\t"
    "v_lshlrev_b32 v36, 1, v38\n\t"
    "v_and_b32 v27, %18, v26\n\t"
    "v_and_b32 v34, %18, v33\n\t"
    "v_lshrrev_b32 v28, 4, v26\n\t"
    "v_lshrrev_b32 v35, 4, v33\n\t"
    "v_lshlrev_b32 v30, 2, v38\n\t"
    "v_and_or_b32 v34, v36, %19, v34\n\t"
    "v_and_or_b32 v27, v29, %19, v27\n\t"
    "v_and_b32 v35, %18, v35\n\t"
    "v_and_b32 v28, %18, v28\n\t"
    "v_perm_b32 v18, v34, v31, %20\n\t"
    "v_perm_b32 v16, v27, v24, %20\n\t"
    "v_and_or_b32 v35, v38, %19, v35\n\t"
    "v_and_or_b32 v28, v30, %19, v28\n\t"
    "v_perm_b32 v19, v34, v31, %21\n\t"
    "v_perm_b32 v17, v27, v24, %21\n\t"
    "v_perm_b32 v22, v35, v32, %20\n\t"
    "v_perm_b32 v20, v28, v25, %20\n\t"
    "v_perm_b32 v23, v35, v32, %21\n\t"
    "v_perm_b32 v21, v28, v25, %21\n\t"
    "v_and_b32_dpp v4, %15, %16 quad_perm:[2,2,2,2] row_mask:0xf bank_mask:0xf\n\t"
    "v_and_b32_dpp v5, %15, %17 quad_perm:[2,2,2,2] row_mask:0xf bank_mask:0xf\n\t"
    "v_and_b32_dpp v6, %15, %16 quad_perm:[3,3,3,3] row_mask:0xf bank_mask:0xf\n\t"
    "v_and_b32_dpp v7, %15, %17 quad_perm:[3,3,3,3] row_mask:0xf bank_mask:0xf\n\t"
    "s_nop 1\n\t"
    "v_mfma_f32_16x16x32_bf16 %0, v[4:7], v[16:19], %0\n\t"
    "v_mfma_f32_16x16x32_bf16 %1, v[4:7], v[20:23], %1\n\t"
    : "+v"(acc0), "+v"(acc1)
    : "a"(pk[13][0]), "a"(pk[13][1]), "a"(pk[13][2]), "a"(pk[13][3]), "a"(pk[13][4]), "a"(pk[13][5]), "a"(pk[13][6]), "a"(pk[13][7]), "a"(pk[13][8]), "a"(pk[13][9]), "a"(pk[13][10]), "a"(pk[13][11]), "a"(pk[13][12]), "v"(pp), "v"(mask0), "v"(mask1), "s"(0x0f0f0f0fu), "s"(0x80808080u), "s"(0x05010400u), "s"(0x07030602u)
    : "v0", "v1", "v2", "v3", "v4", "v5", "v6", "v7", "v8", "v9", "v10", "v11", "v12", "v13", "v14", "v15", "v16", "v17", "v18", "v19", "v20", "v21", "v22", "v23", "v24", "v25", "v26", "v27", "v28", "v29", "v30", "v31", "v32", "v33", "v34", "v35", "v36", "v37", "v38", "v39");
}
__device__ __forceinline__ void sk7(float& X, const u32 (&pk)[30][13], u32x4 q, u64 modd, u64 mhi) {
  asm volatile(
    "v_accvgpr_read_b32 v38, %13\n\t"
    "v_accvgpr_read_b32 v26, %3\n\t"
    "v_accvgpr_read_b32 v33, %6\n\t"
    "v_accvgpr_read_b32 v24, %1\n\t"
    "v_accvgpr_read_b32 v31, %4\n\t"
    "v_accvgpr_read_b32 v25, %2\n\t"
    "v_accvgpr_read_b32 v32, %5\n\t"
    "v_lshlrev_b32 v29, 7, v38\n\t"
    "v_lshlrev_b32 v36, 5, v38\n\t"
    "v_and_b32 v27, %17, v26\n\t"
    "v_and_b32 v34, %17, v33\n\t"
    "v_lshrrev_b32 v28, 4, v26\n\t"
    "v_lshrrev_b32 v35, 4, v33\n\t"
    "v_lshlrev_b32 v30, 6, v38\n\t"
    "v_lshlrev_b32 v37, 4, v38\n\t"
    "v_and_or_b32 v27, v29, %18, v27\n\t"
    "v_and_or_b32 v34, v36, %18, v34\n\t"
    "v_and_b32 v28, %17, v28\n\t"
    "v_and_b32 v35, %17, v35\n\t"
    "v_perm_b32 v16, v27, v24, %19\n\t"
    "v_perm_b32 v20, v34, v31, %19\n\t"
    "v_and_or_b32 v28, v30, %18, v28\n\t"
    "v_and_or_b32 v35, v37, %18, v35\n\t"
    "v_perm_b32 v17, v27, v24, %20\n\t"
    "v_perm_b32 v21, v34, v31, %20\n\t"
    "v_perm_b32 v18, v28, v25, %19\n\t"
    "v_perm_b32 v22, v35, v32, %19\n\t"
    "v_perm_b32 v19, v28, v25, %20\n\t"
    "v_perm_b32 v23, v35, v32, %20\n\t"
    "v_accvgpr_read_b32 v26, %9\n\t"
    "v_mfma_f32_16x16x32_bf16 v[0:3], v[16:19], %14, 0\n\t"
    "v_accvgpr_read_b32 v33, %12\n\t"
    "v_accvgpr_read_b32 v24, %7\n\t"
    "v_mfma_f32_16x16x32_bf16 v[4:7], v[20:23], %14, 0\n\t"
    "v_accvgpr_read_b32 v31, %10\n\t"
    "v_accvgpr_read_b32 v25, %8\n\t"
    "v_accvgpr_read_b32 v32, %11\n\t"
    "v_lshlrev_b32 v29, 3, v38\n\t"
    "v_lshlrev_b32 v36, 1, v38\n\t"
    "v_and_b32 v27, %17, v26\n\t"
    "v_and_b32 v34, %17, v33\n\t"
    "v_lshrrev_b32 v28, 4, v26\n\t"
    "v_lshrrev_b32 v35, 4, v33\n\t"
    "v_lshlrev_b32 v30, 2, v38\n\t"
    "v_and_or_b32 v34, v36, %18, v34\n\t"
    "v_and_or_b32 v27, v29, %18, v27\n\t"
    "v_and_b32 v35, %17, v35\n\t"
    "v_and_b32 v28, %17, v28\n\t"
    "v_perm_b32 v20, v34, v31, %19\n\t"
    "v_perm_b32 v16, v27, v24, %19\n\t"
    "v_and_or_b32 v35, v38, %18, v35\n\t"
    "v_and_or_b32 v28, v30, %18, v28\n\t"
    "v_perm_b32 v21, v34, v31, %20\n\t"
    "v_perm_b32 v17, v27, v24, %20\n\t"
    "v_perm_b32 v22, v35, v32, %19\n\t"
    "v_perm_b32 v18, v28, v25, %19\n\t"
    "v_perm_b32 v23, v35, v32, %20\n\t"
    "v_perm_b32 v19, v28, v25, %20\n\t"
    "v_add_f32_dpp v0, v1, v0 quad_perm:[1,1,1,1] row_mask:0xf bank_mask:0xf\n\t"
    "v_add_f32_dpp v5, v6, v5 quad_perm:[2,2,2,2] row_mask:0xf bank_mask:0xf\n\t"
    "v_mfma_f32_16x16x32_bf16 v[8:11], v[16:19], %14, 0\n\t"
    "v_add_f32_dpp v0, v2, v0 quad_perm:[2,2,2,2] row_mask:0xf bank_mask:0xf\n\t"
    "v_mfma_f32_16x16x32_bf16 v[12:15], v[20:23], %14, 0\n\t"
    "v_add_f32_dpp v5, v7, v5 quad_perm:[3,3,3,3] row_mask:0xf bank_mask:0xf\n\t"
    "v_add_f32_dpp v0, v3, v0 quad_perm:[3,3,3,3] row_mask:0xf bank_mask:0xf\n\t"
    "v_add_f32_dpp v5, v4, v5 quad_perm:[0,0,0,0] row_mask:0xf bank_mask:0xf\n\t"
    "v_cndmask_b32_e64 v0, v0, v5, %15\n\t"
    "s_nop 1\n\t"
    "v_add_f32_dpp v10, v11, v10 quad_perm:[3,3,3,3] row_mask:0xf bank_mask:0xf\n\t"
    "s_nop 0\n\t"
    "v_add_f32_dpp v15, v12, v15 quad_perm:[0,0,0,0] row_mask:0xf bank_mask:0xf\n\t"
    "v_add_f32_dpp v10, v8, v10 quad_perm:[0,0,0,0] row_mask:0xf bank_mask:0xf\n\t"
    "v_add_f32_dpp v15, v13, v15 quad_perm:[1,1,1,1] row_mask:0xf bank_mask:0xf\n\t"
    "v_add_f32_dpp v10, v9, v10 quad_perm:[1,1,1,1] row_mask:0xf bank_mask:0xf\n\t"
    "v_add_f32_dpp v15, v14, v15 quad_perm:[2,2,2,2] row_mask:0xf bank_mask:0xf\n\t"
    "v_cndmask_b32_e64 v10, v10, v15, %15\n\t"
    "v_cndmask_b32_e64 %0, v0, v10, %16\n\t"
    : "=&v"(X)
    : "a"(pk[14][0]), "a"(pk[14][1]), "a"(pk[14][2]), "a"(pk[14][3]), "a"(pk[14][4]), "a"(pk[14][5]), "a"(pk[14][6]), "a"(pk[14][7]), "a"(pk[14][8]), "a"(pk[14][9]), "a"(pk[14][10]), "a"(pk[14][11]), "a"(pk[14][12]), "v"(q), "s"(modd), "s"(mhi), "s"(0x0f0f0f0fu), "s"(0x80808080u), "s"(0x05010400u), "s"(0x07030602u)
    : "v0", "v1", "v2", "v3", "v4", "v5", "v6", "v7", "v8", "v9", "v10", "v11", "v12", "v13", "v14", "v15", "v16", "v17", "v18", "v19", "v20", "v21", "v22", "v23", "v24", "v25", "v26", "v27", "v28", "v29", "v30", "v31", "v32", "v33", "v34", "v35", "v36", "v37", "v38", "v39");
}
__device__ __forceinline__ void sv7(f32x4& acc0, f32x4& acc1, const u32 (&pk)[30][13], u32 pp, u32 mask0, u32 mask1) {
  asm volatile(
    "v_accvgpr_read_b32 v38, %14\n\t"
    "v_accvgpr_read_b32 v26, %4\n\t"
    "v_accvgpr_read_b32 v33, %7\n\t"
    "v_accvgpr_read_b32 v24, %2\n\t"
    "v_accvgpr_read_b32 v31, %5\n\t"
    "v_accvgpr_read_b32 v25, %3\n\t"
    "v_accvgpr_read_b32 v32, %6\n\t"
    "v_lshlrev_b32 v29, 7, v38\n\t"
    "v_lshlrev_b32 v36, 5, v38\n\t"
    "v_and_b32 v27, %18, v26\n\t"
    "v_and_b32 v34, %18, v33\n\t"
    "v_lshrrev_b32 v28, 4, v26\n\t"
    "v_lshrrev_b32 v35, 4, v33\n\t"
    "v_lshlrev_b32 v30, 6, v38\n\t"
    "v_lshlrev_b32 v37, 4, v38\n\t"
    "v_and_or_b32 v27, v29, %19, v27\n\t"
    "v_and_or_b32 v34, v36, %19, v34\n\t"
    "v_and_b32 v28, %18, v28\n\t"
    "v_and_b32 v35, %18, v35\n\t"
    "v_perm_b32 v16, v27, v24, %20\n\t"
    "v_perm_b32 v18, v34, v31, %20\n\t"
    "v_and_or_b32 v28, v30, %19, v28\n\t"
    "v_and_or_b32 v35, v37, %19, v35\n\t"
    "v_perm_b32 v17, v27, v24, %21\n\t"
    "v_perm_b32 v19, v34, v31, %21\n\t"
    "v_perm_b32 v20, v28, v25, %20\n\t"
    "v_perm_b32 v22, v35, v32, %20\n\t"
    "v_perm_b32 v21, v28, v25, %21\n\t"
    "v_perm_b32 v23, v35, v32, %21\n\t"
    "v_and_b32_dpp v0, %15, %16 quad_perm:[0,0,0,0] row_mask:0xf bank_mask:0xf\n\t"
    "v_and_b32_dpp v1, %15, %17 quad_perm:[0,0,0,0] row_mask:0xf bank_mask:0xf\n\t"
    "v_and_b32_dpp v2, %15, %16 quad_perm:[1,1,1,1] row_mask:0xf bank_mask:0xf\n\t"
    "v_and_b32_dpp v3, %15, %17 quad_perm:[1,1,1,1] row_mask:0xf bank_mask:0xf\n\t"
    "v_accvgpr_read_b32 v26, %10\n\t"
    "v_accvgpr_read_b32 v33, %13\n\t"
    "v_mfma_f32_16x16x32_bf16 %0, v[0:3], v[16:19], %0\n\t"
    "v_accvgpr_read_b32 v24, %8\n\t"
    "v_accvgpr_read_b32 v31, %11\n\t"
    "v_accvgpr_read_b32 v25, %9\n\t"
    "v_mfma_f32_16x16x32_bf16 %1, v[0:3], v[20:23], %1\n\t"
    "v_accvgpr_read_b32 v32, %12\n\t"
    "v_lshlrev_b32 v29, 3, v38\n\t"
    "v_lshlrev_b32 v36, 1, v38\n\t"
    "v_and_b32 v27, %18, v26\n\t"
    "v_and_b32 v34, %18, v33\n\t"
    "v_lshrrev_b32 v28, 4, v26\n\t"
    "v_lshrrev_b32 v35, 4, v33\n\t"
    "v_lshlrev_b32 v30, 2, v38\n\t"
    "v_and_or_b32 v34, v36, %19, v34\n\t"
    "v_and_or_b32 v27, v29, %19, v27\n\t"
    "v_and_b32 v35, %18, v35\n\t"
    "v_and_b32 v28, %18, v28\n\t"
    "v_perm_b32 v18, v34, v31, %20\n\t"
    "v_perm_b32 v16, v27, v24, %20\n\t"
    "v_and_or_b32 v35, v38, %19, v35\n\t"
    "v_and_or_b32 v28, v30, %19, v28\n\t"
    "v_perm_b32 v19, v34, v31, %21\n\t"
    "v_perm_b32 v17, v27, v24, %21\n\t"
    "v_perm_b32 v22, v35, v32, %20\n\t"
    "v_perm_b32 v20, v28, v25, %20\n\t"
    "v_perm_b32 v23, v35, v32, %21\n\t"
    "v_perm_b32 v21, v28, v25, %21\n\t"
    "v_and_b32_dpp v4, %15, %16 quad_perm:[2,2,2,2] row_mask:0xf bank_mask:0xf\n\t"
    "v_and_b32_dpp v5, %15, %17 quad_perm:[2,2,2,2] row_mask:0xf bank_mask:0xf\n\t"
    "v_and_b32_dpp v6, %15, %16 quad_perm:[3,3,3,3] row_mask:0xf bank_mask:0xf\n\t"
    "v_and_b32_dpp v7, %15, %17 quad_perm:[3,3,3,3] row_mask:0xf bank_mask:0xf\n\t"
    "s_nop 1\n\t"
    "v_mfma_f32_16x16x32_bf16 %0, v[4:7], v[16:19], %0\n\t"
    "v_mfma_f32_16x16x32_bf16 %1, v[4:7], v[20:23], %1\n\t"
    : "+v"(acc0), "+v"(acc1)
    : "a"(pk[15][0]), "a"(pk[15][1]), "a"(pk[15][2]), "a"(pk[15][3]), "a"(pk[15][4]), "a"(pk[15][5]), "a"(pk[15][6]), "a"(pk[15][7]), "a"(pk[15][8]), "a"(pk[15][9]), "a"(pk[15][10]), "a"(pk[15][11]), "a"(pk[15][12]), "v"(pp), "v"(mask0), "v"(mask1), "s"(0x0f0f0f0fu), "s"(0x80808080u), "s"(0x05010400u), "s"(0x07030602u)
    : "v0", "v1", "v2", "v3", "v4", "v5", "v6", "v7", "v8", "v9", "v10", "v11", "v12", "v13", "v14", "v15", "v16", "v17", "v18", "v19", "v20", "v21", "v22", "v23", "v24", "v25", "v26", "v27", "v28", "v29", "v30", "v31", "v32", "v33", "v34", "v35", "v36", "v37", "v38", "v39");
}
__device__ __forceinline__ void sk8(float& X, const u32 (&pk)[30][13], u32x4 q, u64 modd, u64 mhi) {
  asm volatile(
    "v_accvgpr_read_b32 v38, %13\n\t"
    "v_accvgpr_read_b32 v26, %3\n\t"
    "v_accvgpr_read_b32 v33, %6\n\t"
    "v_accvgpr_read_b32 v24, %1\n\t"
    "v_accvgpr_read_b32 v31, %4\n\t"
    "v_accvgpr_read_b32 v25, %2\n\t"
    "v_accvgpr_read_b32 v32, %5\n\t"
    "v_lshlrev_b32 v29, 7, v38\n\t"
    "v_lshlrev_b32 v36, 5, v38\n\t"
    "v_and_b32 v27, %17, v26\n\t"
    "v_and_b32 v34, %17, v33\n\t"
    "v_lshrrev_b32 v28, 4, v26\n\t"
    "v_lshrrev_b32 v35, 4, v33\n\t"
    "v_lshlrev_b32 v30, 6, v38\n\t"
    "v_lshlrev_b32 v37, 4, v38\n\t"
    "v_and_or_b32 v27, v29, %18, v27\n\t"
    "v_and_or_b32 v34, v36, %18, v34\n\t"
    "v_and_b32 v28, %17, v28\n\t"
    "v_and_b32 v35, %17, v35\n\t"
    "v_perm_b32 v16, v27, v24, %19\n\t"
    "v_perm_b32 v20, v34, v31, %19\n\t"
    "v_and_or_b32 v28, v30, %18, v28\n\t"
    "v_and_or_b32 v35, v37, %18, v35\n\t"
    "v_perm_b32 v17, v27, v24, %20\n\t"
    "v_perm_b32 v21, v34, v31, %20\n\t"
    "v_perm_b32 v18, v28, v25, %19\n\t"
    "v_perm_b32 v22, v35, v32, %19\n\t"
    "v_perm_b32 v19, v28, v25, %20\n\t"
    "v_perm_b32 v23, v35, v32, %20\n\t"
    "v_accvgpr_read_b32 v26, %9\n\t"
    "v_mfma_f32_16x16x32_bf16 v[0:3], v[16:19], %14, 0\n\t"
    "v_accvgpr_read_b32 v33, %12\n\t"
    "v_accvgpr_read_b32 v24, %7\n\t"
    "v_mfma_f32_16x16x32_bf16 v[4:7], v[20:23], %14, 0\n\t"
    "v_accvgpr_read_b32 v31, %10\n\t"
    "v_accvgpr_read_b32 v25, %8\n\t"
    "v_accvgpr_read_b32 v32, %11\n\t"
    "v_lshlrev_b32 v29, 3, v38\n\t"
    "v_lshlrev_b32 v36, 1, v38\n\t"
    "v_and_b32 v27, %17, v26\n\t"
    "v_and_b32 v34, %17, v33\n\t"
    "v_lshrrev_b32 v28, 4, v26\n\t"
    "v_lshrrev_b32 v35, 4, v33\n\t"
    "v_lshlrev_b32 v30, 2, v38\n\t"
    "v_and_or_b32 v34, v36, %18, v34\n\t"
    "v_and_or_b32 v27, v29, %18, v27\n\t"
    "v_and_b32 v35, %17, v35\n\t"
    "v_and_b32 v28, %17, v28\n\t"
    "v_perm_b32 v20, v34, v31, %19\n\t"
    "v_perm_b32 v16, v27, v24, %19\n\t"
    "v_and_or_b32 v35, v38, %18, v35\n\t"
    "v_and_or_b32 v28, v30, %18, v28\n\t"
    "v_perm_b32 v21, v34, v31, %20\n\t"
    "v_perm_b32 v17, v27, v24, %20\n\t"
    "v_perm_b32 v22, v35, v32, %19\n\t"
    "v_perm_b32 v18, v28, v25, %19\n\t"
    "v_perm_b32 v23, v35, v32, %20\n\t"
    "v_perm_b32 v19, v28, v25, %20\n\t"
    "v_add_f32_dpp v0, v1, v0 quad_perm:[1,1,1,1] row_mask:0xf bank_mask:0xf\n\t"
    "v_add_f32_dpp v5, v6, v5 quad_perm:[2,2,2,2] row_mask:0xf bank_mask:0xf\n\t"
    "v_mfma_f32_16x16x32_bf16 v[8:11], v[16:19], %14, 0\n\t"
    "v_add_f32_dpp v0, v2, v0 quad_perm:[2,2,2,2] row_mask:0xf bank_mask:0xf\n\t"
    "v_mfma_f32_16x16x32_bf16 v[12:15], v[20:23], %14, 0\n\t"
    "v_add_f32_dpp v5, v7, v5 quad_perm:[3,3,3,3] row_mask:0xf bank_mask:0xf\n\t"
    "v_add_f32_dpp v0, v3, v0 quad_perm:[3,3,3,3] row_mask:0xf bank_mask:0xf\n\t"
    "v_add_f32_dpp v5, v4, v5 quad_perm:[0,0,0,0] row_mask:0xf bank_mask:0xf\n\t"
    "v_cndmask_b32_e64 v0, v0, v5, %15\n\t"
    "s_nop 1\n\t"
    "v_add_f32_dpp v10, v11, v10 quad_perm:[3,3,3,3] row_mask:0xf bank_mask:0xf\n\t"
    "s_nop 0\n\t"
    "v_add_f32_dpp v15, v12, v15 quad_perm:[0,0,0,0] row_mask:0xf bank_mask:0xf\n\t"
    "v_add_f32_dpp v10, v8, v10 quad_perm:[0,0,0,0] row_mask:0xf bank_mask:0xf\n\t"
    "v_add_f32_dpp v15, v13, v15 quad_perm:[1,1,1,1] row_mask:0xf bank_mask:0xf\n\t"
    "v_add_f32_dpp v10, v9, v10 quad_perm:[1,1,1,1] row_mask:0xf bank_mask:0xf\n\t"
    "v_add_f32_dpp v15, v14, v15 quad_perm:[2,2,2,2] row_mask:0xf bank_mask:0xf\n\t"
    "v_cndmask_b32_e64 v10, v10, v15, %15\n\t"
    "v_cndmask_b32_e64 %0, v0, v10, %16\n\t"
    : "=&v"(X)
    : "a"(pk[16][0]), "a"(pk[16][1]), "a"(pk[16][2]), "a"(pk[16][3]), "a"(pk[16][4]), "a"(pk[16][5]), "a"(pk[16][6]), "a"(pk[16][7]), "a"(pk[16][8]), "a"(pk[16][9]), "a"(pk[16][10]), "a"(pk[16][11]), "a"(pk[16][12]), "v"(q), "s"(modd), "s"(mhi), "s"(0x0f0f0f0fu), "s"(0x80808080u), "s"(0x05010400u), "s"(0x07030602u)
    : "v0", "v1", "v2", "v3", "v4", "v5", "v6", "v7", "v8", "v9", "v10", "v11", "v12", "v13", "v14", "v15", "v16", "v17", "v18", "v19", "v20", "v21", "v22", "v23", "v24", "v25", "v26", "v27", "v28", "v29", "v30", "v31", "v32", "v33", "v34", "v35", "v36", "v37", "v38", "v39");
}
__device__ __forceinline__ void sv8(f32x4& acc0, f32x4& acc1, const u32 (&pk)[30][13], u32 pp, u32 mask0, u32 mask1) {
  asm volatile(
    "v_accvgpr_read_b32 v38, %14\n\t"
    "v_accvgpr_read_b32 v26, %4\n\t"
    "v_accvgpr_read_b32 v33, %7\n\t"
    "v_accvgpr_read_b32 v24, %2\n\t"
    "v_accvgpr_read_b32 v31, %5\n\t"
    "v_accvgpr_read_b32 v25, %3\n\t"
    "v_accvgpr_read_b32 v32, %6\n\t"
    "v_lshlrev_b32 v29, 7, v38\n\t"
    "v_lshlrev_b32 v36, 5, v38\n\t"
    "v_and_b32 v27, %18, v26\n\t"
    "v_and_b32 v34, %18, v33\n\t"
    "v_lshrrev_b32 v28, 4, v26\n\t"
    "v_lshrrev_b32 v35, 4, v33\n\t"
    "v_lshlrev_b32 v30, 6, v38\n\t"
    "v_lshlrev_b32 v37, 4, v38\n\t"
    "v_and_or_b32 v27, v29, %19, v27\n\t"
    "v_and_or_b32 v34, v36, %19, v34\n\t"
    "v_and_b32 v28, %18, v28\n\t"
    "v_and_b32 v35, %18, v35\n\t"
    "v_perm_b32 v16, v27, v24, %20\n\t"
    "v_perm_b32 v18, v34, v31, %20\n\t"
    "v_and_or_b32 v28, v30, %19, v28\n\t"
    "v_and_or_b32 v35, v37, %19, v35\n\t"
    "v_perm_b32 v17, v27, v24, %21\n\t"
    "v_perm_b32 v19, v34, v31, %21\n\t"
    "v_perm_b32 v20, v28, v25, %20\n\t"
    "v_perm_b32 v22, v35, v32, %20\n\t"
    "v_perm_b32 v21, v28, v25, %21\n\t"
    "v_perm_b32 v23, v35, v32, %21\n\t"
    "v_and_b32_dpp v0, %15, %16 quad_perm:[0,0,0,0] row_mask:0xf bank_mask:0xf\n\t"
    "v_and_b32_dpp v1, %15, %17 quad_perm:[0,0,0,0] row_mask:0xf bank_mask:0xf\n\t"
    "v_and_b32_dpp v2, %15, %16 quad_perm:[1,1,1,1] row_mask:0xf bank_mask:0xf\n\t"
    "v_and_b32_dpp v3, %15, %17 quad_perm:[1,1,1,1] row_mask:0xf bank_mask:0xf\n\t"
    "v_accvgpr_read_b32 v26, %10\n\t"
    "v_accvgpr_read_b32 v33, %13\n\t"
    "v_mfma_f32_16x16x32_bf16 %0, v[0:3], v[16:19], %0\n\t"
    "v_accvgpr_read_b32 v24, %8\n\t"
    "v_accvgpr_read_b32 v31, %11\n\t"
    "v_accvgpr_read_b32 v25, %9\n\t"
    "v_mfma_f32_16x16x32_bf16 %1, v[0:3], v[20:23], %1\n\t"
    "v_accvgpr_read_b32 v32, %12\n\t"
    "v_lshlrev_b32 v29, 3, v38\n\t"
    "v_lshlrev_b32 v36, 1, v38\n\t"
    "v_and_b32 v27, %18, v26\n\t"
    "v_and_b32 v34, %18, v33\n\t"
    "v_lshrrev_b32 v28, 4, v26\n\t"
    "v_lshrrev_b32 v35, 4, v33\n\t"
    "v_lshlrev_b32 v30, 2, v38\n\t"
    "v_and_or_b32 v34, v36, %19, v34\n\t"
    "v_and_or_b32 v27, v29, %19, v27\n\t"
    "v_and_b32 v35, %18, v35\n\t"
    "v_and_b32 v28, %18, v28\n\t"
    "v_perm_b32 v18, v34, v31, %20\n\t"
    "v_perm_b32 v16, v27, v24, %20\n\t"
    "v_and_or_b32 v35, v38, %19, v35\n\t"
    "v_and_or_b32 v28, v30, %19, v28\n\t"
    "v_perm_b32 v19, v34, v31, %21\n\t"
    "v_perm_b32 v17, v27, v24, %21\n\t"
    "v_perm_b32 v22, v35, v32, %20\n\t"
    "v_perm_b32 v20, v28, v25, %20\n\t"
    "v_perm_b32 v23, v35, v32, %21\n\t"
    "v_perm_b32 v21, v28, v25, %21\n\t"
    "v_and_b32_dpp v4, %15, %16 quad_perm:[2,2,2,2] row_mask:0xf bank_mask:0xf\n\t"
    "v_and_b32_dpp v5, %15, %17 quad_perm:[2,2,2,2] row_mask:0xf bank_mask:0xf\n\t"
    "v_and_b32_dpp v6, %15, %16 quad_perm:[3,3,3,3] row_mask:0xf bank_mask:0xf\n\t"
    "v_and_b32_dpp v7, %15, %17 quad_perm:[3,3,3,3] row_mask:0xf bank_mask:0xf\n\t"
    "s_nop 1\n\t"
    "v_mfma_f32_16x16x32_bf16 %0, v[4:7], v[16:19], %0\n\t"
    "v_mfma_f32_16x16x32_bf16 %1, v[4:7], v[20:23], %1\n\t"
    : "+v"(acc0), "+v"(acc1)
    : "a"(pk[17][0]), "a"(pk[17][1]), "a"(pk[17][2]), "a"(pk[17][3]), "a"(pk[17][4]), "a"(pk[17][5]), "a"(pk[17][6]), "a"(pk[17][7]), "a"(pk[17][8]), "a"(pk[17][9]), "a"(pk[17][10]), "a"(pk[17][11]), "a"(pk[17][12]), "v"(pp), "v"(mask0), "v"(mask1), "s"(0x0f0f0f0fu), "s"(0x80808080u), "s"(0x05010400u), "s"(0x07030602u)
    : "v0", "v1", "v2", "v3", "v4", "v5", "v6", "v7", "v8", "v9", "v10", "v11", "v12", "v13", "v14", "v15", "v16", "v17", "v18", "v19", "v20", "v21", "v22", "v23", "v24", "v25", "v26", "v27", "v28", "v29", "v30", "v31", "v32", "v33", "v34", "v35", "v36", "v37", "v38", "v39");
}
__device__ __forceinline__ void sk9(float& X, const u32 (&pk)[30][13], u32x4 q, u64 modd, u64 mhi) {
  asm volatile(
    "v_accvgpr_read_b32 v38, %13\n\t"
    "v_accvgpr_read_b32 v26, %3\n\t"
    "v_accvgpr_read_b32 v33, %6\n\t"
    "v_accvgpr_read_b32 v24, %1\n\t"
    "v_accvgpr_read_b32 v31, %4\n\t"
    "v_accvgpr_read_b32 v25, %2\n\t"
    "v_accvgpr_read_b32 v32, %5\n\t"
    "v_lshlrev_b32 v29, 7, v38\n\t"
    "v_lshlrev_b32 v36, 5, v38\n\t"
    "v_and_b32 v27, %17, v26\n\t"
    "v_and_b32 v34, %17, v33\n\t"
    "v_lshrrev_b32 v28, 4, v26\n\t"
    "v_lshrrev_b32 v35, 4, v33\n\t"
    "v_lshlrev_b32 v30, 6, v38\n\t"
    "v_lshlrev_b32 v37, 4, v38\n\t"
    "v_and_or_b32 v27, v29, %18, v27\n\t"
    "v_and_or_b32 v34, v36, %18, v34\n\t"
    "v_and_b32 v28, %17, v28\n\t"
    "v_and_b32 v35, %17, v35\n\t"
    "v_perm_b32 v16, v27, v24, %19\n\t"
    "v_perm_b32 v20, v34, v31, %19\n\t"
    "v_and_or_b32 v28, v30, %18, v28\n\t"
    "v_and_or_b32 v35, v37, %18, v35\n\t"
    "v_perm_b32 v17, v27, v24, %20\n\t"
    "v_perm_b32 v21, v34, v31, %20\n\t"
    "v_perm_b32 v18, v28, v25, %19\n\t"
    "v_perm_b32 v22, v35, v32, %19\n\t"
    "v_perm_b32 v19, v28, v25, %20\n\t"
    "v_perm_b32 v23, v35, v32, %20\n\t"
    "v_accvgpr_read_b32 v26, %9\n\t"
    "v_mfma_f32_16x16x32_bf16 v[0:3], v[16:19], %14, 0\n\t"
    "v_accvgpr_read_b32 v33, %12\n\t"
    "v_accvgpr_read_b32 v24, %7\n\t"
    "v_mfma_f32_16x16x32_bf16 v[4:7], v[20:23], %14, 0\n\t"
    "v_accvgpr_read_b32 v31, %10\n\t"
    "v_accvgpr_read_b32 v25, %8\n\t"
    "v_accvgpr_read_b32 v32, %11\n\t"
    "v_lshlrev_b32 v29, 3, v38\n\t"
    "v_lshlrev_b32 v36, 1, v38\n\t"
    "v_and_b32 v27, %17, v26\n\t"
    "v_and_b32 v34, %17, v33\n\t"
    "v_lshrrev_b32 v28, 4, v26\n\t"
    "v_lshrrev_b32 v35, 4, v33\n\t"
    "v_lshlrev_b32 v30, 2, v38\n\t"
    "v_and_or_b32 v34, v36, %18, v34\n\t"
    "v_and_or_b32 v27, v29, %18, v27\n\t"
    "v_and_b32 v35, %17, v35\n\t"
    "v_and_b32 v28, %17, v28\n\t"
    "v_perm_b32 v20, v34, v31, %19\n\t"
    "v_perm_b32 v16, v27, v24, %19\n\t"
    "v_and_or_b32 v35, v38, %18, v35\n\t"
    "v_and_or_b32 v28, v30, %18, v28\n\t"
    "v_perm_b32 v21, v34, v31, %20\n\t"
    "v_perm_b32 v17, v27, v24, %20\n\t"
    "v_perm_b32 v22, v35, v32, %19\n\t"
    "v_perm_b32 v18, v28, v25, %19\n\t"
    "v_perm_b32 v23, v35, v32, %20\n\t"
    "v_perm_b32 v19, v28, v25, %20\n\t"
    "v_add_f32_dpp v0, v1, v0 quad_perm:[1,1,1,1] row_mask:0xf bank_mask:0xf\n\t"
    "v_add_f32_dpp v5, v6, v5 quad_perm:[2,2,2,2] row_mask:0xf bank_mask:0xf\n\t"
    "v_mfma_f32_16x16x32_bf16 v[8:11], v[16:19], %14, 0\n\t"
    "v_add_f32_dpp v0, v2, v0 quad_perm:[2,2,2,2] row_mask:0xf bank_mask:0xf\n\t"
    "v_mfma_f32_16x16x32_bf16 v[12:15], v[20:23], %14, 0\n\t"
    "v_add_f32_dpp v5, v7, v5 quad_perm:[3,3,3,3] row_mask:0xf bank_mask:0xf\n\t"
    "v_add_f32_dpp v0, v3, v0 quad_perm:[3,3,3,3] row_mask:0xf bank_mask:0xf\n\t"
    "v_add_f32_dpp v5, v4, v5 quad_perm:[0,0,0,0] row_mask:0xf bank_mask:0xf\n\t"
    "v_cndmask_b32_e64 v0, v0, v5, %15\n\t"
    "s_nop 1\n\t"
    "v_add_f32_dpp v10, v11, v10 quad_perm:[3,3,3,3] row_mask:0xf bank_mask:0xf\n\t"
    "s_nop 0\n\t"
    "v_add_f32_dpp v15, v12, v15 quad_perm:[0,0,0,0] row_mask:0xf bank_mask:0xf\n\t"
    "v_add_f32_dpp v10, v8, v10 quad_perm:[0,0,0,0] row_mask:0xf bank_mask:0xf\n\t"
    "v_add_f32_dpp v15, v13, v15 quad_perm:[1,1,1,1] row_mask:0xf bank_mask:0xf\n\t"
    "v_add_f32_dpp v10, v9, v10 quad_perm:[1,1,1,1] row_mask:0xf bank_mask:0xf\n\t"
    "v_add_f32_dpp v15, v14, v15 quad_perm:[2,2,2,2] row_mask:0xf bank_mask:0xf\n\t"
    "v_cndmask_b32_e64 v10, v10, v15, %15\n\t"
    "v_cndmask_b32_e64 %0, v0, v10, %16\n\t"
    : "=&v"(X)
    : "a"(pk[18][0]), "a"(pk[18][1]), "a"(pk[18][2]), "a"(pk[18][3]), "a"(pk[18][4]), "a"(pk[18][5]), "a"(pk[18][6]), "a"(pk[18][7]), "a"(pk[18][8]), "a"(pk[18][9]), "a"(pk[18][10]), "a"(pk[18][11]), "a"(pk[18][12]), "v"(q), "s"(modd), "s"(mhi), "s"(0x0f0f0f0fu), "s"(0x80808080u), "s"(0x05010400u), "s"(0x07030602u)
    : "v0", "v1", "v2", "v3", "v4", "v5", "v6", "v7", "v8", "v9", "v10", "v11", "v12", "v13", "v14", "v15", "v16", "v17", "v18", "v19", "v20", "v21", "v22", "v23", "v24", "v25", "v26", "v27", "v28", "v29", "v30", "v31", "v32", "v33", "v34", "v35", "v36", "v37", "v38", "v39");
}
__device__ __forceinline__ void sv9(f32x4& acc0, f32x4& acc1, const u32 (&pk)[30][13], u32 pp, u32 mask0, u32 mask1) {
  asm volatile(
    "v_accvgpr_read_b32 v26, %4\n\t"
    "v_accvgpr_read_b32 v33, %7\n\t"
    "v_accvgpr_read_b32 v24, %2\n\t"
    "v_accvgpr_read_b32 v31, %5\n\t"
    "v_accvgpr_read_b32 v25, %3\n\t"
    "v_accvgpr_read_b32 v32, %6\n\t"
    "v_lshlrev_b32 v29, 7, %14\n\t"
    "v_lshlrev_b32 v36, 5, %14\n\t"
    "v_and_b32 v27, %18, v26\n\t"
    "v_and_b32 v34, %18, v33\n\t"
    "v_lshrrev_b32 v28, 4, v26\n\t"
    "v_lshrrev_b32 v35, 4, v33\n\t"
    "v_lshlrev_b32 v30, 6, %14\n\t"
    "v_lshlrev_b32 v37, 4, %14\n\t"
    "v_and_or_b32 v27, v29, %19, v27\n\t"
    "v_and_or_b32 v34, v36, %19, v34\n\t"
    "v_and_b32 v28, %18, v28\n\t"
    "v_and_b32 v35, %18, v35\n\t"
    "v_perm_b32 v16, v27, v24, %20\n\t"
    "v_perm_b32 v18, v34, v31, %20\n\t"
    "v_and_or_b32 v28, v30, %19, v28\n\t"
    "v_and_or_b32 v35, v37, %19, v35\n\t"
    "v_perm_b32 v17, v27, v24, %21\n\t"
    "v_perm_b32 v19, v34, v31, %21\n\t"
    "v_perm_b32 v20, v28, v25, %20\n\t"
    "v_perm_b32 v22, v35, v32, %20\n\t"
    "v_perm_b32 v21, v28, v25, %21\n\t"
    "v_perm_b32 v23, v35, v32, %21\n\t"
    "v_and_b32_dpp v0, %15, %16 quad_perm:[0,0,0,0] row_mask:0xf bank_mask:0xf\n\t"
    "v_and_b32_dpp v1, %15, %17 quad_perm:[0,0,0,0] row_mask:0xf bank_mask:0xf\n\t"
    "v_and_b32_dpp v2, %15, %16 quad_perm:[1,1,1,1] row_mask:0xf bank_mask:0xf\n\t"
    "v_and_b32_dpp v3, %15, %17 quad_perm:[1,1,1,1] row_mask:0xf bank_mask:0xf\n\t"
    "v_accvgpr_read_b32 v26, %10\n\t"
    "v_lshlrev_b32 v36, 1, %14\n\t"
    "v_mfma_f32_16x16x32_bf16 %0, v[0:3], v[16:19], %0\n\t"
    "v_accvgpr_read_b32 v24, %8\n\t"
    "v_and_b32 v34, %18, %13\n\t"
    "v_accvgpr_read_b32 v25, %9\n\t"
    "v_mfma_f32_16x16x32_bf16 %1, v[0:3], v[20:23], %1\n\t"
    "v_lshrrev_b32 v35, 4, %13\n\t"
    "v_lshlrev_b32 v29, 3, %14\n\t"
    "v_and_or_b32 v34, v36, %19, v34\n\t"
    "v_and_b32 v27, %18, v26\n\t"
    "v_and_b32 v35, %18, v35\n\t"
    "v_lshrrev_b32 v28, 4, v26\n\t"
    "v_perm_b32 v18, v34, %11, %20\n\t"
    "v_lshlrev_b32 v30, 2, %14\n\t"
    "v_and_or_b32 v35, %14, %19, v35\n\t"
    "v_and_or_b32 v27, v29, %19, v27\n\t"
    "v_perm_b32 v19, v34, %11, %21\n\t"
    "v_and_b32 v28, %18, v28\n\t"
    "v_perm_b32 v22, v35, %12, %20\n\t"
    "v_perm_b32 v16, v27, v24, %20\n\t"
    "v_perm_b32 v23, v35, %12, %21\n\t"
    "v_and_or_b32 v28, v30, %19, v28\n\t"
    "v_perm_b32 v17, v27, v24, %21\n\t"
    "v_perm_b32 v20, v28, v25, %20\n\t"
    "v_perm_b32 v21, v28, v25, %21\n\t"
    "v_and_b32_dpp v4, %15, %16 quad_perm:[2,2,2,2] row_mask:0xf bank_mask:0xf\n\t"
    "v_and_b32_dpp v5, %15, %17 quad_perm:[2,2,2,2] row_mask:0xf bank_mask:0xf\n\t"
    "v_and_b32_dpp v6, %15, %16 quad_perm:[3,3,3,3] row_mask:0xf bank_mask:0xf\n\t"
    "v_and_b32_dpp v7, %15, %17 quad_perm:[3,3,3,3] row_mask:0xf bank_mask:0xf\n\t"
    "s_nop 1\n\t"
    "v_mfma_f32_16x16x32_bf16 %0, v[4:7], v[16:19], %0\n\t"
    "v_mfma_f32_16x16x32_bf16 %1, v[4:7], v[20:23], %1\n\t"
    : "+v"(acc0), "+v"(acc1)
    : "a"(pk[19][0]), "a"(pk[19][1]), "a"(pk[19][2]), "a"(pk[19][3]), "a"(pk[19][4]), "a"(pk[19][5]), "a"(pk[19][6]), "a"(pk[19][7]), "a"(pk[19][8]), "v"(pk[19][9]), "v"(pk[19][10]), "v"(pk[19][11]), "v"(pk[19][12]), "v"(pp), "v"(mask0), "v"(mask1), "s"(0x0f0f0f0fu), "s"(0x80808080u), "s"(0x05010400u), "s"(0x07030602u)
    : "v0", "v1", "v2", "v3", "v4", "v5", "v6", "v7", "v8", "v9", "v10", "v11", "v12", "v13", "v14", "v15", "v16", "v17", "v18", "v19", "v20", "v21", "v22", "v23", "v24", "v25", "v26", "v27", "v28", "v29", "v30", "v31", "v32", "v33", "v34", "v35", "v36", "v37", "v38", "v39");
}
__device__ __forceinline__ void sk10(float& X, const u32 (&pk)[30][13], u32x4 q, u64 modd, u64 mhi) {
  asm volatile(
    "v_lshlrev_b32 v29, 7, %13\n\t"
    "v_lshlrev_b32 v36, 5, %13\n\t"
    "v_and_b32 v27, %17, %3\n\t"
    "v_and_b32 v34, %17, %6\n\t"
    "v_lshrrev_b32 v28, 4, %3\n\t"
    "v_lshrrev_b32 v35, 4, %6\n\t"
    "v_lshlrev_b32 v30, 6, %13\n\t"
    "v_lshlrev_b32 v37, 4, %13\n\t"
    "v_and_or_b32 v27, v29, %18, v27\n\t"
    "v_and_or_b32 v34, v36, %18, v34\n\t"
    "v_and_b32 v28, %17, v28\n\t"
    "v_and_b32 v35, %17, v35\n\t"
    "v_perm_b32 v16, v27, %1, %19\n\t"
    "v_perm_b32 v20, v34, %4, %19\n\t"
    "v_and_or_b32 v28, v30, %18, v28\n\t"
    "v_and_or_b32 v35, v37, %18, v35\n\t"
    "v_perm_b32 v17, v27, %1, %20\n\t"
    "v_perm_b32 v21, v34, %4, %20\n\t"
    "v_perm_b32 v18, v28, %2, %19\n\t"
    "v_perm_b32 v22, v35, %5, %19\n\t"
    "v_perm_b32 v19, v28, %2, %20\n\t"
    "v_perm_b32 v23, v35, %5, %20\n\t"
    "v_lshlrev_b32 v29, 3, %13\n\t"
    "v_mfma_f32_16x16x32_bf16 v[0:3], v[16:19], %14, 0\n\t"
    "v_lshlrev_b32 v36, 1, %13\n\t"
    "v_and_b32 v27, %17, %9\n\t"
    "v_mfma_f32_16x16x32_bf16 v[4:7], v[20:23], %14, 0\n\t"
    "v_and_b32 v34, %17, %12\n\t"
    "v_lshrrev_b32 v28, 4, %9\n\t"
    "v_lshrrev_b32 v35, 4, %12\n\t"
    "v_lshlrev_b32 v30, 2, %13\n\t"
    "v_and_or_b32 v34, v36, %18, v34\n\t"
    "v_and_or_b32 v27, v29, %18, v27\n\t"
    "v_and_b32 v35, %17, v35\n\t"
    "v_and_b32 v28, %17, v28\n\t"
    "v_perm_b32 v20, v34, %10, %19\n\t"
    "v_perm_b32 v16, v27, %7, %19\n\t"
    "v_and_or_b32 v35, %13, %18, v35\n\t"
    "v_and_or_b32 v28, v30, %18, v28\n\t"
    "v_perm_b32 v21, v34, %10, %20\n\t"
    "v_perm_b32 v17, v27, %7, %20\n\t"
    "v_perm_b32 v22, v35, %11, %19\n\t"
    "v_perm_b32 v18, v28, %8, %19\n\t"
    "v_perm_b32 v23, v35, %11, %20\n\t"
    "v_perm_b32 v19, v28, %8, %20\n\t"
    "v_add_f32_dpp v0, v1, v0 quad_perm:[1,1,1,1] row_mask:0xf bank_mask:0xf\n\t"
    "v_add_f32_dpp v5, v6, v5 quad_perm:[2,2,2,2] row_mask:0xf bank_mask:0xf\n\t"
    "v_mfma_f32_16x16x32_bf16 v[8:11], v[16:19], %14, 0\n\t"
    "v_add_f32_dpp v0, v2, v0 quad_perm:[2,2,2,2] row_mask:0xf bank_mask:0xf\n\t"
    "v_mfma_f32_16x16x32_bf16 v[12:15], v[20:23], %14, 0\n\t"
    "v_add_f32_dpp v5, v7, v5 quad_perm:[3,3,3,3] row_mask:0xf bank_mask:0xf\n\t"
    "v_add_f32_dpp v0, v3, v0 quad_perm:[3,3,3,3] row_mask:0xf bank_mask:0xf\n\t"
    "v_add_f32_dpp v5, v4, v5 quad_perm:[0,0,0,0] row_mask:0xf bank_mask:0xf\n\t"
    "v_cndmask_b32_e64 v0, v0, v5, %15\n\t"
    "s_nop 1\n\t"
    "v_add_f32_dpp v10, v11, v10 quad_perm:[3,3,3,3] row_mask:0xf bank_mask:0xf\n\t"
    "s_nop 0\n\t"
    "v_add_f32_dpp v15, v12, v15 quad_perm:[0,0,0,0] row_mask:0xf bank_mask:0xf\n\t"
    "v_add_f32_dpp v10, v8, v10 quad_perm:[0,0,0,0] row_mask:0xf bank_mask:0xf\n\t"
    "v_add_f32_dpp v15, v13, v15 quad_perm:[1,1,1,1] row_mask:0xf bank_mask:0xf\n\t"
    "v_add_f32_dpp v10, v9, v10 quad_perm:[1,1,1,1] row_mask:0xf bank_mask:0xf\n\t"
    "v_add_f32_dpp v15, v14, v15 quad_perm:[2,2,2,2] row_mask:0xf bank_mask:0xf\n\t"
    "v_cndmask_b32_e64 v10, v10, v15, %15\n\t"
    "v_cndmask_b32_e64 %0, v0, v10, %16\n\t"
    : "=&v"(X)
    : "v"(pk[20][0]), "v"(pk[20][1]), "v"(pk[20][2]), "v"(pk[20][3]), "v"(pk[20][4]), "v"(pk[20][5]), "v"(pk[20][6]), "v"(pk[20][7]), "v"(pk[20][8]), "v"(pk[20][9]), "v"(pk[20][10]), "v"(pk[20][11]), "v"(pk[20][12]), "v"(q), "s"(modd), "s"(mhi), "s"(0x0f0f0f0fu), "s"(0x80808080u), "s"(0x05010400u), "s"(0x07030602u)
    : "v0", "v1", "v2", "v3", "v4", "v5", "v6", "v7", "v8", "v9", "v10", "v11", "v12", "v13", "v14", "v15", "v16", "v17", "v18", "v19", "v20", "v21", "v22", "v23", "v24", "v25", "v26", "v27", "v28", "v29", "v30", "v31", "v32", "v33", "v34", "v35", "v36", "v37", "v38", "v39");
}
__device__ __forceinline__ void sv10(f32x4& acc0, f32x4& acc1, const u32 (&pk)[30][13], u32 pp, u32 mask0, u32 mask1) {
  asm volatile(
    "v_lshlrev_b32 v29, 7, %14\n\t"
    "v_lshlrev_b32 v36, 5, %14\n\t"
    "v_and_b32 v27, %18, %4\n\t"
    "v_and_b32 v34, %18, %7\n\t"
    "v_lshrrev_b32 v28, 4, %4\n\t"
    "v_lshrrev_b32 v35, 4, %7\n\t"
    "v_lshlrev_b32 v30, 6, %14\n\t"
    "v_lshlrev_b32 v37, 4, %14\n\t"
    "v_and_or_b32 v27, v29, %19, v27\n\t"
    "v_and_or_b32 v34, v36, %19, v34\n\t"
    "v_and_b32 v28, %18, v28\n\t"
    "v_and_b32 v35, %18, v35\n\t"
    "v_perm_b32 v16, v27, %2, %20\n\t"
    "v_perm_b32 v18, v34, %5, %20\n\t"
    "v_and_or_b32 v28, v30, %19, v28\n\t"
    "v_and_or_b32 v35, v37, %19, v35\n\t"
    "v_perm_b32 v17, v27, %2, %21\n\t"
    "v_perm_b32 v19, v34, %5, %21\n\t"
    "v_perm_b32 v20, v28, %3, %20\n\t"
    "v_perm_b32 v22, v35, %6, %20\n\t"
    "v_perm_b32 v21, v28, %3, %21\n\t"
    "v_perm_b32 v23, v35, %6, %21\n\t"
    "v_and_b32_dpp v0, %15, %16 quad_perm:[0,0,0,0] row_mask:0xf bank_mask:0xf\n\t"
    "v_and_b32_dpp v1, %15, %17 quad_perm:[0,0,0,0] row_mask:0xf bank_mask:0xf\n\t"
    "v_and_b32_dpp v2, %15, %16 quad_perm:[1,1,1,1] row_mask:0xf bank_mask:0xf\n\t"
    "v_and_b32_dpp v3, %15, %17 quad_perm:[1,1,1,1] row_mask:0xf bank_mask:0xf\n\t"
    "v_lshlrev_b32 v29, 3, %14\n\t"
    "v_lshlrev_b32 v36, 1, %14\n\t"
    "v_mfma_f32_16x16x32_bf16 %0, v[0:3], v[16:19], %0\n\t"
    "v_and_b32 v27, %18, %10\n\t"
    "v_and_b32 v34, %18, %13\n\t"
    "v_lshrrev_b32 v28, 4, %10\n\t"
    "v_mfma_f32_16x16x32_bf16 %1, v[0:3], v[20:23], %1\n\t"
    "v_lshrrev_b32 v35, 4, %13\n\t"
    "v_lshlrev_b32 v30, 2, %14\n\t"
    "v_and_or_b32 v34, v36, %19, v34\n\t"
    "v_and_or_b32 v27, v29, %19, v27\n\t"
    "v_and_b32 v35, %18, v35\n\t"
    "v_and_b32 v28, %18, v28\n\t"
    "v_perm_b32 v18, v34, %11, %20\n\t"
    "v_perm_b32 v16, v27, %8, %20\n\t"
    "v_and_or_b32 v35, %14, %19, v35\n\t"
    "v_and_or_b32 v28, v30, %19, v28\n\t"
    "v_perm_b32 v19, v34, %11, %21\n\t"
    "v_perm_b32 v17, v27, %8, %21\n\t"
    "v_perm_b32 v22, v35, %12, %20\n\t"
    "v_perm_b32 v20, v28, %9, %20\n\t"
    "v_perm_b32 v23, v35, %12, %21\n\t"
    "v_perm_b32 v21, v28, %9, %21\n\t"
    "v_and_b32_dpp v4, %15, %16 quad_perm:[2,2,2,2] row_mask:0xf bank_mask:0xf\n\t"
    "v_and_b32_dpp v5, %15, %17 quad_perm:[2,2,2,2] row_mask:0xf bank_mask:0xf\n\t"
    "v_and_b32_dpp v6, %15, %16 quad_perm:[3,3,3,3] row_mask:0xf bank_mask:0xf\n\t"
    "v_and_b32_dpp v7, %15, %17 quad_perm:[3,3,3,3] row_mask:0xf bank_mask:0xf\n\t"
    "s_nop 1\n\t"
    "v_mfma_f32_16x16x32_bf16 %0, v[4:7], v[16:19], %0\n\t"
    "v_mfma_f32_16x16x32_bf16 %1, v[4:7], v[20:23], %1\n\t"
    : "+v"(acc0), "+v"(acc1)
    : "v"(pk[21][0]), "v"(pk[21][1]), "v"(pk[21][2]), "v"(pk[21][3]), "v"(pk[21][4]), "v"(pk[21][5]), "v"(pk[21][6]), "v"(pk[21][7]), "v"(pk[21][8]), "v"(pk[21][9]), "v"(pk[21][10]), "v"(pk[21][11]), "v"(pk[21][12]), "v"(pp), "v"(mask0), "v"(mask1), "s"(0x0f0f0f0fu), "s"(0x80808080u), "s"(0x05010400u), "s"(0x07030602u)
    : "v0", "v1", "v2", "v3", "v4", "v5", "v6", "v7", "v8", "v9", "v10", "v11", "v12", "v13", "v14", "v15", "v16", "v17", "v18", "v19", "v20", "v21", "v22", "v23", "v24", "v25", "v26", "v27", "v28", "v29", "v30", "v31", "v32", "v33", "v34", "v35", "v36", "v37", "v38", "v39");
}
__device__ __forceinline__ void sk11(float& X, const u32 (&pk)[30][13], u32x4 q, u64 modd, u64 mhi) {
  asm volatile(
    "v_lshlrev_b32 v29, 7, %13\n\t"
    "v_lshlrev_b32 v36, 5, %13\n\t"
    "v_and_b32 v27, %17, %3\n\t"
    "v_and_b32 v34, %17, %6\n\t"
    "v_lshrrev_b32 v28, 4, %3\n\t"
    "v_lshrrev_b32 v35, 4, %6\n\t"
    "v_lshlrev_b32 v30, 6, %13\n\t"
    "v_lshlrev_b32 v37, 4, %13\n\t"
    "v_and_or_b32 v27, v29, %18, v27\n\t"
    "v_and_or_b32 v34, v36, %18, v34\n\t"
    "v_and_b32 v28, %17, v28\n\t"
    "v_and_b32 v35, %17, v35\n\t"
    "v_perm_b32 v16, v27, %1, %19\n\t"
    "v_perm_b32 v20, v34, %4, %19\n\t"
    "v_and_or_b32 v28, v30, %18, v28\n\t"
    "v_and_or_b32 v35, v37, %18, v35\n\t"
    "v_perm_b32 v17, v27, %1, %20\n\t"
    "v_perm_b32 v21, v34, %4, %20\n\t"
    "v_perm_b32 v18, v28, %2, %19\n\t"
    "v_perm_b32 v22, v35, %5, %19\n\t"
    "v_perm_b32 v19, v28, %2, %20\n\t"
    "v_perm_b32 v23, v35, %5, %20\n\t"
    "v_lshlrev_b32 v29, 3, %13\n\t"
    "v_mfma_f32_16x16x32_bf16 v[0:3], v[16:19], %14, 0\n\t"
    "v_lshlrev_b32 v36, 1, %13\n\t"
    "v_and_b32 v27, %17, %9\n\t"
    "v_mfma_f32_16x16x32_bf16 v[4:7], v[20:23], %14, 0\n\t"
    "v_and_b32 v34, %17, %12\n\t"
    "v_lshrrev_b32 v28, 4, %9\n\t"
    "v_lshrrev_b32 v35, 4, %12\n\t"
    "v_lshlrev_b32 v30, 2, %13\n\t"
    "v_and_or_b32 v34, v36, %18, v34\n\t"
    "v_and_or_b32 v27, v29, %18, v27\n\t"
    "v_and_b32 v35, %17, v35\n\t"
    "v_and_b32 v28, %17, v28\n\t"
    "v_perm_b32 v20, v34, %10, %19\n\t"
    "v_perm_b32 v16, v27, %7, %19\n\t"
    "v_and_or_b32 v35, %13, %18, v35\n\t"
    "v_and_or_b32 v28, v30, %18, v28\n\t"
    "v_perm_b32 v21, v34, %10, %20\n\t"
    "v_perm_b32 v17, v27, %7, %20\n\t"
    "v_perm_b32 v22, v35, %11, %19\n\t"
    "v_perm_b32 v18, v28, %8, %19\n\t"
    "v_perm_b32 v23, v35, %11, %20\n\t"
    "v_perm_b32 v19, v28, %8, %20\n\t"
    "v_add_f32_dpp v0, v1, v0 quad_perm:[1,1,1,1] row_mask:0xf bank_mask:0xf\n\t"
    "v_add_f32_dpp v5, v6, v5 quad_perm:[2,2,2,2] row_mask:0xf bank_mask:0xf\n\t"
    "v_mfma_f32_16x16x32_bf16 v[8:11], v[16:19], %14, 0\n\t"
    "v_add_f32_dpp v0, v2, v0 quad_perm:[2,2,2,2] row_mask:0xf bank_mask:0xf\n\t"
    "v_mfma_f32_16x16x32_bf16 v[12:15], v[20:23], %14, 0\n\t"
    "v_add_f32_dpp v5, v7, v5 quad_perm:[3,3,3,3] row_mask:0xf bank_mask:0xf\n\t"
    "v_add_f32_dpp v0, v3, v0 quad_perm:[3,3,3,3] row_mask:0xf bank_mask:0xf\n\t"
    "v_add_f32_dpp v5, v4, v5 quad_perm:[0,0,0,0] row_mask:0xf bank_mask:0xf\n\t"
    "v_cndmask_b32_e64 v0, v0, v5, %15\n\t"
    "s_nop 1\n\t"
    "v_add_f32_dpp v10, v11, v10 quad_perm:[3,3,3,3] row_mask:0xf bank_mask:0xf\n\t"
    "s_nop 0\n\t"
    "v_add_f32_dpp v15, v12, v15 quad_perm:[0,0,0,0] row_mask:0xf bank_mask:0xf\n\t"
    "v_add_f32_dpp v10, v8, v10 quad_perm:[0,0,0,0] row_mask:0xf bank_mask:0xf\n\t"
    "v_add_f32_dpp v15, v13, v15 quad_perm:[1,1,1,1] row_mask:0xf bank_mask:0xf\n\t"
    "v_add_f32_dpp v10, v9, v10 quad_perm:[1,1,1,1] row_mask:0xf bank_mask:0xf\n\t"
    "v_add_f32_dpp v15, v14, v15 quad_perm:[2,2,2,2] row_mask:0xf bank_mask:0xf\n\t"
    "v_cndmask_b32_e64 v10, v10, v15, %15\n\t"
    "v_cndmask_b32_e64 %0, v0, v10, %16\n\t"
    : "=&v"(X)
    : "v"(pk[22][0]), "v"(pk[22][1]), "v"(pk[22][2]), "v"(pk[22][3]), "v"(pk[22][4]), "v"(pk[22][5]), "v"(pk[22][6]), "v"(pk[22][7]), "v"(pk[22][8]), "v"(pk[22][9]), "v"(pk[22][10]), "v"(pk[22][11]), "v"(pk[22][12]), "v"(q), "s"(modd), "s"(mhi), "s"(0x0f0f0f0fu), "s"(0x80808080u), "s"(0x05010400u), "s"(0x07030602u)
    : "v0", "v1", "v2", "v3", "v4", "v5", "v6", "v7", "v8", "v9", "v10", "v11", "v12", "v13", "v14", "v15", "v16", "v17", "v18", "v19", "v20", "v21", "v22", "v23", "v24", "v25", "v26", "v27", "v28", "v29", "v30", "v31", "v32", "v33", "v34", "v35", "v36", "v37", "v38", "v39");
}
__device__ __forceinline__ void sv11(f32x4& acc0, f32x4& acc1, const u32 (&pk)[30][13], u32 pp, u32 mask0, u32 mask1) {
  asm volatile(
    "v_lshlrev_b32 v29, 7, %14\n\t"
    "v_lshlrev_b32 v36, 5, %14\n\t"
    "v_and_b32 v27, %18, %4\n\t"
    "v_and_b32 v34, %18, %7\n\t"
    "v_lshrrev_b32 v28, 4, %4\n\t"
    "v_lshrrev_b32 v35, 4, %7\n\t"
    "v_lshlrev_b32 v30, 6, %14\n\t"
    "v_lshlrev_b32 v37, 4, %14\n\t"
    "v_and_or_b32 v27, v29, %19, v27\n\t"
    "v_and_or_b32 v34, v36, %19, v34\n\t"
    "v_and_b32 v28, %18, v28\n\t"
    "v_and_b32 v35, %18, v35\n\t"
    "v_perm_b32 v16, v27, %2, %20\n\t"
    "v_perm_b32 v18, v34, %5, %20\n\t"
    "v_and_or_b32 v28, v30, %19, v28\n\t"
    "v_and_or_b32 v35, v37, %19, v35\n\t"
    "v_perm_b32 v17, v27, %2, %21\n\t"
    "v_perm_b32 v19, v34, %5, %21\n\t"
    "v_perm_b32 v20, v28, %3, %20\n\t"
    "v_perm_b32 v22, v35, %6, %20\n\t"
    "v_perm_b32 v21, v28, %3, %21\n\t"
    "v_perm_b32 v23, v35, %6, %21\n\t"
    "v_and_b32_dpp v0, %15, %16 quad_perm:[0,0,0,0] row_mask:0xf bank_mask:0xf\n\t"
    "v_and_b32_dpp v1, %15, %17 quad_perm:[0,0,0,0] row_mask:0xf bank_mask:0xf\n\t"
    "v_and_b32_dpp v2, %15, %16 quad_perm:[1,1,1,1] row_mask:0xf bank_mask:0xf\n\t"
    "v_and_b32_dpp v3, %15, %17 quad_perm:[1,1,1,1] row_mask:0xf bank_mask:0xf\n\t"
    "v_lshlrev_b32 v29, 3, %14\n\t"
    "v_lshlrev_b32 v36, 1, %14\n\t"
    "v_mfma_f32_16x16x32_bf16 %0, v[0:3], v[16:19], %0\n\t"
    "v_and_b32 v27, %18, %10\n\t"
    "v_and_b32 v34, %18, %13\n\t"
    "v_lshrrev_b32 v28, 4, %10\n\t"
    "v_mfma_f32_16x16x32_bf16 %1, v[0:3], v[20:23], %1\n\t"
    "v_lshrrev_b32 v35, 4, %13\n\t"
    "v_lshlrev_b32 v30, 2, %14\n\t"
    "v_and_or_b32 v34, v36, %19, v34\n\t"
    "v_and_or_b32 v27, v29, %19, v27\n\t"
    "v_and_b32 v35, %18, v35\n\t"
    "v_and_b32 v28, %18, v28\n\t"
    "v_perm_b32 v18, v34, %11, %20\n\t"
    "v_perm_b32 v16, v27, %8, %20\n\t"
    "v_and_or_b32 v35, %14, %19, v35\n\t"
    "v_and_or_b32 v28, v30, %19, v28\n\t"
    "v_perm_b32 v19, v34, %11, %21\n\t"
    "v_perm_b32 v17, v27, %8, %21\n\t"
    "v_perm_b32 v22, v35, %12, %20\n\t"
    "v_perm_b32 v20, v28, %9, %20\n\t"
    "v_perm_b32 v23, v35, %12, %21\n\t"
    "v_perm_b32 v21, v28, %9, %21\n\t"
    "v_and_b32_dpp v4, %15, %16 quad_perm:[2,2,2,2] row_mask:0xf bank_mask:0xf\n\t"
    "v_and_b32_dpp v5, %15, %17 quad_perm:[2,2,2,2] row_mask:0xf bank_mask:0xf\n\t"
    "v_and_b32_dpp v6, %15, %16 quad_perm:[3,3,3,3] row_mask:0xf bank_mask:0xf\n\t"
    "v_and_b32_dpp v7, %15, %17 quad_perm:[3,3,3,3] row_mask:0xf bank_mask:0xf\n\t"
    "s_nop 1\n\t"
    "v_mfma_f32_16x16x32_bf16 %0, v[4:7], v[16:19], %0\n\t"
    "v_mfma_f32_16x16x32_bf16 %1, v[4:7], v[20:23], %1\n\t"
    : "+v"(acc0), "+v"(acc1)
    : "v"(pk[23][0]), "v"(pk[23][1]), "v"(pk[23][2]), "v"(pk[23][3]), "v"(pk[23][4]), "v"(pk[23][5]), "v"(pk[23][6]), "v"(pk[23][7]), "v"(pk[23][8]), "v"(pk[23][9]), "v"(pk[23][10]), "v"(pk[23][11]), "v"(pk[23][12]), "v"(pp), "v"(mask0), "v"(mask1), "s"(0x0f0f0f0fu), "s"(0x80808080u), "s"(0x05010400u), "s"(0x07030602u)
    : "v0", "v1", "v2", "v3", "v4", "v5", "v6", "v7", "v8", "v9", "v10", "v11", "v12", "v13", "v14", "v15", "v16", "v17", "v18", "v19", "v20", "v21", "v22", "v23", "v24", "v25", "v26", "v27", "v28", "v29", "v30", "v31", "v32", "v33", "v34", "v35", "v36", "v37", "v38", "v39");
}
__device__ __forceinline__ void sk12(float& X, const u32 (&pk)[30][13], u32x4 q, u64 modd, u64 mhi) {
  asm volatile(
    "v_lshlrev_b32 v29, 7, %13\n\t"
    "v_lshlrev_b32 v36, 5, %13\n\t"
    "v_and_b32 v27, %17, %3\n\t"
    "v_and_b32 v34, %17, %6\n\t"
    "v_lshrrev_b32 v28, 4, %3\n\t"
    "v_lshrrev_b32 v35, 4, %6\n\t"
    "v_lshlrev_b32 v30, 6, %13\n\t"
    "v_lshlrev_b32 v37, 4, %13\n\t"
    "v_and_or_b32 v27, v29, %18, v27\n\t"
    "v_and_or_b32 v34, v36, %18, v34\n\t"
    "v_and_b32 v28, %17, v28\n\t"
    "v_and_b32 v35, %17, v35\n\t"
    "v_perm_b32 v16, v27, %1, %19\n\t"
    "v_perm_b32 v20, v34, %4, %19\n\t"
    "v_and_or_b32 v28, v30, %18, v28\n\t"
    "v_and_or_b32 v35, v37, %18, v35\n\t"
    "v_perm_b32 v17, v27, %1, %20\n\t"
    "v_perm_b32 v21, v34, %4, %20\n\t"
    "v_perm_b32 v18, v28, %2, %19\n\t"
    "v_perm_b32 v22, v35, %5, %19\n\t"
    "v_perm_b32 v19, v28, %2, %20\n\t"
    "v_perm_b32 v23, v35, %5, %20\n\t"
    "v_lshlrev_b32 v29, 3, %13\n\t"
    "v_mfma_f32_16x16x32_bf16 v[0:3], v[16:19], %14, 0\n\t"
    "v_lshlrev_b32 v36, 1, %13\n\t"
    "v_and_b32 v27, %17, %9\n\t"
    "v_mfma_f32_16x16x32_bf16 v[4:7], v[20:23], %14, 0\n\t"
    "v_and_b32 v34, %17, %12\n\t"
    "v_lshrrev_b32 v28, 4, %9\n\t"
    "v_lshrrev_b32 v35, 4, %12\n\t"
    "v_lshlrev_b32 v30, 2, %13\n\t"
    "v_and_or_b32 v34, v36, %18, v34\n\t"
    "v_and_or_b32 v27, v29, %18, v27\n\t"
    "v_and_b32 v35, %17, v35\n\t"
    "v_and_b32 v28, %17, v28\n\t"
    "v_perm_b32 v20, v34, %10, %19\n\t"
    "v_perm_b32 v16, v27, %7, %19\n\t"
    "v_and_or_b32 v35, %13, %18, v35\n\t"
    "v_and_or_b32 v28, v30, %18, v28\n\t"
    "v_perm_b32 v21, v34, %10, %20\n\t"
    "v_perm_b32 v17, v27, %7, %20\n\t"
    "v_perm_b32 v22, v35, %11, %19\n\t"
    "v_perm_b32 v18, v28, %8, %19\n\t"
    "v_perm_b32 v23, v35, %11, %20\n\t"
    "v_perm_b32 v19, v28, %8, %20\n\t"
    "v_add_f32_dpp v0, v1, v0 quad_perm:[1,1,1,1] row_mask:0xf bank_mask:0xf\n\t"
    "v_add_f32_dpp v5, v6, v5 quad_perm:[2,2,2,2] row_mask:0xf bank_mask:0xf\n\t"
    "v_mfma_f32_16x16x32_bf16 v[8:11], v[16:19], %14, 0\n\t"
    "v_add_f32_dpp v0, v2, v0 quad_perm:[2,2,2,2] row_mask:0xf bank_mask:0xf\n\t"
    "v_mfma_f32_16x16x32_bf16 v[12:15], v[20:23], %14, 0\n\t"
    "v_add_f32_dpp v5, v7, v5 quad_perm:[3,3,3,3] row_mask:0xf bank_mask:0xf\n\t"
    "v_add_f32_dpp v0, v3, v0 quad_perm:[3,3,3,3] row_mask:0xf bank_mask:0xf\n\t"
    "v_add_f32_dpp v5, v4, v5 quad_perm:[0,0,0,0] row_mask:0xf bank_mask:0xf\n\t"
    "v_cndmask_b32_e64 v0, v0, v5, %15\n\t"
    "s_nop 1\n\t"
    "v_add_f32_dpp v10, v11, v10 quad_perm:[3,3,3,3] row_mask:0xf bank_mask:0xf\n\t"
    "s_nop 0\n\t"
    "v_add_f32_dpp v15, v12, v15 quad_perm:[0,0,0,0] row_mask:0xf bank_mask:0xf\n\t"
    "v_add_f32_dpp v10, v8, v10 quad_perm:[0,0,0,0] row_mask:0xf bank_mask:0xf\n\t"
    "v_add_f32_dpp v15, v13, v15 quad_perm:[1,1,1,1] row_mask:0xf bank_mask:0xf\n\t"
    "v_add_f32_dpp v10, v9, v10 quad_perm:[1,1,1,1] row_mask:0xf bank_mask:0xf\n\t"
    "v_add_f32_dpp v15, v14, v15 quad_perm:[2,2,2,2] row_mask:0xf bank_mask:0xf\n\t"
    "v_cndmask_b32_e64 v10, v10, v15, %15\n\t"
    "v_cndmask_b32_e64 %0, v0, v10, %16\n\t"
    : "=&v"(X)
    : "v"(pk[24][0]), "v"(pk[24][1]), "v"(pk[24][2]), "v"(pk[24][3]), "v"(pk[24][4]), "v"(pk[24][5]), "v"(pk[24][6]), "v"(pk[24][7]), "v"(pk[24][8]), "v"(pk[24][9]), "v"(pk[24][10]), "v"(pk[24][11]), "v"(pk[24][12]), "v"(q), "s"(modd), "s"(mhi), "s"(0x0f0f0f0fu), "s"(0x80808080u), "s"(0x05010400u), "s"(0x07030602u)
    : "v0", "v1", "v2", "v3", "v4", "v5", "v6", "v7", "v8", "v9", "v10", "v11", "v12", "v13", "v14", "v15", "v16", "v17", "v18", "v19", "v20", "v21", "v22", "v23", "v24", "v25", "v26", "v27", "v28", "v29", "v30", "v31", "v32", "v33", "v34", "v35", "v36", "v37", "v38", "v39");
}
__device__ __forceinline__ void sv12(f32x4& acc0, f32x4& acc1, const u32 (&pk)[30][13], u32 pp, u32 mask0, u32 mask1) {
  asm volatile(
    "v_lshlrev_b32 v29, 7, %14\n\t"
    "v_lshlrev_b32 v36, 5, %14\n\t"
    "v_and_b32 v27, %18, %4\n\t"
    "v_and_b32 v34, %18, %7\n\t"
    "v_lshrrev_b32 v28, 4, %4\n\t"
    "v_lshrrev_b32 v35, 4, %7\n\t"
    "v_lshlrev_b32 v30, 6, %14\n\t"
    "v_lshlrev_b32 v37, 4, %14\n\t"
    "v_and_or_b32 v27, v29, %19, v27\n\t"
    "v_and_or_b32 v34, v36, %19, v34\n\t"
    "v_and_b32 v28, %18, v28\n\t"
    "v_and_b32 v35, %18, v35\n\t"
    "v_perm_b32 v16, v27, %2, %20\n\t"
    "v_perm_b32 v18, v34, %5, %20\n\t"
    "v_and_or_b32 v28, v30, %19, v28\n\t"
    "v_and_or_b32 v35, v37, %19, v35\n\t"
    "v_perm_b32 v17, v27, %2, %21\n\t"
    "v_perm_b32 v19, v34, %5, %21\n\t"
    "v_perm_b32 v20, v28, %3, %20\n\t"
    "v_perm_b32 v22, v35, %6, %20\n\t"
    "v_perm_b32 v21, v28, %3, %21\n\t"
    "v_perm_b32 v23, v35, %6, %21\n\t"
    "v_and_b32_dpp v0, %15, %16 quad_perm:[0,0,0,0] row_mask:0xf bank_mask:0xf\n\t"
    "v_and_b32_dpp v1, %15, %17 quad_perm:[0,0,0,0] row_mask:0xf bank_mask:0xf\n\t"
    "v_and_b32_dpp v2, %15, %16 quad_perm:[1,1,1,1] row_mask:0xf bank_mask:0xf\n\t"
    "v_and_b32_dpp v3, %15, %17 quad_perm:[1,1,1,1] row_mask:0xf bank_mask:0xf\n\t"
    "v_lshlrev_b32 v29, 3, %14\n\t"
    "v_lshlrev_b32 v36, 1, %14\n\t"
    "v_mfma_f32_16x16x32_bf16 %0, v[0:3], v[16:19], %0\n\t"
    "v_and_b32 v27, %18, %10\n\t"
    "v_and_b32 v34, %18, %13\n\t"
    "v_lshrrev_b32 v28, 4, %10\n\t"
    "v_mfma_f32_16x16x32_bf16 %1, v[0:3], v[20:23], %1\n\t"
    "v_lshrrev_b32 v35, 4, %13\n\t"
    "v_lshlrev_b32 v30, 2, %14\n\t"
    "v_and_or_b32 v34, v36, %19, v34\n\t"
    "v_and_or_b32 v27, v29, %19, v27\n\t"
    "v_and_b32 v35, %18, v35\n\t"
    "v_and_b32 v28, %18, v28\n\t"
    "v_perm_b32 v18, v34, %11, %20\n\t"
    "v_perm_b32 v16, v27, %8, %20\n\t"
    "v_and_or_b32 v35, %14, %19, v35\n\t"
    "v_and_or_b32 v28, v30, %19, v28\n\t"
    "v_perm_b32 v19, v34, %11, %21\n\t"
    "v_perm_b32 v17, v27, %8, %21\n\t"
    "v_perm_b32 v22, v35, %12, %20\n\t"
    "v_perm_b32 v20, v28, %9, %20\n\t"
    "v_perm_b32 v23, v35, %12, %21\n\t"
    "v_perm_b32 v21, v28, %9, %21\n\t"
    "v_and_b32_dpp v4, %15, %16 quad_perm:[2,2,2,2] row_mask:0xf bank_mask:0xf\n\t"
    "v_and_b32_dpp v5, %15, %17 quad_perm:[2,2,2,2] row_mask:0xf bank_mask:0xf\n\t"
    "v_and_b32_dpp v6, %15, %16 quad_perm:[3,3,3,3] row_mask:0xf bank_mask:0xf\n\t"
    "v_and_b32_dpp v7, %15, %17 quad_perm:[3,3,3,3] row_mask:0xf bank_mask:0xf\n\t"
    "s_nop 1\n\t"
    "v_mfma_f32_16x16x32_bf16 %0, v[4:7], v[16:19], %0\n\t"
    "v_mfma_f32_16x16x32_bf16 %1, v[4:7], v[20:23], %1\n\t"
    : "+v"(acc0), "+v"(acc1)
    : "v"(pk[25][0]), "v"(pk[25][1]), "v"(pk[25][2]), "v"(pk[25][3]), "v"(pk[25][4]), "v"(pk[25][5]), "v"(pk[25][6]), "v"(pk[25][7]), "v"(pk[25][8]), "v"(pk[25][9]), "v"(pk[25][10]), "v"(pk[25][11]), "v"(pk[25][12]), "v"(pp), "v"(mask0), "v"(mask1), "s"(0x0f0f0f0fu), "s"(0x80808080u), "s"(0x05010400u), "s"(0x07030602u)
    : "v0", "v1", "v2", "v3", "v4", "v5", "v6", "v7", "v8", "v9", "v10", "v11", "v12", "v13", "v14", "v15", "v16", "v17", "v18", "v19", "v20", "v21", "v22", "v23", "v24", "v25", "v26", "v27", "v28", "v29", "v30", "v31", "v32", "v33", "v34", "v35", "v36", "v37", "v38", "v39");
}
__device__ __forceinline__ void sk13(float& X, const u32 (&pk)[30][13], u32x4 q, u64 modd, u64 mhi) {
  asm volatile(
    "v_lshlrev_b32 v29, 7, %13\n\t"
    "v_lshlrev_b32 v36, 5, %13\n\t"
    "v_and_b32 v27, %17, %3\n\t"
    "v_and_b32 v34, %17, %6\n\t"
    "v_lshrrev_b32 v28, 4, %3\n\t"
    "v_lshrrev_b32 v35, 4, %6\n\t"
    "v_lshlrev_b32 v30, 6, %13\n\t"
    "v_lshlrev_b32 v37, 4, %13\n\t"
    "v_and_or_b32 v27, v29, %18, v27\n\t"
    "v_and_or_b32 v34, v36, %18, v34\n\t"
    "v_and_b32 v28, %17, v28\n\t"
    "v_and_b32 v35, %17, v35\n\t"
    "v_perm_b32 v16, v27, %1, %19\n\t"
    "v_perm_b32 v20, v34, %4, %19\n\t"
    "v_and_or_b32 v28, v30, %18, v28\n\t"
    "v_and_or_b32 v35, v37, %18, v35\n\t"
    "v_perm_b32 v17, v27, %1, %20\n\t"
    "v_perm_b32 v21, v34, %4, %20\n\t"
    "v_perm_b32 v18, v28, %2, %19\n\t"
    "v_perm_b32 v22, v35, %5, %19\n\t"
    "v_perm_b32 v19, v28, %2, %20\n\t"
    "v_perm_b32 v23, v35, %5, %20\n\t"
    "v_lshlrev_b32 v29, 3, %13\n\t"
    "v_mfma_f32_16x16x32_bf16 v[0:3], v[16:19], %14, 0\n\t"
    "v_lshlrev_b32 v36, 1, %13\n\t"
    "v_and_b32 v27, %17, %9\n\t"
    "v_mfma_f32_16x16x32_bf16 v[4:7], v[20:23], %14, 0\n\t"
    "v_and_b32 v34, %17, %12\n\t"
    "v_lshrrev_b32 v28, 4, %9\n\t"
    "v_lshrrev_b32 v35, 4, %12\n\t"
    "v_lshlrev_b32 v30, 2, %13\n\t"
    "v_and_or_b32 v34, v36, %18, v34\n\t"
    "v_and_or_b32 v27, v29, %18, v27\n\t"
    "v_and_b32 v35, %17, v35\n\t"
    "v_and_b32 v28, %17, v28\n\t"
    "v_perm_b32 v20, v34, %10, %19\n\t"
    "v_perm_b32 v16, v27, %7, %19\n\t"
    "v_and_or_b32 v35, %13, %18, v35\n\t"
    "v_and_or_b32 v28, v30, %18, v28\n\t"
    "v_perm_b32 v21, v34, %10, %20\n\t"
    "v_perm_b32 v17, v27, %7, %20\n\t"
    "v_perm_b32 v22, v35, %11, %19\n\t"
    "v_perm_b32 v18, v28, %8, %19\n\t"
    "v_perm_b32 v23, v35, %11, %20\n\t"
    "v_perm_b32 v19, v28, %8, %20\n\t"
    "v_add_f32_dpp v0, v1, v0 quad_perm:[1,1,1,1] row_mask:0xf bank_mask:0xf\n\t"
    "v_add_f32_dpp v5, v6, v5 quad_perm:[2,2,2,2] row_mask:0xf bank_mask:0xf\n\t"
    "v_mfma_f32_16x16x32_bf16 v[8:11], v[16:19], %14, 0\n\t"
    "v_add_f32_dpp v0, v2, v0 quad_perm:[2,2,2,2] row_mask:0xf bank_mask:0xf\n\t"
    "v_mfma_f32_16x16x32_bf16 v[12:15], v[20:23], %14, 0\n\t"
    "v_add_f32_dpp v5, v7, v5 quad_perm:[3,3,3,3] row_mask:0xf bank_mask:0xf\n\t"
    "v_add_f32_dpp v0, v3, v0 quad_perm:[3,3,3,3] row_mask:0xf bank_mask:0xf\n\t"
    "v_add_f32_dpp v5, v4, v5 quad_perm:[0,0,0,0] row_mask:0xf bank_mask:0xf\n\t"
    "v_cndmask_b32_e64 v0, v0, v5, %15\n\t"
    "s_nop 1\n\t"
    "v_add_f32_dpp v10, v11, v10 quad_perm:[3,3,3,3] row_mask:0xf bank_mask:0xf\n\t"
    "s_nop 0\n\t"
    "v_add_f32_dpp v15, v12, v15 quad_perm:[0,0,0,0] row_mask:0xf bank_mask:0xf\n\t"
    "v_add_f32_dpp v10, v8, v10 quad_perm:[0,0,0,0] row_mask:0xf bank_mask:0xf\n\t"
    "v_add_f32_dpp v15, v13, v15 quad_perm:[1,1,1,1] row_mask:0xf bank_mask:0xf\n\t"
    "v_add_f32_dpp v10, v9, v10 quad_perm:[1,1,1,1] row_mask:0xf bank_mask:0xf\n\t"
    "v_add_f32_dpp v15, v14, v15 quad_perm:[2,2,2,2] row_mask:0xf bank_mask:0xf\n\t"
    "v_cndmask_b32_e64 v10, v10, v15, %15\n\t"
    "v_cndmask_b32_e64 %0, v0, v10, %16\n\t"
    : "=&v"(X)
    : "v"(pk[26][0]), "v"(pk[26][1]), "v"(pk[26][2]), "v"(pk[26][3]), "v"(pk[26][4]), "v"(pk[26][5]), "v"(pk[26][6]), "v"(pk[26][7]), "v"(pk[26][8]), "v"(pk[26][9]), "v"(pk[26][10]), "v"(pk[26][11]), "v"(pk[26][12]), "v"(q), "s"(modd), "s"(mhi), "s"(0x0f0f0f0fu), "s"(0x80808080u), "s"(0x05010400u), "s"(0x07030602u)
    : "v0", "v1", "v2", "v3", "v4", "v5", "v6", "v7", "v8", "v9", "v10", "v11", "v12", "v13", "v14", "v15", "v16", "v17", "v18", "v19", "v20", "v21", "v22", "v23", "v24", "v25", "v26", "v27", "v28", "v29", "v30", "v31", "v32", "v33", "v34", "v35", "v36", "v37", "v38", "v39");
}
__device__ __forceinline__ void sv13(f32x4& acc0, f32x4& acc1, const u32 (&pk)[30][13], u32 pp, u32 mask0, u32 mask1) {
  asm volatile(
    "v_lshlrev_b32 v29, 7, %14\n\t"
    "v_lshlrev_b32 v36, 5, %14\n\t"
    "v_and_b32 v27, %18, %4\n\t"
    "v_and_b32 v34, %18, %7\n\t"
    "v_lshrrev_b32 v28, 4, %4\n\t"
    "v_lshrrev_b32 v35, 4, %7\n\t"
    "v_lshlrev_b32 v30, 6, %14\n\t"
    "v_lshlrev_b32 v37, 4, %14\n\t"
    "v_and_or_b32 v27, v29, %19, v27\n\t"
    "v_and_or_b32 v34, v36, %19, v34\n\t"
    "v_and_b32 v28, %18, v28\n\t"
    "v_and_b32 v35, %18, v35\n\t"
    "v_perm_b32 v16, v27, %2, %20\n\t"
    "v_perm_b32 v18, v34, %5, %20\n\t"
    "v_and_or_b32 v28, v30, %19, v28\n\t"
    "v_and_or_b32 v35, v37, %19, v35\n\t"
    "v_perm_b32 v17, v27, %2, %21\n\t"
    "v_perm_b32 v19, v34, %5, %21\n\t"
    "v_perm_b32 v20, v28, %3, %20\n\t"
    "v_perm_b32 v22, v35, %6, %20\n\t"
    "v_perm_b32 v21, v28, %3, %21\n\t"
    "v_perm_b32 v23, v35, %6, %21\n\t"
    "v_and_b32_dpp v0, %15, %16 quad_perm:[0,0,0,0] row_mask:0xf bank_mask:0xf\n\t"
    "v_and_b32_dpp v1, %15, %17 quad_perm:[0,0,0,0] row_mask:0xf bank_mask:0xf\n\t"
    "v_and_b32_dpp v2, %15, %16 quad_perm:[1,1,1,1] row_mask:0xf bank_mask:0xf\n\t"
    "v_and_b32_dpp v3, %15, %17 quad_perm:[1,1,1,1] row_mask:0xf bank_mask:0xf\n\t"
    "v_lshlrev_b32 v29, 3, %14\n\t"
    "v_lshlrev_b32 v36, 1, %14\n\t"
    "v_mfma_f32_16x16x32_bf16 %0, v[0:3], v[16:19], %0\n\t"
    "v_and_b32 v27, %18, %10\n\t"
    "v_and_b32 v34, %18, %13\n\t"
    "v_lshrrev_b32 v28, 4, %10\n\t"
    "v_mfma_f32_16x16x32_bf16 %1, v[0:3], v[20:23], %1\n\t"
    "v_lshrrev_b32 v35, 4, %13\n\t"
    "v_lshlrev_b32 v30, 2, %14\n\t"
    "v_and_or_b32 v34, v36, %19, v34\n\t"
    "v_and_or_b32 v27, v29, %19, v27\n\t"
    "v_and_b32 v35, %18, v35\n\t"
    "v_and_b32 v28, %18, v28\n\t"
    "v_perm_b32 v18, v34, %11, %20\n\t"
    "v_perm_b32 v16, v27, %8, %20\n\t"
    "v_and_or_b32 v35, %14, %19, v35\n\t"
    "v_and_or_b32 v28, v30, %19, v28\n\t"
    "v_perm_b32 v19, v34, %11, %21\n\t"
    "v_perm_b32 v17, v27, %8, %21\n\t"
    "v_perm_b32 v22, v35, %12, %20\n\t"
    "v_perm_b32 v20, v28, %9, %20\n\t"
    "v_perm_b32 v23, v35, %12, %21\n\t"
    "v_perm_b32 v21, v28, %9, %21\n\t"
    "v_and_b32_dpp v4, %15, %16 quad_perm:[2,2,2,2] row_mask:0xf bank_mask:0xf\n\t"
    "v_and_b32_dpp v5, %15, %17 quad_perm:[2,2,2,2] row_mask:0xf bank_mask:0xf\n\t"
    "v_and_b32_dpp v6, %15, %16 quad_perm:[3,3,3,3] row_mask:0xf bank_mask:0xf\n\t"
    "v_and_b32_dpp v7, %15, %17 quad_perm:[3,3,3,3] row_mask:0xf bank_mask:0xf\n\t"
    "s_nop 1\n\t"
    "v_mfma_f32_16x16x32_bf16 %0, v[4:7], v[16:19], %0\n\t"
    "v_mfma_f32_16x16x32_bf16 %1, v[4:7], v[20:23], %1\n\t"
    : "+v"(acc0), "+v"(acc1)
    : "v"(pk[27][0]), "v"(pk[27][1]), "v"(pk[27][2]), "v"(pk[27][3]), "v"(pk[27][4]), "v"(pk[27][5]), "v"(pk[27][6]), "v"(pk[27][7]), "v"(pk[27][8]), "v"(pk[27][9]), "v"(pk[27][10]), "v"(pk[27][11]), "v"(pk[27][12]), "v"(pp), "v"(mask0), "v"(mask1), "s"(0x0f0f0f0fu), "s"(0x80808080u), "s"(0x05010400u), "s"(0x07030602u)
    : "v0", "v1", "v2", "v3", "v4", "v5", "v6", "v7", "v8", "v9", "v10", "v11", "v12", "v13", "v14", "v15", "v16", "v17", "v18", "v19", "v20", "v21", "v22", "v23", "v24", "v25", "v26", "v27", "v28", "v29", "v30", "v31", "v32", "v33", "v34", "v35", "v36", "v37", "v38", "v39");
}
__device__ __forceinline__ void sk14(float& X, const u32 (&pk)[30][13], u32x4 q, u64 modd, u64 mhi) {
  asm volatile(
    "v_lshlrev_b32 v29, 7, %13\n\t"
    "v_lshlrev_b32 v36, 5, %13\n\t"
    "v_and_b32 v27, %17, %3\n\t"
    "v_and_b32 v34, %17, %6\n\t"
    "v_lshrrev_b32 v28, 4, %3\n\t"
    "v_lshrrev_b32 v35, 4, %6\n\t"
    "v_lshlrev_b32 v30, 6, %13\n\t"
    "v_lshlrev_b32 v37, 4, %13\n\t"
    "v_and_or_b32 v27, v29, %18, v27\n\t"
    "v_and_or_b32 v34, v36, %18, v34\n\t"
    "v_and_b32 v28, %17, v28\n\t"
    "v_and_b32 v35, %17, v35\n\t"
    "v_perm_b32 v16, v27, %1, %19\n\t"
    "v_perm_b32 v20, v34, %4, %19\n\t"
    "v_and_or_b32 v28, v30, %18, v28\n\t"
    "v_and_or_b32 v35, v37, %18, v35\n\t"
    "v_perm_b32 v17, v27, %1, %20\n\t"
    "v_perm_b32 v21, v34, %4, %20\n\t"
    "v_perm_b32 v18, v28, %2, %19\n\t"
    "v_perm_b32 v22, v35, %5, %19\n\t"
    "v_perm_b32 v19, v28, %2, %20\n\t"
    "v_perm_b32 v23, v35, %5, %20\n\t"
    "v_lshlrev_b32 v29, 3, %13\n\t"
    "v_mfma_f32_16x16x32_bf16 v[0:3], v[16:19], %14, 0\n\t"
    "v_lshlrev_b32 v36, 1, %13\n\t"
    "v_and_b32 v27, %17, %9\n\t"
    "v_mfma_f32_16x16x32_bf16 v[4:7], v[20:23], %14, 0\n\t"
    "v_and_b32 v34, %17, %12\n\t"
    "v_lshrrev_b32 v28, 4, %9\n\t"
    "v_lshrrev_b32 v35, 4, %12\n\t"
    "v_lshlrev_b32 v30, 2, %13\n\t"
    "v_and_or_b32 v34, v36, %18, v34\n\t"
    "v_and_or_b32 v27, v29, %18, v27\n\t"
    "v_and_b32 v35, %17, v35\n\t"
    "v_and_b32 v28, %17, v28\n\t"
    "v_perm_b32 v20, v34, %10, %19\n\t"
    "v_perm_b32 v16, v27, %7, %19\n\t"
    "v_and_or_b32 v35, %13, %18, v35\n\t"
    "v_and_or_b32 v28, v30, %18, v28\n\t"
    "v_perm_b32 v21, v34, %10, %20\n\t"
    "v_perm_b32 v17, v27, %7, %20\n\t"
    "v_perm_b32 v22, v35, %11, %19\n\t"
    "v_perm_b32 v18, v28, %8, %19\n\t"
    "v_perm_b32 v23, v35, %11, %20\n\t"
    "v_perm_b32 v19, v28, %8, %20\n\t"
    "v_add_f32_dpp v0, v1, v0 quad_perm:[1,1,1,1] row_mask:0xf bank_mask:0xf\n\t"
    "v_add_f32_dpp v5, v6, v5 quad_perm:[2,2,2,2] row_mask:0xf bank_mask:0xf\n\t"
    "v_mfma_f32_16x16x32_bf16 v[8:11], v[16:19], %14, 0\n\t"
    "v_add_f32_dpp v0, v2, v0 quad_perm:[2,2,2,2] row_mask:0xf bank_mask:0xf\n\t"
    "v_mfma_f32_16x16x32_bf16 v[12:15], v[20:23], %14, 0\n\t"
    "v_add_f32_dpp v5, v7, v5 quad_perm:[3,3,3,3] row_mask:0xf bank_mask:0xf\n\t"
    "v_add_f32_dpp v0, v3, v0 quad_perm:[3,3,3,3] row_mask:0xf bank_mask:0xf\n\t"
    "v_add_f32_dpp v5, v4, v5 quad_perm:[0,0,0,0] row_mask:0xf bank_mask:0xf\n\t"
    "v_cndmask_b32_e64 v0, v0, v5, %15\n\t"
    "s_nop 1\n\t"
    "v_add_f32_dpp v10, v11, v10 quad_perm:[3,3,3,3] row_mask:0xf bank_mask:0xf\n\t"
    "s_nop 0\n\t"
    "v_add_f32_dpp v15, v12, v15 quad_perm:[0,0,0,0] row_mask:0xf bank_mask:0xf\n\t"
    "v_add_f32_dpp v10, v8, v10 quad_perm:[0,0,0,0] row_mask:0xf bank_mask:0xf\n\t"
    "v_add_f32_dpp v15, v13, v15 quad_perm:[1,1,1,1] row_mask:0xf bank_mask:0xf\n\t"
    "v_add_f32_dpp v10, v9, v10 quad_perm:[1,1,1,1] row_mask:0xf bank_mask:0xf\n\t"
    "v_add_f32_dpp v15, v14, v15 quad_perm:[2,2,2,2] row_mask:0xf bank_mask:0xf\n\t"
    "v_cndmask_b32_e64 v10, v10, v15, %15\n\t"
    "v_cndmask_b32_e64 %0, v0, v10, %16\n\t"
    : "=&v"(X)
    : "v"(pk[28][0]), "v"(pk[28][1]), "v"(pk[28][2]), "v"(pk[28][3]), "v"(pk[28][4]), "v"(pk[28][5]), "v"(pk[28][6]), "v"(pk[28][7]), "v"(pk[28][8]), "v"(pk[28][9]), "v"(pk[28][10]), "v"(pk[28][11]), "v"(pk[28][12]), "v"(q), "s"(modd), "s"(mhi), "s"(0x0f0f0f0fu), "s"(0x80808080u), "s"(0x05010400u), "s"(0x07030602u)
    : "v0", "v1", "v2", "v3", "v4", "v5", "v6", "v7", "v8", "v9", "v10", "v11", "v12", "v13", "v14", "v15", "v16", "v17", "v18", "v19", "v20", "v21", "v22", "v23", "v24", "v25", "v26", "v27", "v28", "v29", "v30", "v31", "v32", "v33", "v34", "v35", "v36", "v37", "v38", "v39");
}
__device__ __forceinline__ void sv14(f32x4& acc0, f32x4& acc1, const u32 (&pk)[30][13], u32 pp, u32 mask0, u32 mask1) {
  asm volatile(
    "v_lshlrev_b32 v29, 7, %14\n\t"
    "v_lshlrev_b32 v36, 5, %14\n\t"
    "v_and_b32 v27, %18, %4\n\t"
    "v_and_b32 v34, %18, %7\n\t"
    "v_lshrrev_b32 v28, 4, %4\n\t"
    "v_lshrrev_b32 v35, 4, %7\n\t"
    "v_lshlrev_b32 v30, 6, %14\n\t"
    "v_lshlrev_b32 v37, 4, %14\n\t"
    "v_and_or_b32 v27, v29, %19, v27\n\t"
    "v_and_or_b32 v34, v36, %19, v34\n\t"
    "v_and_b32 v28, %18, v28\n\t"
    "v_and_b32 v35, %18, v35\n\t"
    "v_perm_b32 v16, v27, %2, %20\n\t"
    "v_perm_b32 v18, v34, %5, %20\n\t"
    "v_and_or_b32 v28, v30, %19, v28\n\t"
    "v_and_or_b32 v35, v37, %19, v35\n\t"
    "v_perm_b32 v17, v27, %2, %21\n\t"
    "v_perm_b32 v19, v34, %5, %21\n\t"
    "v_perm_b32 v20, v28, %3, %20\n\t"
    "v_perm_b32 v22, v35, %6, %20\n\t"
    "v_perm_b32 v21, v28, %3, %21\n\t"
    "v_perm_b32 v23, v35, %6, %21\n\t"
    "v_and_b32_dpp v0, %15, %16 quad_perm:[0,0,0,0] row_mask:0xf bank_mask:0xf\n\t"
    "v_and_b32_dpp v1, %15, %17 quad_perm:[0,0,0,0] row_mask:0xf bank_mask:0xf\n\t"
    "v_and_b32_dpp v2, %15, %16 quad_perm:[1,1,1,1] row_mask:0xf bank_mask:0xf\n\t"
    "v_and_b32_dpp v3, %15, %17 quad_perm:[1,1,1,1] row_mask:0xf bank_mask:0xf\n\t"
    "v_lshlrev_b32 v29, 3, %14\n\t"
    "v_lshlrev_b32 v36, 1, %14\n\t"
    "v_mfma_f32_16x16x32_bf16 %0, v[0:3], v[16:19], %0\n\t"
    "v_and_b32 v27, %18, %10\n\t"
    "v_and_b32 v34, %18, %13\n\t"
    "v_lshrrev_b32 v28, 4, %10\n\t"
    "v_mfma_f32_16x16x32_bf16 %1, v[0:3], v[20:23], %1\n\t"
    "v_lshrrev_b32 v35, 4, %13\n\t"
    "v_lshlrev_b32 v30, 2, %14\n\t"
    "v_and_or_b32 v34, v36, %19, v34\n\t"
    "v_and_or_b32 v27, v29, %19, v27\n\t"
    "v_and_b32 v35, %18, v35\n\t"
    "v_and_b32 v28, %18, v28\n\t"
    "v_perm_b32 v18, v34, %11, %20\n\t"
    "v_perm_b32 v16, v27, %8, %20\n\t"
    "v_and_or_b32 v35, %14, %19, v35\n\t"
    "v_and_or_b32 v28, v30, %19, v28\n\t"
    "v_perm_b32 v19, v34, %11, %21\n\t"
    "v_perm_b32 v17, v27, %8, %21\n\t"
    "v_perm_b32 v22, v35, %12, %20\n\t"
    "v_perm_b32 v20, v28, %9, %20\n\t"
    "v_perm_b32 v23, v35, %12, %21\n\t"
    "v_perm_b32 v21, v28, %9, %21\n\t"
    "v_and_b32_dpp v4, %15, %16 quad_perm:[2,2,2,2] row_mask:0xf bank_mask:0xf\n\t"
    "v_and_b32_dpp v5, %15, %17 quad_perm:[2,2,2,2] row_mask:0xf bank_mask:0xf\n\t"
    "v_and_b32_dpp v6, %15, %16 quad_perm:[3,3,3,3] row_mask:0xf bank_mask:0xf\n\t"
    "v_and_b32_dpp v7, %15, %17 quad_perm:[3,3,3,3] row_mask:0xf bank_mask:0xf\n\t"
    "s_nop 1\n\t"
    "v_mfma_f32_16x16x32_bf16 %0, v[4:7], v[16:19], %0\n\t"
    "v_mfma_f32_16x16x32_bf16 %1, v[4:7], v[20:23], %1\n\t"
    : "+v"(acc0), "+v"(acc1)
    : "v"(pk[29][0]), "v"(pk[29][1]), "v"(pk[29][2]), "v"(pk[29][3]), "v"(pk[29][4]), "v"(pk[29][5]), "v"(pk[29][6]), "v"(pk[29][7]), "v"(pk[29][8]), "v"(pk[29][9]), "v"(pk[29][10]), "v"(pk[29][11]), "v"(pk[29][12]), "v"(pp), "v"(mask0), "v"(mask1), "s"(0x0f0f0f0fu), "s"(0x80808080u), "s"(0x05010400u), "s"(0x07030602u)
    : "v0", "v1", "v2", "v3", "v4", "v5", "v6", "v7", "v8", "v9", "v10", "v11", "v12", "v13", "v14", "v15", "v16", "v17", "v18", "v19", "v20", "v21", "v22", "v23", "v24", "v25", "v26", "v27", "v28", "v29", "v30", "v31", "v32", "v33", "v34", "v35", "v36", "v37", "v38", "v39");
}

__device__ __forceinline__ void repack16(u32 (&o)[16], const u32x4 (&r)[4]) {
#pragma unroll
  for (int d = 0; d < 8; ++d) {
    u32 a = r[d >> 2][d & 3], b = r[2 + (d >> 2)][d & 3];
    o[2 * d] = __builtin_amdgcn_perm(b, a, 0x05040100u);
    o[2 * d + 1] = __builtin_amdgcn_perm(b, a, 0x07060302u);
  }
}
__device__ __forceinline__ float reduce16(const float (&v)[16], int lane) {
  float a[8], b[4];
#pragma unroll
  for (int j = 0; j < 8; ++j) a[j] = swap32_sum(v[j], v[j + 8]);
#pragma unroll
  for (int j = 0; j < 4; ++j) b[j] = swap16_sum(a[j], a[j + 4]);
#pragma unroll
  for (int j = 0; j < 4; ++j) b[j] += dppf<ROR8>(b[j]);
  float c0 = (lane & 8) ? b[2] : b[0], c1 = (lane & 8) ? b[3] : b[1];
  c0 += dppf<HMIR>(c0); c1 += dppf<HMIR>(c1);
  float d = (lane & 4) ? c1 : c0;
  d += dppf<XOR1>(d); d += dppf<XOR2>(d);
  return d;
}
__device__ __forceinline__ void gemv_a(u32 xp, const u32 (&wp)[16], float sh, int w, int lane, LAS float* red) {
  float v[16];
#pragma unroll
  for (int j = 0; j < 16; ++j) v[j] = dot2(wp[j], xp, 0.f);
  float d0 = bf_lo(xp) - sh, d1 = bf_hi(xp) - sh;
  float t = swap32_sum(d0 + d1, fmaf(d0, d0, d1 * d1));
  t = row16_sum(t); t = swap16_sum(t, t);
  float r = reduce16(v, lane);
  const bool main = (lane & 3) == 0;
  if (main || (lane & 31) == 1) red[w * 20 + (main ? lane >> 2 : 16 + (lane >> 5))] = main ? r : t;
}
__device__ __forceinline__ void colsum_a(const u32 (&wp)[16], int w, int lane, LAS float* cs) {
  float v[16];
#pragma unroll
  for (int j = 0; j < 16; ++j) v[j] = dot2(wp[j], 0x3f803f80u, 0.f);
  float r = reduce16(v, lane);
  if ((lane & 3) == 0) cs[w * 16 + (lane >> 2)] = r;
}
__device__ __forceinline__ f32x2 sum4(LAS const float* p, int stride) {
  f32x2 a = *(LAS const f32x2*)p, b = *(LAS const f32x2*)(p + stride), c = *(LAS const f32x2*)(p + 2 * stride), d = *(LAS const f32x2*)(p + 3 * stride);
  return (a + b) + (c + d);
}
__device__ __forceinline__ void ln_stats(LAS const float* red, float sh, float& mean, float& rstd) {
  f32x2 s = sum4(red + 16, 20);
  float md = s[0] * (1.f / 512.f);
  float var = fmaxf(fmaf(s[1], 1.f / 512.f, -md * md), 0.f);
  rstd = __builtin_amdgcn_rsqf(var + 1e-5f);
  mean = sh + md;
}
__device__ __forceinline__ u32 poll_pair(u32 voff, char* ws, u32 tag) {
  u32x2 g;
  for (u32 sp = 0; sp < SPIN_CAP; ++sp) {
    asm volatile("global_load_dwordx2 %0, %1, %2 sc1\n\ts_waitcnt vmcnt(0)" : "=&v"(g) : "v"(voff), "s"(ws) : "memory");
    if (__all(g[1] == tag)) break;
  }
  return g[0];
}
__device__ __forceinline__ u32x2 pack24(float y0, float y1, u32 tag) {
  u32 r0 = __float_as_uint(y0) + 0x80u, r1 = __float_as_uint(y1) + 0x80u;
  return u32x2{(r0 & 0xffffff00u) | (r1 >> 24), ((r1 << 8) & 0xffff0000u) | tag};
}
__device__ __forceinline__ float unp0(u32 w0) { return __uint_as_float(w0 & 0xffffff00u); }
__device__ __forceinline__ float unp1(u32 w0, u32 w1) { return __uint_as_float((w0 << 24) | ((w1 >> 8) & 0x00ffff00u)); }
__device__ __forceinline__ void poll_word(u32 voff, char* ws, u32 tag, int w) {
  if (w == 0) {
    for (u32 sp = 0; sp < SPIN_CAP; ++sp) {
      u32 v;
      asm volatile("global_load_dword %0, %1, %2 sc1\n\ts_waitcnt vmcnt(0)" : "=&v"(v) : "v"(voff), "s"(ws) : "memory");
      if (__all(v == tag)) break;
    }
  }
  lds_barrier();
}
__device__ __forceinline__ void reduce_inbox(u32 base, int nprod, char* ws, u32 tag, int w, int lane, LAS float* red, u32& p0, u32& p1) {
  const bool live = w * 64 + lane < nprod;
  u32x4 g;
  for (u32 sp = 0; sp < SPIN_CAP; ++sp) {
    asm volatile("global_load_dwordx4 %0, %1, %2 sc1\n\ts_waitcnt vmcnt(0)" : "=&v"(g) : "v"(base + (w * 64 + lane) * 16), "s"(ws) : "memory");
    if (__all(!live || ((g[1] & 0xffffu) == tag && (g[3] & 0xffffu) == tag))) break;
    __builtin_amdgcn_s_sleep(2);
  }
  float v0 = live ? unp0(g[0]) : 0.f, v1 = live ? unp1(g[0], g[1]) : 0.f, v2 = live ? unp0(g[2]) : 0.f, v3 = live ? unp1(g[2], g[3]) : 0.f;
  float a = swap32_sum(v0, v2), b = swap32_sum(v1, v3);
  float t = swap16_sum(a, b);
  t = row16_sum(t);
  if ((lane & 15) == 0) red[w * 4 + (lane >> 4)] = t;
  lds_barrier();
  f32x4 r0 = *(LAS f32x4*)(red), r1 = *(LAS f32x4*)(red + 4), r2 = *(LAS f32x4*)(red + 8), r3 = *(LAS f32x4*)(red + 12);
  f32x4 y = (r0 + r1) + (r2 + r3);
  u32 n0 = pk_bf16(bf_lo(p0) + rbf(y[0]), bf_hi(p0) + rbf(y[1])), n1 = pk_bf16(bf_lo(p1) + rbf(y[2]), bf_hi(p1) + rbf(y[3]));
  p0 = __builtin_amdgcn_readfirstlane(n0); p1 = __builtin_amdgcn_readfirstlane(n1);
}
__device__ __forceinline__ void reduce_q(const float (&v)[16], int w, int lane, LAS float* red) {
  float a[8]; f32x4 b;
#pragma unroll
  for (int j = 0; j < 8; ++j) a[j] = swap32_sum(v[j], v[j + 8]);
#pragma unroll
  for (int j = 0; j < 4; ++j) { float x = swap16_sum(a[j], a[j + 4]); x += dppf<ROR8>(x); x += dppf<0x124>(x); b[j] = x; }
  if ((lane & 12) == 0) *(LAS f32x4*)(red + w * 64 + (lane & 32) + (lane & 3) * 8 + ((lane >> 4) & 1) * 4) = b;
}
__device__ __forceinline__ void qproj_a(u32 xs, u32 xg, const u32 (&wp)[16], float sh, int w, int lane, LAS float* red) {
  float v[16];
#pragma unroll
  for (int j = 0; j < 16; ++j) v[j] = dot2(wp[j], xg, 0.f);
  float d0 = bf_lo(xs) - sh, d1 = bf_hi(xs) - sh;
  float t = swap32_sum(d0 + d1, fmaf(d0, d0, d1 * d1));
  t = row16_sum(t); t = swap16_sum(t, t);
  reduce_q(v, w, lane, red);
  if ((lane & 31) == 4) red[256 + 2 * w + (lane >> 5)] = t;
}
__device__ __forceinline__ void qcolsum_a(const u32 (&wp)[16], int w, int lane, LAS float* cs) {
  float v[16];
#pragma unroll
  for (int j = 0; j < 16; ++j) v[j] = dot2(wp[j], 0x3f803f80u, 0.f);
  reduce_q(v, w, lane, cs);
}
__device__ __forceinline__ u32 q_slot(int rb, int cb, int j) {
  const int chunk = 8 * (cb & 1) + (j >> 2), head = (cb >> 1) & 3, grp = cb >> 3;
  return rb * 16384 + grp * 2048 + ((j >> 1) & 1) * 1024 + ((chunk >> 2) * 16 + head * 4 + (chunk & 3)) * 16 + (j & 1) * 8;
}
__device__ __forceinline__ void qproj_b(int w, int lane, LAS float* red, f32x2 cq, float sh, u32 dst, u32 tag, char* ws) {
  if (w == 0 && lane < 32) {
    f32x2 s = sum4(red + 256, 2), d = sum4(red + 2 * lane, 64);
    float md = s[0] * (1.f / 512.f);
    float rstd = __builtin_amdgcn_rsqf(fmaxf(fmaf(s[1], 1.f / 512.f, -md * md), 0.f) + 1e-5f), mean = sh + md;
    st8_sc1(dst, pack24(rstd * fmaf(-mean, cq[0], d[0]), rstd * fmaf(-mean, cq[1], d[1]), tag), ws);
  }
}
__device__ __forceinline__ bool q_ok(const u32x4 (&g)[8], u32 tag) {
  u32 bad = 0;
#pragma unroll
  for (int i = 0; i < 8; ++i) bad |= (g[i][1] ^ tag) | (g[i][3] ^ tag);
  return (bad & 0xffffu) == 0;
}
__device__ __forceinline__ void q_load(u32x4 (&g)[8], u32 voff, char* ws) {
  const u32 v1 = voff + 16384, v2 = voff + 32768, v3 = voff + 49152;
  asm volatile("global_load_dwordx4 %0, %8, %12 sc1\n\tglobal_load_dwordx4 %1, %8, %12 offset:1024 sc1\n\t"
               "global_load_dwordx4 %2, %9, %12 sc1\n\tglobal_load_dwordx4 %3, %9, %12 offset:1024 sc1\n\t"
               "global_load_dwordx4 %4, %10, %12 sc1\n\tglobal_load_dwordx4 %5, %10, %12 offset:1024 sc1\n\t"
               "global_load_dwordx4 %6, %11, %12 sc1\n\tglobal_load_dwordx4 %7, %11, %12 offset:1024 sc1\n\ts_waitcnt vmcnt(0)"
               : "=&v"(g[0]), "=&v"(g[1]), "=&v"(g[2]), "=&v"(g[3]), "=&v"(g[4]), "=&v"(g[5]), "=&v"(g[6]), "=&v"(g[7])
               : "v"(voff), "v"(v1), "v"(v2), "v"(v3), "s"(ws) : "memory");
}
__device__ __forceinline__ u32x4 q_sum(const u32x4 (&g)[8]) {
  u32x4 q;
#pragma unroll
  for (int h = 0; h < 2; ++h)
#pragma unroll
    for (int k = 0; k < 2; ++k) {
      float a = (unp0(g[h][2 * k]) + unp0(g[2 + h][2 * k])) + (unp0(g[4 + h][2 * k]) + unp0(g[6 + h][2 * k]));
      float b = (unp1(g[h][2 * k], g[h][2 * k + 1]) + unp1(g[2 + h][2 * k], g[2 + h][2 * k + 1])) + (unp1(g[4 + h][2 * k], g[4 + h][2 * k + 1]) + unp1(g[6 + h][2 * k], g[6 + h][2 * k + 1]));
      q[2 * h + k] = pk_bf16(a, b);
    }
  return q;
}
__device__ __forceinline__ void poll_pair2(u32& xs, u32& xg, u32 va, u32 vb, char* ws, u32 tag) {
  u32x2 a, b;
  for (u32 sp = 0; sp < SPIN_CAP; ++sp) {
    asm volatile("global_load_dwordx2 %0, %2, %4 sc1\n\tglobal_load_dwordx2 %1, %3, %4 sc1\n\ts_waitcnt vmcnt(0)" : "=&v"(a), "=&v"(b) : "v"(va), "v"(vb), "s"(ws) : "memory");
    if (__all(a[1] == tag && b[1] == tag)) break;
  }
  xs = a[0]; xg = b[0];
}
__device__ __forceinline__ bool flags_ok(u32x4 a, u32x4 b, u64 tagR) {
  u32 lo = (u32)tagR, hi = (u32)(tagR >> 32);
  return a[0] == lo && a[1] == hi && a[2] == lo && a[3] == hi && b[0] == lo && b[1] == hi && b[2] == lo && b[3] == hi;
}
__device__ __forceinline__ void flags_retry(u32x4& a, u32x4& b, u64 tagR, int lane, char* ws) {
  for (u32 sp = 0; !__all(flags_ok(a, b, tagR)) && sp < SPIN_CAP; ++sp) {
    asm volatile("global_load_dwordx4 %0, %2, %3 sc1\n\tglobal_load_dwordx4 %1, %2, %3 offset:1024 sc1\n\ts_waitcnt vmcnt(0)"
                 : "=&v"(a), "=&v"(b) : "v"(OFF_R + lane * 16), "s"(ws) : "memory");
  }
}
__global__ void __launch_bounds__(256, 1) mega(Params p) {
  __shared__ __attribute__((aligned(16))) char lds_raw[163840];
  LAS char* lds = (LAS char*)lds_raw;
  char* ws = p.ws;
  const int c = blockIdx.x, tid = threadIdx.x, lane = tid & 63, w = __builtin_amdgcn_readfirstlane(tid >> 6);
  const int gam = c >> 5, s = c & 31, cl = c;
  const int tau = (c & 7) * 28 + (c >> 3);
  const bool ffn_on = (c >> 3) < 28;
  STAMP(0);
  const u32 tp = now32();
  const char* pc0; asm volatile("s_getpc_b64 %0" : "=s"(pc0));
  u32 cpf = 0;
  auto code_prefetch = [&](u32 skip) {
    u32 line = (((c >> 3) * 4 + w) * 8 + (lane_id() & 7)) * 128u;
    line = line < CODE_BYTES ? line : 0u;
    asm volatile("s_cmp_lg_u32 %3, 0\n\ts_cbranch_scc1 .Lcp%=\n\tglobal_load_dword %0, %1, %2\n.Lcp%=:" : "+v"(cpf) : "v"(line), "s"(pc0), "s"(skip) : "memory", "scc");
  };
  u64 E, tagR; u32 x00, xo0, xo1; float sh;
  asm volatile("s_load_dwordx2 %0, %4, 0\n\ts_load_dword %1, %5, 0\n\ts_load_dword %2, %6, 0\n\ts_load_dword %3, %6, 4"
               : "=&s"(E), "=&s"(x00), "=&s"(xo0), "=&s"(xo1) : "s"(ws), "s"(p.x), "s"(p.x + (c & 127) * 8) : "memory");
#define SCALARS_READY() do { asm volatile("s_waitcnt lgkmcnt(0)" : "+s"(E), "+s"(x00), "+s"(xo0), "+s"(xo1) : : "memory"); \
    tagR = E + 0x9e3779b97f4a7c15ull; tagR = (tagR ^ (tagR >> 30)) * 0xbf58476d1ce4e5b9ull; tagR = (tagR ^ (tagR >> 27)) * 0x94d049bb133111ebull; tagR ^= tagR >> 31; \
    sh = bf_lo(x00); } while (0)
  const int gph = ((c >> 3) + 1 * w) % 4;

  LAS float* obuf = (LAS float*)(lds + SCR);
  LAS float* mlbuf = (LAS float*)(lds + SCR + 8192);
  LAS float* red_q = (LAS float*)(lds + SCR + 8192 + 256);
  LAS float* red_o = red_q + 272;
  LAS float* red_g = red_o + 16;
  LAS float* red_d = red_g + 80;
  LAS float* cs_q = red_d + 16;
  LAS float* cs_g = cs_q + 256;
  LAS float* red_h = cs_g + 64;
  LAS u32x4* wq_lds = (LAS u32x4*)(lds + SCR + 12288);

  const u32 koff = 256 * ((lane >> 2) & 3) + 64 * (lane >> 4) + 16 * (lane & 3), voff = lane * 16, vofr = voff;
  const char* kbase = p.K + ((size_t)gam << 24) + s * 524288 + w * 4096;
  const char* vbase = p.V + ((size_t)gam << 24) + s * 524288 + w * 4096;
  const u32 ring = (u32)(uintptr_t)(lds + w * RING_BYTES);

  const int k0 = 2 * tid;
  const int rb = c >> 6, cb = c & 63;
  const u32 off_q = OFF_G6 + gam * 2048 + lane * 16;
  const u32 off_wq = (128 * rb + 2 * (tid >> 2)) * 8192 + cb * 128 + (tid & 3) * 16;
  const u32 off_gu = k0 * 3584 + (ffn_on ? tau : 0) * 16;
  const u32 off_wo = cl * 16384 + tid * 4;
  const u32 off_wd = (ffn_on ? tau : 0) * 8192 + tid * 4;
  const bool dest = c < 128;
  auto small_clears = [&]() {
    const u32 off_clr = lane < 32 ? OFF_G6 + q_slot(rb, cb, lane)
                      : lane < 34 ? OFF_G3 + (c & 127) * XS + (lane - 32) * 8
                      : lane < 36 ? OFF_G5 + (c & 127) * XS + (lane - 34) * 8
                      : OFF_SX + (c & 127) * 128;
    const bool clr_on = lane < 32 || (lane < 37 && dest);
    if (w == 0 && clr_on) st8_sc1(off_clr, u32x2{0u, 0u}, ws);
  };
  auto issue_half = [&](const char* sb, u32 lo, u32 dst) {
    asm volatile("s_mov_b32 m0, %2\n\ts_nop 0\n\t"
                 "global_load_lds_dwordx4 %0, %1 nt\n\tglobal_load_lds_dwordx4 %0, %1 offset:1024 nt\n\t"
                 "global_load_lds_dwordx4 %0, %1 offset:2048 nt\n\tglobal_load_lds_dwordx4 %0, %1 offset:3072 nt"
                 : : "v"(lo), "s"(sb), "s"(dst) : "memory");
  };

  auto q_dma = [&]() {
    asm volatile("s_mov_b32 m0, %2\n\ts_nop 0\n\tglobal_load_lds_dwordx4 %0, %1 sc1\n\tglobal_load_lds_dwordx4 %0, %1 offset:1024 sc1"
                 : : "v"(off_q + w * 16384), "s"(ws), "s"((u32)(uintptr_t)obuf + w * 2048) : "memory");
  };
  u32 pk[2 * NS][13];
  u32 badv = 0, badmask = 0, base2k, base2v; float sck, scv;
  const u32 laddr = ring + voff, laddr8 = laddr + 8;
  auto pick_base = [&](LAS const char* src, u32& base2, float& sc, int qshift) {
    u32 mx = 0;
#pragma unroll
    for (int i = 0; i < 4; ++i) {
      u32x4 v = *(LAS const u32x4*)(src + i * 1024);
#pragma unroll
      for (int k = 0; k < 4; ++k) { u32 a = (v[k] >> 8) & 0x7fu, b = (v[k] >> 24) & 0x7fu; mx = max(mx, max(a, b)); }
    }
    float f = rows_max(row16_max((float)mx));
    int base = __builtin_amdgcn_readfirstlane((int)f) + MARGIN - 15;
    base = base < 8 ? 8 : base > 63 ? 63 : base;
    base2 = (u32)base * 0x01000100u;
    sc = __uint_as_float((u32)(127 + 2 * base - qshift) << 23);
  };
  u32 xr, xr2;
  u32x4 wqr[4], fl0, fl1, qf;
  u32x4 gur[4] = {{0u, 0u, 0u, 0u}, {0u, 0u, 0u, 0u}, {0u, 0u, 0u, 0u}, {0u, 0u, 0u, 0u}};
  u32 wor[16], wdr[8];

  u32 gu_p[16], wo_p[16], wd_p[8];
  auto repack_weights = [&]() {
    u32x4 t[4] = {gur[0], gur[1], gur[2], gur[3]};
    repack16(gu_p, t);
    colsum_a(gu_p, w, lane, cs_g);
#pragma unroll
    for (int j = 0; j < 8; ++j) {
      wo_p[2 * j] = __builtin_amdgcn_perm(wor[2 * j + 1], wor[2 * j], 0x05040100u);
      wo_p[2 * j + 1] = __builtin_amdgcn_perm(wor[2 * j + 1], wor[2 * j], 0x07060302u);
    }
#pragma unroll
    for (int j = 0; j < 4; ++j) {
      wd_p[2 * j] = __builtin_amdgcn_perm(wdr[2 * j + 1], wdr[2 * j], 0x05040100u);
      wd_p[2 * j + 1] = __builtin_amdgcn_perm(wdr[2 * j + 1], wdr[2 * j], 0x07060302u);
    }
  };
  code_prefetch(0u);
  ld4(xr, tid * 4, p.x);
  ld4(xr2, (64 * rb + (tid >> 2)) * 4, p.x);
  ld16(wqr[0], off_wq, p.wq, 0);
  ld16(wqr[1], off_wq, p.wq, 64);
  ld16(wqr[2], off_wq + 8192, p.wq, 0);
  ld16(wqr[3], off_wq + 8192, p.wq, 64);
  st16_sc1(OFF_G1 + c * 4096 + tid * 16, u32x4{0u, 0u, 0u, 0u}, ws);
  st16_sc1(OFF_G2 + c * 4096 + tid * 16, u32x4{0u, 0u, 0u, 0u}, ws);
  small_clears();
  issue_half(kbase, koff ^ 0u, ring + 0);
  issue_half(vbase, vofr ^ 0u, ring + 4096);
  issue_half(kbase, koff ^ 16384u, ring + 8192);
  issue_half(vbase, vofr ^ 16384u, ring + 12288);
  pace(tp, 122);
  asm volatile("s_waitcnt vmcnt(12)" ::: "memory");
  pick_base(lds + w * RING_BYTES + 0 + voff, base2k, sck, 64);
  asm volatile(
    "s_waitcnt vmcnt(12)\n\t"
    "ds_read_b128 v[16:19], %14 offset:0\n\t"
    "ds_read_b128 v[20:23], %14 offset:1024\n\t"
    "ds_read_b128 v[24:27], %14 offset:2048\n\t"
    "ds_read_b128 v[28:31], %14 offset:3072\n\t"
    "v_xor_b32 v15, 32768, %20\n\t"
    "s_add_u32 m0, %22, 16384\n\t"
    "s_nop 0\n\t"
    "global_load_lds_dwordx4 v15, %21 nt\n\t"
    "global_load_lds_dwordx4 v15, %21 offset:1024 nt\n\t"
    "global_load_lds_dwordx4 v15, %21 offset:2048 nt\n\t"
    "global_load_lds_dwordx4 v15, %21 offset:3072 nt\n\t"
    "v_mov_b32 v32, -1\n\t"
    "v_mov_b32 v33, 0\n\t"
    "s_waitcnt lgkmcnt(2)\n\t"
    "v_perm_b32 v4, v17, v16, %16\n\t"
    "v_perm_b32 v11, v21, v20, %16\n\t"
    "v_perm_b32 v0, v17, v16, %17\n\t"
    "v_perm_b32 v7, v21, v20, %17\n\t"
    "v_accvgpr_write_b32 %0, v4\n\t"
    "v_accvgpr_write_b32 %3, v11\n\t"
    "v_perm_b32 v5, v19, v18, %16\n\t"
    "v_perm_b32 v12, v23, v22, %16\n\t"
    "v_perm_b32 v1, v19, v18, %17\n\t"
    "v_perm_b32 v8, v23, v22, %17\n\t"
    "v_accvgpr_write_b32 %1, v5\n\t"
    "v_accvgpr_write_b32 %4, v12\n\t"
    "v_and_b32 v16, 0x7fff7fff, v16\n\t"
    "v_and_b32 v20, 0x7fff7fff, v20\n\t"
    "v_pk_add_u16 v2, v16, -1\n\t"
    "v_pk_add_u16 v9, v20, -1\n\t"
    "v_pk_sub_u16 v16, v16, %15 clamp\n\t"
    "v_pk_sub_u16 v20, v20, %15 clamp\n\t"
    "v_pk_min_u16 v32, v32, v2\n\t"
    "v_pk_min_u16 v32, v32, v9\n\t"
    "v_and_b32 v17, 0x7fff7fff, v17\n\t"
    "v_and_b32 v21, 0x7fff7fff, v21\n\t"
    "v_pk_add_u16 v3, v17, -1\n\t"
    "v_pk_add_u16 v10, v21, -1\n\t"
    "v_pk_sub_u16 v17, v17, %15 clamp\n\t"
    "v_pk_sub_u16 v21, v21, %15 clamp\n\t"
    "v_pk_min_u16 v32, v32, v3\n\t"
    "v_pk_min_u16 v32, v32, v10\n\t"
    "v_and_b32 v18, 0x7fff7fff, v18\n\t"
    "v_and_b32 v22, 0x7fff7fff, v22\n\t"
    "v_pk_add_u16 v2, v18, -1\n\t"
    "v_pk_add_u16 v9, v22, -1\n\t"
    "v_pk_sub_u16 v18, v18, %15 clamp\n\t"
    "v_pk_sub_u16 v22, v22, %15 clamp\n\t"
    "v_pk_min_u16 v32, v32, v2\n\t"
    "v_pk_min_u16 v32, v32, v9\n\t"
    "v_and_b32 v19, 0x7fff7fff, v19\n\t"
    "v_and_b32 v23, 0x7fff7fff, v23\n\t"
    "v_pk_add_u16 v3, v19, -1\n\t"
    "v_pk_add_u16 v10, v23, -1\n\t"
    "v_pk_sub_u16 v19, v19, %15 clamp\n\t"
    "v_pk_sub_u16 v23, v23, %15 clamp\n\t"
    "v_pk_min_u16 v32, v32, v3\n\t"
    "v_pk_min_u16 v32, v32, v10\n\t"
    "v_perm_b32 v2, v17, v16, %17\n\t"
    "v_perm_b32 v9, v21, v20, %17\n\t"
    "v_perm_b32 v3, v19, v18, %17\n\t"
    "v_perm_b32 v10, v23, v22, %17\n\t"
    "v_or3_b32 v33, v33, v2, v3\n\t"
    "v_or3_b32 v33, v33, v9, v10\n\t"
    "v_lshl_or_b32 v6, v3, 4, v2\n\t"
    "v_lshl_or_b32 v13, v10, 4, v9\n\t"
    "v_accvgpr_write_b32 %2, v6\n\t"
    "v_accvgpr_write_b32 %5, v13\n\t"
    "v_and_b32 v14, %19, v0\n\t"
    "v_lshrrev_b32 v14, 1, v14\n\t"
    "v_and_or_b32 v14, v1, %19, v14\n\t"
    "v_lshrrev_b32 v14, 1, v14\n\t"
    "v_and_or_b32 v14, v7, %19, v14\n\t"
    "v_lshrrev_b32 v14, 1, v14\n\t"
    "v_and_or_b32 v14, v8, %19, v14\n\t"
    "s_waitcnt lgkmcnt(0)\n\t"
    "v_perm_b32 v4, v25, v24, %16\n\t"
    "v_perm_b32 v11, v29, v28, %16\n\t"
    "v_perm_b32 v0, v25, v24, %17\n\t"
    "v_perm_b32 v7, v29, v28, %17\n\t"
    "v_accvgpr_write_b32 %6, v4\n\t"
    "v_accvgpr_write_b32 %9, v11\n\t"
    "v_perm_b32 v5, v27, v26, %16\n\t"
    "v_perm_b32 v12, v31, v30, %16\n\t"
    "v_perm_b32 v1, v27, v26, %17\n\t"
    "v_perm_b32 v8, v31, v30, %17\n\t"
    "v_accvgpr_write_b32 %7, v5\n\t"
    "v_accvgpr_write_b32 %10, v12\n\t"
    "v_and_b32 v24, 0x7fff7fff, v24\n\t"
    "v_and_b32 v28, 0x7fff7fff, v28\n\t"
    "v_pk_add_u16 v2, v24, -1\n\t"
    "v_pk_add_u16 v9, v28, -1\n\t"
    "v_pk_sub_u16 v24, v24, %15 clamp\n\t"
    "v_pk_sub_u16 v28, v28, %15 clamp\n\t"
    "v_pk_min_u16 v32, v32, v2\n\t"
    "v_pk_min_u16 v32, v32, v9\n\t"
    "v_and_b32 v25, 0x7fff7fff, v25\n\t"
    "v_and_b32 v29, 0x7fff7fff, v29\n\t"
    "v_pk_add_u16 v3, v25, -1\n\t"
    "v_pk_add_u16 v10, v29, -1\n\t"
    "v_pk_sub_u16 v25, v25, %15 clamp\n\t"
    "v_pk_sub_u16 v29, v29, %15 clamp\n\t"
    "v_pk_min_u16 v32, v32, v3\n\t"
    "v_pk_min_u16 v32, v32, v10\n\t"
    "v_and_b32 v26, 0x7fff7fff, v26\n\t"
    "v_and_b32 v30, 0x7fff7fff, v30\n\t"
    "v_pk_add_u16 v2, v26, -1\n\t"
    "v_pk_add_u16 v9, v30, -1\n\t"
    "v_pk_sub_u16 v26, v26, %15 clamp\n\t"
    "v_pk_sub_u16 v30, v30, %15 clamp\n\t"
    "v_pk_min_u16 v32, v32, v2\n\t"
    "v_pk_min_u16 v32, v32, v9\n\t"
    "v_and_b32 v27, 0x7fff7fff, v27\n\t"
    "v_and_b32 v31, 0x7fff7fff, v31\n\t"
    "v_pk_add_u16 v3, v27, -1\n\t"
    "v_pk_add_u16 v10, v31, -1\n\t"
    "v_pk_sub_u16 v27, v27, %15 clamp\n\t"
    "v_pk_sub_u16 v31, v31, %15 clamp\n\t"
    "v_pk_min_u16 v32, v32, v3\n\t"
    "v_pk_min_u16 v32, v32, v10\n\t"
    "v_perm_b32 v2, v25, v24, %17\n\t"
    "v_perm_b32 v9, v29, v28, %17\n\t"
    "v_perm_b32 v3, v27, v26, %17\n\t"
    "v_perm_b32 v10, v31, v30, %17\n\t"
    "v_or3_b32 v33, v33, v2, v3\n\t"
    "v_or3_b32 v33, v33, v9, v10\n\t"
    "v_lshl_or_b32 v6, v3, 4, v2\n\t"
    "v_lshl_or_b32 v13, v10, 4, v9\n\t"
    "v_accvgpr_write_b32 %8, v6\n\t"
    "v_accvgpr_write_b32 %11, v13\n\t"
    "v_lshrrev_b32 v14, 1, v14\n\t"
    "v_and_or_b32 v14, v0, %19, v14\n\t"
    "v_lshrrev_b32 v14, 1, v14\n\t"
    "v_and_or_b32 v14, v1, %19, v14\n\t"
    "v_lshrrev_b32 v14, 1, v14\n\t"
    "v_and_or_b32 v14, v7, %19, v14\n\t"
    "v_lshrrev_b32 v14, 1, v14\n\t"
    "v_and_or_b32 v14, v8, %19, v14\n\t"
    "v_accvgpr_write_b32 %12, v14\n\t"
    "v_pk_sub_u16 v32, %18, v32 clamp\n\t"
    "v_and_b32 v33, 0xf0f0f0f0, v33\n\t"
    "v_or3_b32 %13, %13, v32, v33\n\t"
    : "=&a"(pk[0][0]), "=&a"(pk[0][1]), "=&a"(pk[0][2]), "=&a"(pk[0][3]), "=&a"(pk[0][4]), "=&a"(pk[0][5]), "=&a"(pk[0][6]), "=&a"(pk[0][7]), "=&a"(pk[0][8]), "=&a"(pk[0][9]), "=&a"(pk[0][10]), "=&a"(pk[0][11]), "=&a"(pk[0][12]), "+v"(badv)
    : "v"(laddr), "s"(base2k), "s"(0x06040200u), "s"(0x07050301u), "s"(base2k + 0x00ff00ffu), "s"(0x80808080u), "v"(koff), "s"(kbase), "s"(ring)
    : "memory", "scc", "v0", "v1", "v2", "v3", "v4", "v5", "v6", "v7", "v8", "v9", "v10", "v11", "v12", "v13", "v14", "v15", "v16", "v17", "v18", "v19", "v20", "v21", "v22", "v23", "v24", "v25", "v26", "v27", "v28", "v29", "v30", "v31", "v32", "v33");
  asm volatile("s_waitcnt vmcnt(20)" : "+v"(cpf), "+v"(xr), "+v"(xr2), "+v"(wqr[0]), "+v"(wqr[1]), "+v"(wqr[2]), "+v"(wqr[3]) : : "memory");
  STAMP(1);
  SCALARS_READY();
  { u32 wq_p[16]; repack16(wq_p, wqr);
    for (int j = 0; j < 4; ++j) wq_lds[j * 256 + tid] = u32x4{wq_p[4 * j], wq_p[4 * j + 1], wq_p[4 * j + 2], wq_p[4 * j + 3]};
    qproj_a(xr, xr2, wq_p, sh, w, lane, red_q); qcolsum_a(wq_p, w, lane, cs_q); }
  STAMP(2);
  pace(tp, 213);
  asm volatile("s_waitcnt vmcnt(12)" ::: "memory");
  pick_base(lds + w * RING_BYTES + 4096 + voff, base2v, scv, 0);
  asm volatile(
    "s_waitcnt vmcnt(12)\n\t"
    "ds_read_b128 v[16:19], %14 offset:4096\n\t"
    "ds_read_b128 v[20:23], %14 offset:5120\n\t"
    "ds_read_b128 v[24:27], %14 offset:6144\n\t"
    "ds_read_b128 v[28:31], %14 offset:7168\n\t"
    "v_xor_b32 v15, 32768, %20\n\t"
    "s_add_u32 m0, %22, 20480\n\t"
    "s_nop 0\n\t"
    "global_load_lds_dwordx4 v15, %21 nt\n\t"
    "global_load_lds_dwordx4 v15, %21 offset:1024 nt\n\t"
    "global_load_lds_dwordx4 v15, %21 offset:2048 nt\n\t"
    "global_load_lds_dwordx4 v15, %21 offset:3072 nt\n\t"
    "v_mov_b32 v32, -1\n\t"
    "v_mov_b32 v33, 0\n\t"
    "s_waitcnt lgkmcnt(2)\n\t"
    "v_perm_b32 v4, v17, v16, %16\n\t"
    "v_perm_b32 v11, v21, v20, %16\n\t"
    "v_perm_b32 v0, v17, v16, %17\n\t"
    "v_perm_b32 v7, v21, v20, %17\n\t"
    "v_accvgpr_write_b32 %0, v4\n\t"
    "v_accvgpr_write_b32 %3, v11\n\t"
    "v_perm_b32 v5, v19, v18, %16\n\t"
    "v_perm_b32 v12, v23, v22, %16\n\t"
    "v_perm_b32 v1, v19, v18, %17\n\t"
    "v_perm_b32 v8, v23, v22, %17\n\t"
    "v_accvgpr_write_b32 %1, v5\n\t"
    "v_accvgpr_write_b32 %4, v12\n\t"
    "v_and_b32 v16, 0x7fff7fff, v16\n\t"
    "v_and_b32 v20, 0x7fff7fff, v20\n\t"
    "v_pk_add_u16 v2, v16, -1\n\t"
    "v_pk_add_u16 v9, v20, -1\n\t"
    "v_pk_sub_u16 v16, v16, %15 clamp\n\t"
    "v_pk_sub_u16 v20, v20, %15 clamp\n\t"
    "v_pk_min_u16 v32, v32, v2\n\t"
    "v_pk_min_u16 v32, v32, v9\n\t"
    "v_and_b32 v17, 0x7fff7fff, v17\n\t"
    "v_and_b32 v21, 0x7fff7fff, v21\n\t"
    "v_pk_add_u16 v3, v17, -1\n\t"
    "v_pk_add_u16 v10, v21, -1\n\t"
    "v_pk_sub_u16 v17, v17, %15 clamp\n\t"
    "v_pk_sub_u16 v21, v21, %15 clamp\n\t"
    "v_pk_min_u16 v32, v32, v3\n\t"
    "v_pk_min_u16 v32, v32, v10\n\t"
    "v_and_b32 v18, 0x7fff7fff, v18\n\t"
    "v_and_b32 v22, 0x7fff7fff, v22\n\t"
    "v_pk_add_u16 v2, v18, -1\n\t"
    "v_pk_add_u16 v9, v22, -1\n\t"
    "v_pk_sub_u16 v18, v18, %15 clamp\n\t"
    "v_pk_sub_u16 v22, v22, %15 clamp\n\t"
    "v_pk_min_u16 v32, v32, v2\n\t"
    "v_pk_min_u16 v32, v32, v9\n\t"
    "v_and_b32 v19, 0x7fff7fff, v19\n\t"
    "v_and_b32 v23, 0x7fff7fff, v23\n\t"
    "v_pk_add_u16 v3, v19, -1\n\t"
    "v_pk_add_u16 v10, v23, -1\n\t"
    "v_pk_sub_u16 v19, v19, %15 clamp\n\t"
    "v_pk_sub_u16 v23, v23, %15 clamp\n\t"
    "v_pk_min_u16 v32, v32, v3\n\t"
    "v_pk_min_u16 v32, v32, v10\n\t"
    "v_perm_b32 v2, v17, v16, %17\n\t"
    "v_perm_b32 v9, v21, v20, %17\n\t"
    "v_perm_b32 v3, v19, v18, %17\n\t"
    "v_perm_b32 v10, v23, v22, %17\n\t"
    "v_or3_b32 v33, v33, v2, v3\n\t"
    "v_or3_b32 v33, v33, v9, v10\n\t"
    "v_lshl_or_b32 v6, v3, 4, v2\n\t"
    "v_lshl_or_b32 v13, v10, 4, v9\n\t"
    "v_accvgpr_write_b32 %2, v6\n\t"
    "v_accvgpr_write_b32 %5, v13\n\t"
    "v_and_b32 v14, %19, v0\n\t"
    "v_lshrrev_b32 v14, 1, v14\n\t"
    "v_and_or_b32 v14, v1, %19, v14\n\t"
    "v_lshrrev_b32 v14, 1, v14\n\t"
    "v_and_or_b32 v14, v7, %19, v14\n\t"
    "v_lshrrev_b32 v14, 1, v14\n\t"
    "v_and_or_b32 v14, v8, %19, v14\n\t"
    "s_waitcnt lgkmcnt(0)\n\t"
    "v_perm_b32 v4, v25, v24, %16\n\t"
    "v_perm_b32 v11, v29, v28, %16\n\t"
    "v_perm_b32 v0, v25, v24, %17\n\t"
    "v_perm_b32 v7, v29, v28, %17\n\t"
    "v_accvgpr_write_b32 %6, v4\n\t"
    "v_accvgpr_write_b32 %9, v11\n\t"
    "v_perm_b32 v5, v27, v26, %16\n\t"
    "v_perm_b32 v12, v31, v30, %16\n\t"
    "v_perm_b32 v1, v27, v26, %17\n\t"
    "v_perm_b32 v8, v31, v30, %17\n\t"
    "v_accvgpr_write_b32 %7, v5\n\t"
    "v_accvgpr_write_b32 %10, v12\n\t"
    "v_and_b32 v24, 0x7fff7fff, v24\n\t"
    "v_and_b32 v28, 0x7fff7fff, v28\n\t"
    "v_pk_add_u16 v2, v24, -1\n\t"
    "v_pk_add_u16 v9, v28, -1\n\t"
    "v_pk_sub_u16 v24, v24, %15 clamp\n\t"
    "v_pk_sub_u16 v28, v28, %15 clamp\n\t"
    "v_pk_min_u16 v32, v32, v2\n\t"
    "v_pk_min_u16 v32, v32, v9\n\t"
    "v_and_b32 v25, 0x7fff7fff, v25\n\t"
    "v_and_b32 v29, 0x7fff7fff, v29\n\t"
    "v_pk_add_u16 v3, v25, -1\n\t"
    "v_pk_add_u16 v10, v29, -1\n\t"
    "v_pk_sub_u16 v25, v25, %15 clamp\n\t"
    "v_pk_sub_u16 v29, v29, %15 clamp\n\t"
    "v_pk_min_u16 v32, v32, v3\n\t"
    "v_pk_min_u16 v32, v32, v10\n\t"
    "v_and_b32 v26, 0x7fff7fff, v26\n\t"
    "v_and_b32 v30, 0x7fff7fff, v30\n\t"
    "v_pk_add_u16 v2, v26, -1\n\t"
    "v_pk_add_u16 v9, v30, -1\n\t"
    "v_pk_sub_u16 v26, v26, %15 clamp\n\t"
    "v_pk_sub_u16 v30, v30, %15 clamp\n\t"
    "v_pk_min_u16 v32, v32, v2\n\t"
    "v_pk_min_u16 v32, v32, v9\n\t"
    "v_and_b32 v27, 0x7fff7fff, v27\n\t"
    "v_and_b32 v31, 0x7fff7fff, v31\n\t"
    "v_pk_add_u16 v3, v27, -1\n\t"
    "v_pk_add_u16 v10, v31, -1\n\t"
    "v_pk_sub_u16 v27, v27, %15 clamp\n\t"
    "v_pk_sub_u16 v31, v31, %15 clamp\n\t"
    "v_pk_min_u16 v32, v32, v3\n\t"
    "v_pk_min_u16 v32, v32, v10\n\t"
    "v_perm_b32 v2, v25, v24, %17\n\t"
    "v_perm_b32 v9, v29, v28, %17\n\t"
    "v_perm_b32 v3, v27, v26, %17\n\t"
    "v_perm_b32 v10, v31, v30, %17\n\t"
    "v_or3_b32 v33, v33, v2, v3\n\t"
    "v_or3_b32 v33, v33, v9, v10\n\t"
    "v_lshl_or_b32 v6, v3, 4, v2\n\t"
    "v_lshl_or_b32 v13, v10, 4, v9\n\t"
    "v_accvgpr_write_b32 %8, v6\n\t"
    "v_accvgpr_write_b32 %11, v13\n\t"
    "v_lshrrev_b32 v14, 1, v14\n\t"
    "v_and_or_b32 v14, v0, %19, v14\n\t"
    "v_lshrrev_b32 v14, 1, v14\n\t"
    "v_and_or_b32 v14, v1, %19, v14\n\t"
    "v_lshrrev_b32 v14, 1, v14\n\t"
    "v_and_or_b32 v14, v7, %19, v14\n\t"
    "v_lshrrev_b32 v14, 1, v14\n\t"
    "v_and_or_b32 v14, v8, %19, v14\n\t"
    "v_accvgpr_write_b32 %12, v14\n\t"
    "v_pk_sub_u16 v32, %18, v32 clamp\n\t"
    "v_and_b32 v33, 0xf0f0f0f0, v33\n\t"
    "v_or3_b32 %13, %13, v32, v33\n\t"
    : "=&a"(pk[1][0]), "=&a"(pk[1][1]), "=&a"(pk[1][2]), "=&a"(pk[1][3]), "=&a"(pk[1][4]), "=&a"(pk[1][5]), "=&a"(pk[1][6]), "=&a"(pk[1][7]), "=&a"(pk[1][8]), "=&a"(pk[1][9]), "=&a"(pk[1][10]), "=&a"(pk[1][11]), "=&a"(pk[1][12]), "+v"(badv)
    : "v"(laddr), "s"(base2v), "s"(0x06040200u), "s"(0x07050301u), "s"(base2v + 0x00ff00ffu), "s"(0x80808080u), "v"(vofr), "s"(vbase), "s"(ring)
    : "memory", "scc", "v0", "v1", "v2", "v3", "v4", "v5", "v6", "v7", "v8", "v9", "v10", "v11", "v12", "v13", "v14", "v15", "v16", "v17", "v18", "v19", "v20", "v21", "v22", "v23", "v24", "v25", "v26", "v27", "v28", "v29", "v30", "v31", "v32", "v33");
  badmask |= (__any(badv != 0) ? 1u : 0u) << 0; badv = 0;
  pace(tp, 305);
  asm volatile(
    "s_waitcnt vmcnt(12)\n\t"
    "ds_read_b128 v[16:19], %14 offset:8192\n\t"
    "ds_read_b128 v[20:23], %14 offset:9216\n\t"
    "ds_read_b128 v[24:27], %14 offset:10240\n\t"
    "ds_read_b128 v[28:31], %14 offset:11264\n\t"
    "v_xor_b32 v15, 49152, %20\n\t"
    "s_add_u32 m0, %22, 24576\n\t"
    "s_nop 0\n\t"
    "global_load_lds_dwordx4 v15, %21 nt\n\t"
    "global_load_lds_dwordx4 v15, %21 offset:1024 nt\n\t"
    "global_load_lds_dwordx4 v15, %21 offset:2048 nt\n\t"
    "global_load_lds_dwordx4 v15, %21 offset:3072 nt\n\t"
    "v_mov_b32 v32, -1\n\t"
    "v_mov_b32 v33, 0\n\t"
    "s_waitcnt lgkmcnt(2)\n\t"
    "v_perm_b32 v4, v17, v16, %16\n\t"
    "v_perm_b32 v11, v21, v20, %16\n\t"
    "v_perm_b32 v0, v17, v16, %17\n\t"
    "v_perm_b32 v7, v21, v20, %17\n\t"
    "v_accvgpr_write_b32 %0, v4\n\t"
    "v_accvgpr_write_b32 %3, v11\n\t"
    "v_perm_b32 v5, v19, v18, %16\n\t"
    "v_perm_b32 v12, v23, v22, %16\n\t"
    "v_perm_b32 v1, v19, v18, %17\n\t"
    "v_perm_b32 v8, v23, v22, %17\n\t"
    "v_accvgpr_write_b32 %1, v5\n\t"
    "v_accvgpr_write_b32 %4, v12\n\t"
    "v_and_b32 v16, 0x7fff7fff, v16\n\t"
    "v_and_b32 v20, 0x7fff7fff, v20\n\t"
    "v_pk_add_u16 v2, v16, -1\n\t"
    "v_pk_add_u16 v9, v20, -1\n\t"
    "v_pk_sub_u16 v16, v16, %15 clamp\n\t"
    "v_pk_sub_u16 v20, v20, %15 clamp\n\t"
    "v_pk_min_u16 v32, v32, v2\n\t"
    "v_pk_min_u16 v32, v32, v9\n\t"
    "v_and_b32 v17, 0x7fff7fff, v17\n\t"
    "v_and_b32 v21, 0x7fff7fff, v21\n\t"
    "v_pk_add_u16 v3, v17, -1\n\t"
    "v_pk_add_u16 v10, v21, -1\n\t"
    "v_pk_sub_u16 v17, v17, %15 clamp\n\t"
    "v_pk_sub_u16 v21, v21, %15 clamp\n\t"
    "v_pk_min_u16 v32, v32, v3\n\t"
    "v_pk_min_u16 v32, v32, v10\n\t"
    "v_and_b32 v18, 0x7fff7fff, v18\n\t"
    "v_and_b32 v22, 0x7fff7fff, v22\n\t"
    "v_pk_add_u16 v2, v18, -1\n\t"
    "v_pk_add_u16 v9, v22, -1\n\t"
    "v_pk_sub_u16 v18, v18, %15 clamp\n\t"
    "v_pk_sub_u16 v22, v22, %15 clamp\n\t"
    "v_pk_min_u16 v32, v32, v2\n\t"
    "v_pk_min_u16 v32, v32, v9\n\t"
    "v_and_b32 v19, 0x7fff7fff, v19\n\t"
    "v_and_b32 v23, 0x7fff7fff, v23\n\t"
    "v_pk_add_u16 v3, v19, -1\n\t"
    "v_pk_add_u16 v10, v23, -1\n\t"
    "v_pk_sub_u16 v19, v19, %15 clamp\n\t"
    "v_pk_sub_u16 v23, v23, %15 clamp\n\t"
    "v_pk_min_u16 v32, v32, v3\n\t"
    "v_pk_min_u16 v32, v32, v10\n\t"
    "v_perm_b32 v2, v17, v16, %17\n\t"
    "v_perm_b32 v9, v21, v20, %17\n\t"
    "v_perm_b32 v3, v19, v18, %17\n\t"
    "v_perm_b32 v10, v23, v22, %17\n\t"
    "v_or3_b32 v33, v33, v2, v3\n\t"
    "v_or3_b32 v33, v33, v9, v10\n\t"
    "v_lshl_or_b32 v6, v3, 4, v2\n\t"
    "v_lshl_or_b32 v13, v10, 4, v9\n\t"
    "v_accvgpr_write_b32 %2, v6\n\t"
    "v_accvgpr_write_b32 %5, v13\n\t"
    "v_and_b32 v14, %19, v0\n\t"
    "v_lshrrev_b32 v14, 1, v14\n\t"
    "v_and_or_b32 v14, v1, %19, v14\n\t"
    "v_lshrrev_b32 v14, 1, v14\n\t"
    "v_and_or_b32 v14, v7, %19, v14\n\t"
    "v_lshrrev_b32 v14, 1, v14\n\t"
    "v_and_or_b32 v14, v8, %19, v14\n\t"
    "s_waitcnt lgkmcnt(0)\n\t"
    "v_perm_b32 v4, v25, v24, %16\n\t"
    "v_perm_b32 v11, v29, v28, %16\n\t"
    "v_perm_b32 v0, v25, v24, %17\n\t"
    "v_perm_b32 v7, v29, v28, %17\n\t"
    "v_accvgpr_write_b32 %6, v4\n\t"
    "v_accvgpr_write_b32 %9, v11\n\t"
    "v_perm_b32 v5, v27, v26, %16\n\t"
    "v_perm_b32 v12, v31, v30, %16\n\t"
    "v_perm_b32 v1, v27, v26, %17\n\t"
    "v_perm_b32 v8, v31, v30, %17\n\t"
    "v_accvgpr_write_b32 %7, v5\n\t"
    "v_accvgpr_write_b32 %10, v12\n\t"
    "v_and_b32 v24, 0x7fff7fff, v24\n\t"
    "v_and_b32 v28, 0x7fff7fff, v28\n\t"
    "v_pk_add_u16 v2, v24, -1\n\t"
    "v_pk_add_u16 v9, v28, -1\n\t"
    "v_pk_sub_u16 v24, v24, %15 clamp\n\t"
    "v_pk_sub_u16 v28, v28, %15 clamp\n\t"
    "v_pk_min_u16 v32, v32, v2\n\t"
    "v_pk_min_u16 v32, v32, v9\n\t"
    "v_and_b32 v25, 0x7fff7fff, v25\n\t"
    "v_and_b32 v29, 0x7fff7fff, v29\n\t"
    "v_pk_add_u16 v3, v25, -1\n\t"
    "v_pk_add_u16 v10, v29, -1\n\t"
    "v_pk_sub_u16 v25, v25, %15 clamp\n\t"
    "v_pk_sub_u16 v29, v29, %15 clamp\n\t"
    "v_pk_min_u16 v32, v32, v3\n\t"
    "v_pk_min_u16 v32, v32, v10\n\t"
    "v_and_b32 v26, 0x7fff7fff, v26\n\t"
    "v_and_b32 v30, 0x7fff7fff, v30\n\t"
    "v_pk_add_u16 v2, v26, -1\n\t"
    "v_pk_add_u16 v9, v30, -1\n\t"
    "v_pk_sub_u16 v26, v26, %15 clamp\n\t"
    "v_pk_sub_u16 v30, v30, %15 clamp\n\t"
    "v_pk_min_u16 v32, v32, v2\n\t"
    "v_pk_min_u16 v32, v32, v9\n\t"
    "v_and_b32 v27, 0x7fff7fff, v27\n\t"
    "v_and_b32 v31, 0x7fff7fff, v31\n\t"
    "v_pk_add_u16 v3, v27, -1\n\t"
    "v_pk_add_u16 v10, v31, -1\n\t"
    "v_pk_sub_u16 v27, v27, %15 clamp\n\t"
    "v_pk_sub_u16 v31, v31, %15 clamp\n\t"
    "v_pk_min_u16 v32, v32, v3\n\t"
    "v_pk_min_u16 v32, v32, v10\n\t"
    "v_perm_b32 v2, v25, v24, %17\n\t"
    "v_perm_b32 v9, v29, v28, %17\n\t"
    "v_perm_b32 v3, v27, v26, %17\n\t"
    "v_perm_b32 v10, v31, v30, %17\n\t"
    "v_or3_b32 v33, v33, v2, v3\n\t"
    "v_or3_b32 v33, v33, v9, v10\n\t"
    "v_lshl_or_b32 v6, v3, 4, v2\n\t"
    "v_lshl_or_b32 v13, v10, 4, v9\n\t"
    "v_accvgpr_write_b32 %8, v6\n\t"
    "v_accvgpr_write_b32 %11, v13\n\t"
    "v_lshrrev_b32 v14, 1, v14\n\t"
    "v_and_or_b32 v14, v0, %19, v14\n\t"
    "v_lshrrev_b32 v14, 1, v14\n\t"
    "v_and_or_b32 v14, v1, %19, v14\n\t"
    "v_lshrrev_b32 v14, 1, v14\n\t"
    "v_and_or_b32 v14, v7, %19, v14\n\t"
    "v_lshrrev_b32 v14, 1, v14\n\t"
    "v_and_or_b32 v14, v8, %19, v14\n\t"
    "v_accvgpr_write_b32 %12, v14\n\t"
    "v_pk_sub_u16 v32, %18, v32 clamp\n\t"
    "v_and_b32 v33, 0xf0f0f0f0, v33\n\t"
    "v_or3_b32 %13, %13, v32, v33\n\t"
    : "=&a"(pk[2][0]), "=&a"(pk[2][1]), "=&a"(pk[2][2]), "=&a"(pk[2][3]), "=&a"(pk[2][4]), "=&a"(pk[2][5]), "=&a"(pk[2][6]), "=&a"(pk[2][7]), "=&a"(pk[2][8]), "=&a"(pk[2][9]), "=&a"(pk[2][10]), "=&a"(pk[2][11]), "=&a"(pk[2][12]), "+v"(badv)
    : "v"(laddr), "s"(base2k), "s"(0x06040200u), "s"(0x07050301u), "s"(base2k + 0x00ff00ffu), "s"(0x80808080u), "v"(koff), "s"(kbase), "s"(ring)
    : "memory", "scc", "v0", "v1", "v2", "v3", "v4", "v5", "v6", "v7", "v8", "v9", "v10", "v11", "v12", "v13", "v14", "v15", "v16", "v17", "v18", "v19", "v20", "v21", "v22", "v23", "v24", "v25", "v26", "v27", "v28", "v29", "v30", "v31", "v32", "v33");
  lds_barrier();
  qproj_b(w, lane, red_q, sum4(cs_q + 2 * (lane & 31), 64), sh, OFF_G6 + q_slot(rb, cb, lane & 31), 1u, ws);
  pace(tp, 396);
  asm volatile(
    "s_waitcnt vmcnt(12)\n\t"
    "ds_read_b128 v[16:19], %14 offset:12288\n\t"
    "ds_read_b128 v[20:23], %14 offset:13312\n\t"
    "ds_read_b128 v[24:27], %14 offset:14336\n\t"
    "ds_read_b128 v[28:31], %14 offset:15360\n\t"
    "v_xor_b32 v15, 49152, %20\n\t"
    "s_add_u32 m0, %22, 28672\n\t"
    "s_nop 0\n\t"
    "global_load_lds_dwordx4 v15, %21 nt\n\t"
    "global_load_lds_dwordx4 v15, %21 offset:1024 nt\n\t"
    "global_load_lds_dwordx4 v15, %21 offset:2048 nt\n\t"
    "global_load_lds_dwordx4 v15, %21 offset:3072 nt\n\t"
    "v_mov_b32 v32, -1\n\t"
    "v_mov_b32 v33, 0\n\t"
    "s_waitcnt lgkmcnt(2)\n\t"
    "v_perm_b32 v4, v17, v16, %16\n\t"
    "v_perm_b32 v11, v21, v20, %16\n\t"
    "v_perm_b32 v0, v17, v16, %17\n\t"
    "v_perm_b32 v7, v21, v20, %17\n\t"
    "v_accvgpr_write_b32 %0, v4\n\t"
    "v_accvgpr_write_b32 %3, v11\n\t"
    "v_perm_b32 v5, v19, v18, %16\n\t"
    "v_perm_b32 v12, v23, v22, %16\n\t"
    "v_perm_b32 v1, v19, v18, %17\n\t"
    "v_perm_b32 v8, v23, v22, %17\n\t"
    "v_accvgpr_write_b32 %1, v5\n\t"
    "v_accvgpr_write_b32 %4, v12\n\t"
    "v_and_b32 v16, 0x7fff7fff, v16\n\t"
    "v_and_b32 v20, 0x7fff7fff, v20\n\t"
    "v_pk_add_u16 v2, v16, -1\n\t"
    "v_pk_add_u16 v9, v20, -1\n\t"
    "v_pk_sub_u16 v16, v16, %15 clamp\n\t"
    "v_pk_sub_u16 v20, v20, %15 clamp\n\t"
    "v_pk_min_u16 v32, v32, v2\n\t"
    "v_pk_min_u16 v32, v32, v9\n\t"
    "v_and_b32 v17, 0x7fff7fff, v17\n\t"
    "v_and_b32 v21, 0x7fff7fff, v21\n\t"
    "v_pk_add_u16 v3, v17, -1\n\t"
    "v_pk_add_u16 v10, v21, -1\n\t"
    "v_pk_sub_u16 v17, v17, %15 clamp\n\t"
    "v_pk_sub_u16 v21, v21, %15 clamp\n\t"
    "v_pk_min_u16 v32, v32, v3\n\t"
    "v_pk_min_u16 v32, v32, v10\n\t"
    "v_and_b32 v18, 0x7fff7fff, v18\n\t"
    "v_and_b32 v22, 0x7fff7fff, v22\n\t"
    "v_pk_add_u16 v2, v18, -1\n\t"
    "v_pk_add_u16 v9, v22, -1\n\t"
    "v_pk_sub_u16 v18, v18, %15 clamp\n\t"
    "v_pk_sub_u16 v22, v22, %15 clamp\n\t"
    "v_pk_min_u16 v32, v32, v2\n\t"
    "v_pk_min_u16 v32, v32, v9\n\t"
    "v_and_b32 v19, 0x7fff7fff, v19\n\t"
    "v_and_b32 v23, 0x7fff7fff, v23\n\t"
    "v_pk_add_u16 v3, v19, -1\n\t"
    "v_pk_add_u16 v10, v23, -1\n\t"
    "v_pk_sub_u16 v19, v19, %15 clamp\n\t"
    "v_pk_sub_u16 v23, v23, %15 clamp\n\t"
    "v_pk_min_u16 v32, v32, v3\n\t"
    "v_pk_min_u16 v32, v32, v10\n\t"
    "v_perm_b32 v2, v17, v16, %17\n\t"
    "v_perm_b32 v9, v21, v20, %17\n\t"
    "v_perm_b32 v3, v19, v18, %17\n\t"
    "v_perm_b32 v10, v23, v22, %17\n\t"
    "v_or3_b32 v33, v33, v2, v3\n\t"
    "v_or3_b32 v33, v33, v9, v10\n\t"
    "v_lshl_or_b32 v6, v3, 4, v2\n\t"
    "v_lshl_or_b32 v13, v10, 4, v9\n\t"
    "v_accvgpr_write_b32 %2, v6\n\t"
    "v_accvgpr_write_b32 %5, v13\n\t"
    "v_and_b32 v14, %19, v0\n\t"
    "v_lshrrev_b32 v14, 1, v14\n\t"
    "v_and_or_b32 v14, v1, %19, v14\n\t"
    "v_lshrrev_b32 v14, 1, v14\n\t"
    "v_and_or_b32 v14, v7, %19, v14\n\t"
    "v_lshrrev_b32 v14, 1, v14\n\t"
    "v_and_or_b32 v14, v8, %19, v14\n\t"
    "s_waitcnt lgkmcnt(0)\n\t"
    "v_perm_b32 v4, v25, v24, %16\n\t"
    "v_perm_b32 v11, v29, v28, %16\n\t"
    "v_perm_b32 v0, v25, v24, %17\n\t"
    "v_perm_b32 v7, v29, v28, %17\n\t"
    "v_accvgpr_write_b32 %6, v4\n\t"
    "v_accvgpr_write_b32 %9, v11\n\t"
    "v_perm_b32 v5, v27, v26, %16\n\t"
    "v_perm_b32 v12, v31, v30, %16\n\t"
    "v_perm_b32 v1, v27, v26, %17\n\t"
    "v_perm_b32 v8, v31, v30, %17\n\t"
    "v_accvgpr_write_b32 %7, v5\n\t"
    "v_accvgpr_write_b32 %10, v12\n\t"
    "v_and_b32 v24, 0x7fff7fff, v24\n\t"
    "v_and_b32 v28, 0x7fff7fff, v28\n\t"
    "v_pk_add_u16 v2, v24, -1\n\t"
    "v_pk_add_u16 v9, v28, -1\n\t"
    "v_pk_sub_u16 v24, v24, %15 clamp\n\t"
    "v_pk_sub_u16 v28, v28, %15 clamp\n\t"
    "v_pk_min_u16 v32, v32, v2\n\t"
    "v_pk_min_u16 v32, v32, v9\n\t"
    "v_and_b32 v25, 0x7fff7fff, v25\n\t"
    "v_and_b32 v29, 0x7fff7fff, v29\n\t"
    "v_pk_add_u16 v3, v25, -1\n\t"
    "v_pk_add_u16 v10, v29, -1\n\t"
    "v_pk_sub_u16 v25, v25, %15 clamp\n\t"
    "v_pk_sub_u16 v29, v29, %15 clamp\n\t"
    "v_pk_min_u16 v32, v32, v3\n\t"
    "v_pk_min_u16 v32, v32, v10\n\t"
    "v_and_b32 v26, 0x7fff7fff, v26\n\t"
    "v_and_b32 v30, 0x7fff7fff, v30\n\t"
    "v_pk_add_u16 v2, v26, -1\n\t"
    "v_pk_add_u16 v9, v30, -1\n\t"
    "v_pk_sub_u16 v26, v26, %15 clamp\n\t"
    "v_pk_sub_u16 v30, v30, %15 clamp\n\t"
    "v_pk_min_u16 v32, v32, v2\n\t"
    "v_pk_min_u16 v32, v32, v9\n\t"
    "v_and_b32 v27, 0x7fff7fff, v27\n\t"
    "v_and_b32 v31, 0x7fff7fff, v31\n\t"
    "v_pk_add_u16 v3, v27, -1\n\t"
    "v_pk_add_u16 v10, v31, -1\n\t"
    "v_pk_sub_u16 v27, v27, %15 clamp\n\t"
    "v_pk_sub_u16 v31, v31, %15 clamp\n\t"
    "v_pk_min_u16 v32, v32, v3\n\t"
    "v_pk_min_u16 v32, v32, v10\n\t"
    "v_perm_b32 v2, v25, v24, %17\n\t"
    "v_perm_b32 v9, v29, v28, %17\n\t"
    "v_perm_b32 v3, v27, v26, %17\n\t"
    "v_perm_b32 v10, v31, v30, %17\n\t"
    "v_or3_b32 v33, v33, v2, v3\n\t"
    "v_or3_b32 v33, v33, v9, v10\n\t"
    "v_lshl_or_b32 v6, v3, 4, v2\n\t"
    "v_lshl_or_b32 v13, v10, 4, v9\n\t"
    "v_accvgpr_write_b32 %8, v6\n\t"
    "v_accvgpr_write_b32 %11, v13\n\t"
    "v_lshrrev_b32 v14, 1, v14\n\t"
    "v_and_or_b32 v14, v0, %19, v14\n\t"
    "v_lshrrev_b32 v14, 1, v14\n\t"
    "v_and_or_b32 v14, v1, %19, v14\n\t"
    "v_lshrrev_b32 v14, 1, v14\n\t"
    "v_and_or_b32 v14, v7, %19, v14\n\t"
    "v_lshrrev_b32 v14, 1, v14\n\t"
    "v_and_or_b32 v14, v8, %19, v14\n\t"
    "v_accvgpr_write_b32 %12, v14\n\t"
    "v_pk_sub_u16 v32, %18, v32 clamp\n\t"
    "v_and_b32 v33, 0xf0f0f0f0, v33\n\t"
    "v_or3_b32 %13, %13, v32, v33\n\t"
    : "=&a"(pk[3][0]), "=&a"(pk[3][1]), "=&a"(pk[3][2]), "=&a"(pk[3][3]), "=&a"(pk[3][4]), "=&a"(pk[3][5]), "=&a"(pk[3][6]), "=&a"(pk[3][7]), "=&a"(pk[3][8]), "=&a"(pk[3][9]), "=&a"(pk[3][10]), "=&a"(pk[3][11]), "=&a"(pk[3][12]), "+v"(badv)
    : "v"(laddr), "s"(base2v), "s"(0x06040200u), "s"(0x07050301u), "s"(base2v + 0x00ff00ffu), "s"(0x80808080u), "v"(vofr), "s"(vbase), "s"(ring)
    : "memory", "scc", "v0", "v1", "v2", "v3", "v4", "v5", "v6", "v7", "v8", "v9", "v10", "v11", "v12", "v13", "v14", "v15", "v16", "v17", "v18", "v19", "v20", "v21", "v22", "v23", "v24", "v25", "v26", "v27", "v28", "v29", "v30", "v31", "v32", "v33");
  badmask |= (__any(badv != 0) ? 1u : 0u) << 1; badv = 0;
  pace(tp, 488);
  asm volatile(
    "s_waitcnt vmcnt(12)\n\t"
    "ds_read_b128 v[16:19], %14 offset:16384\n\t"
    "ds_read_b128 v[20:23], %14 offset:17408\n\t"
    "ds_read_b128 v[24:27], %14 offset:18432\n\t"
    "ds_read_b128 v[28:31], %14 offset:19456\n\t"
    "v_xor_b32 v15, 65536, %20\n\t"
    "s_add_u32 m0, %22, 0\n\t"
    "s_nop 0\n\t"
    "global_load_lds_dwordx4 v15, %21 nt\n\t"
    "global_load_lds_dwordx4 v15, %21 offset:1024 nt\n\t"
    "global_load_lds_dwordx4 v15, %21 offset:2048 nt\n\t"
    "global_load_lds_dwordx4 v15, %21 offset:3072 nt\n\t"
    "v_mov_b32 v32, -1\n\t"
    "v_mov_b32 v33, 0\n\t"
    "s_waitcnt lgkmcnt(2)\n\t"
    "v_perm_b32 v4, v17, v16, %16\n\t"
    "v_perm_b32 v11, v21, v20, %16\n\t"
    "v_perm_b32 v0, v17, v16, %17\n\t"
    "v_perm_b32 v7, v21, v20, %17\n\t"
    "v_accvgpr_write_b32 %0, v4\n\t"
    "v_accvgpr_write_b32 %3, v11\n\t"
    "v_perm_b32 v5, v19, v18, %16\n\t"
    "v_perm_b32 v12, v23, v22, %16\n\t"
    "v_perm_b32 v1, v19, v18, %17\n\t"
    "v_perm_b32 v8, v23, v22, %17\n\t"
    "v_accvgpr_write_b32 %1, v5\n\t"
    "v_accvgpr_write_b32 %4, v12\n\t"
    "v_and_b32 v16, 0x7fff7fff, v16\n\t"
    "v_and_b32 v20, 0x7fff7fff, v20\n\t"
    "v_pk_add_u16 v2, v16, -1\n\t"
    "v_pk_add_u16 v9, v20, -1\n\t"
    "v_pk_sub_u16 v16, v16, %15 clamp\n\t"
    "v_pk_sub_u16 v20, v20, %15 clamp\n\t"
    "v_pk_min_u16 v32, v32, v2\n\t"
    "v_pk_min_u16 v32, v32, v9\n\t"
    "v_and_b32 v17, 0x7fff7fff, v17\n\t"
    "v_and_b32 v21, 0x7fff7fff, v21\n\t"
    "v_pk_add_u16 v3, v17, -1\n\t"
    "v_pk_add_u16 v10, v21, -1\n\t"
    "v_pk_sub_u16 v17, v17, %15 clamp\n\t"
    "v_pk_sub_u16 v21, v21, %15 clamp\n\t"
    "v_pk_min_u16 v32, v32, v3\n\t"
    "v_pk_min_u16 v32, v32, v10\n\t"
    "v_and_b32 v18, 0x7fff7fff, v18\n\t"
    "v_and_b32 v22, 0x7fff7fff, v22\n\t"
    "v_pk_add_u16 v2, v18, -1\n\t"
    "v_pk_add_u16 v9, v22, -1\n\t"
    "v_pk_sub_u16 v18, v18, %15 clamp\n\t"
    "v_pk_sub_u16 v22, v22, %15 clamp\n\t"
    "v_pk_min_u16 v32, v32, v2\n\t"
    "v_pk_min_u16 v32, v32, v9\n\t"
    "v_and_b32 v19, 0x7fff7fff, v19\n\t"
    "v_and_b32 v23, 0x7fff7fff, v23\n\t"
    "v_pk_add_u16 v3, v19, -1\n\t"
    "v_pk_add_u16 v10, v23, -1\n\t"
    "v_pk_sub_u16 v19, v19, %15 clamp\n\t"
    "v_pk_sub_u16 v23, v23, %15 clamp\n\t"
    "v_pk_min_u16 v32, v32, v3\n\t"
    "v_pk_min_u16 v32, v32, v10\n\t"
    "v_perm_b32 v2, v17, v16, %17\n\t"
    "v_perm_b32 v9, v21, v20, %17\n\t"
    "v_perm_b32 v3, v19, v18, %17\n\t"
    "v_perm_b32 v10, v23, v22, %17\n\t"
    "v_or3_b32 v33, v33, v2, v3\n\t"
    "v_or3_b32 v33, v33, v9, v10\n\t"
    "v_lshl_or_b32 v6, v3, 4, v2\n\t"
    "v_lshl_or_b32 v13, v10, 4, v9\n\t"
    "v_accvgpr_write_b32 %2, v6\n\t"
    "v_accvgpr_write_b32 %5, v13\n\t"
    "v_and_b32 v14, %19, v0\n\t"
    "v_lshrrev_b32 v14, 1, v14\n\t"
    "v_and_or_b32 v14, v1, %19, v14\n\t"
    "v_lshrrev_b32 v14, 1, v14\n\t"
    "v_and_or_b32 v14, v7, %19, v14\n\t"
    "v_lshrrev_b32 v14, 1, v14\n\t"
    "v_and_or_b32 v14, v8, %19, v14\n\t"
    "s_waitcnt lgkmcnt(0)\n\t"
    "v_perm_b32 v4, v25, v24, %16\n\t"
    "v_perm_b32 v11, v29, v28, %16\n\t"
    "v_perm_b32 v0, v25, v24, %17\n\t"
    "v_perm_b32 v7, v29, v28, %17\n\t"
    "v_accvgpr_write_b32 %6, v4\n\t"
    "v_accvgpr_write_b32 %9, v11\n\t"
    "v_perm_b32 v5, v27, v26, %16\n\t"
    "v_perm_b32 v12, v31, v30, %16\n\t"
    "v_perm_b32 v1, v27, v26, %17\n\t"
    "v_perm_b32 v8, v31, v30, %17\n\t"
    "v_accvgpr_write_b32 %7, v5\n\t"
    "v_accvgpr_write_b32 %10, v12\n\t"
    "v_and_b32 v24, 0x7fff7fff, v24\n\t"
    "v_and_b32 v28, 0x7fff7fff, v28\n\t"
    "v_pk_add_u16 v2, v24, -1\n\t"
    "v_pk_add_u16 v9, v28, -1\n\t"
    "v_pk_sub_u16 v24, v24, %15 clamp\n\t"
    "v_pk_sub_u16 v28, v28, %15 clamp\n\t"
    "v_pk_min_u16 v32, v32, v2\n\t"
    "v_pk_min_u16 v32, v32, v9\n\t"
    "v_and_b32 v25, 0x7fff7fff, v25\n\t"
    "v_and_b32 v29, 0x7fff7fff, v29\n\t"
    "v_pk_add_u16 v3, v25, -1\n\t"
    "v_pk_add_u16 v10, v29, -1\n\t"
    "v_pk_sub_u16 v25, v25, %15 clamp\n\t"
    "v_pk_sub_u16 v29, v29, %15 clamp\n\t"
    "v_pk_min_u16 v32, v32, v3\n\t"
    "v_pk_min_u16 v32, v32, v10\n\t"
    "v_and_b32 v26, 0x7fff7fff, v26\n\t"
    "v_and_b32 v30, 0x7fff7fff, v30\n\t"
    "v_pk_add_u16 v2, v26, -1\n\t"
    "v_pk_add_u16 v9, v30, -1\n\t"
    "v_pk_sub_u16 v26, v26, %15 clamp\n\t"
    "v_pk_sub_u16 v30, v30, %15 clamp\n\t"
    "v_pk_min_u16 v32, v32, v2\n\t"
    "v_pk_min_u16 v32, v32, v9\n\t"
    "v_and_b32 v27, 0x7fff7fff, v27\n\t"
    "v_and_b32 v31, 0x7fff7fff, v31\n\t"
    "v_pk_add_u16 v3, v27, -1\n\t"
    "v_pk_add_u16 v10, v31, -1\n\t"
    "v_pk_sub_u16 v27, v27, %15 clamp\n\t"
    "v_pk_sub_u16 v31, v31, %15 clamp\n\t"
    "v_pk_min_u16 v32, v32, v3\n\t"
    "v_pk_min_u16 v32, v32, v10\n\t"
    "v_perm_b32 v2, v25, v24, %17\n\t"
    "v_perm_b32 v9, v29, v28, %17\n\t"
    "v_perm_b32 v3, v27, v26, %17\n\t"
    "v_perm_b32 v10, v31, v30, %17\n\t"
    "v_or3_b32 v33, v33, v2, v3\n\t"
    "v_or3_b32 v33, v33, v9, v10\n\t"
    "v_lshl_or_b32 v6, v3, 4, v2\n\t"
    "v_lshl_or_b32 v13, v10, 4, v9\n\t"
    "v_accvgpr_write_b32 %8, v6\n\t"
    "v_accvgpr_write_b32 %11, v13\n\t"
    "v_lshrrev_b32 v14, 1, v14\n\t"
    "v_and_or_b32 v14, v0, %19, v14\n\t"
    "v_lshrrev_b32 v14, 1, v14\n\t"
    "v_and_or_b32 v14, v1, %19, v14\n\t"
    "v_lshrrev_b32 v14, 1, v14\n\t"
    "v_and_or_b32 v14, v7, %19, v14\n\t"
    "v_lshrrev_b32 v14, 1, v14\n\t"
    "v_and_or_b32 v14, v8, %19, v14\n\t"
    "v_accvgpr_write_b32 %12, v14\n\t"
    "v_pk_sub_u16 v32, %18, v32 clamp\n\t"
    "v_and_b32 v33, 0xf0f0f0f0, v33\n\t"
    "v_or3_b32 %13, %13, v32, v33\n\t"
    : "=&a"(pk[4][0]), "=&a"(pk[4][1]), "=&a"(pk[4][2]), "=&a"(pk[4][3]), "=&a"(pk[4][4]), "=&a"(pk[4][5]), "=&a"(pk[4][6]), "=&a"(pk[4][7]), "=&a"(pk[4][8]), "=&a"(pk[4][9]), "=&a"(pk[4][10]), "=&a"(pk[4][11]), "=&a"(pk[4][12]), "+v"(badv)
    : "v"(laddr), "s"(base2k), "s"(0x06040200u), "s"(0x07050301u), "s"(base2k + 0x00ff00ffu), "s"(0x80808080u), "v"(koff), "s"(kbase), "s"(ring)
    : "memory", "scc", "v0", "v1", "v2", "v3", "v4", "v5", "v6", "v7", "v8", "v9", "v10", "v11", "v12", "v13", "v14", "v15", "v16", "v17", "v18", "v19", "v20", "v21", "v22", "v23", "v24", "v25", "v26", "v27", "v28", "v29", "v30", "v31", "v32", "v33");
  ld4(wor[0], off_wo + 0u, p.wo);
  ld4(wor[1], off_wo + 1024u, p.wo);
  ld4(wor[2], off_wo + 2048u, p.wo);
  ld4(wor[3], off_wo + 3072u, p.wo);
  pace(tp, 579);
  asm volatile(
    "s_waitcnt vmcnt(16)\n\t"
    "ds_read_b128 v[16:19], %14 offset:20480\n\t"
    "ds_read_b128 v[20:23], %14 offset:21504\n\t"
    "ds_read_b128 v[24:27], %14 offset:22528\n\t"
    "ds_read_b128 v[28:31], %14 offset:23552\n\t"
    "v_xor_b32 v15, 65536, %20\n\t"
    "s_add_u32 m0, %22, 4096\n\t"
    "s_nop 0\n\t"
    "global_load_lds_dwordx4 v15, %21 nt\n\t"
    "global_load_lds_dwordx4 v15, %21 offset:1024 nt\n\t"
    "global_load_lds_dwordx4 v15, %21 offset:2048 nt\n\t"
    "global_load_lds_dwordx4 v15, %21 offset:3072 nt\n\t"
    "v_mov_b32 v32, -1\n\t"
    "v_mov_b32 v33, 0\n\t"
    "s_waitcnt lgkmcnt(2)\n\t"
    "v_perm_b32 v4, v17, v16, %16\n\t"
    "v_perm_b32 v11, v21, v20, %16\n\t"
    "v_perm_b32 v0, v17, v16, %17\n\t"
    "v_perm_b32 v7, v21, v20, %17\n\t"
    "v_accvgpr_write_b32 %0, v4\n\t"
    "v_accvgpr_write_b32 %3, v11\n\t"
    "v_perm_b32 v5, v19, v18, %16\n\t"
    "v_perm_b32 v12, v23, v22, %16\n\t"
    "v_perm_b32 v1, v19, v18, %17\n\t"
    "v_perm_b32 v8, v23, v22, %17\n\t"
    "v_accvgpr_write_b32 %1, v5\n\t"
    "v_accvgpr_write_b32 %4, v12\n\t"
    "v_and_b32 v16, 0x7fff7fff, v16\n\t"
    "v_and_b32 v20, 0x7fff7fff, v20\n\t"
    "v_pk_add_u16 v2, v16, -1\n\t"
    "v_pk_add_u16 v9, v20, -1\n\t"
    "v_pk_sub_u16 v16, v16, %15 clamp\n\t"
    "v_pk_sub_u16 v20, v20, %15 clamp\n\t"
    "v_pk_min_u16 v32, v32, v2\n\t"
    "v_pk_min_u16 v32, v32, v9\n\t"
    "v_and_b32 v17, 0x7fff7fff, v17\n\t"
    "v_and_b32 v21, 0x7fff7fff, v21\n\t"
    "v_pk_add_u16 v3, v17, -1\n\t"
    "v_pk_add_u16 v10, v21, -1\n\t"
    "v_pk_sub_u16 v17, v17, %15 clamp\n\t"
    "v_pk_sub_u16 v21, v21, %15 clamp\n\t"
    "v_pk_min_u16 v32, v32, v3\n\t"
    "v_pk_min_u16 v32, v32, v10\n\t"
    "v_and_b32 v18, 0x7fff7fff, v18\n\t"
    "v_and_b32 v22, 0x7fff7fff, v22\n\t"
    "v_pk_add_u16 v2, v18, -1\n\t"
    "v_pk_add_u16 v9, v22, -1\n\t"
    "v_pk_sub_u16 v18, v18, %15 clamp\n\t"
    "v_pk_sub_u16 v22, v22, %15 clamp\n\t"
    "v_pk_min_u16 v32, v32, v2\n\t"
    "v_pk_min_u16 v32, v32, v9\n\t"
    "v_and_b32 v19, 0x7fff7fff, v19\n\t"
    "v_and_b32 v23, 0x7fff7fff, v23\n\t"
    "v_pk_add_u16 v3, v19, -1\n\t"
    "v_pk_add_u16 v10, v23, -1\n\t"
    "v_pk_sub_u16 v19, v19, %15 clamp\n\t"
    "v_pk_sub_u16 v23, v23, %15 clamp\n\t"
    "v_pk_min_u16 v32, v32, v3\n\t"
    "v_pk_min_u16 v32, v32, v10\n\t"
    "v_perm_b32 v2, v17, v16, %17\n\t"
    "v_perm_b32 v9, v21, v20, %17\n\t"
    "v_perm_b32 v3, v19, v18, %17\n\t"
    "v_perm_b32 v10, v23, v22, %17\n\t"
    "v_or3_b32 v33, v33, v2, v3\n\t"
    "v_or3_b32 v33, v33, v9, v10\n\t"
    "v_lshl_or_b32 v6, v3, 4, v2\n\t"
    "v_lshl_or_b32 v13, v10, 4, v9\n\t"
    "v_accvgpr_write_b32 %2, v6\n\t"
    "v_accvgpr_write_b32 %5, v13\n\t"
    "v_and_b32 v14, %19, v0\n\t"
    "v_lshrrev_b32 v14, 1, v14\n\t"
    "v_and_or_b32 v14, v1, %19, v14\n\t"
    "v_lshrrev_b32 v14, 1, v14\n\t"
    "v_and_or_b32 v14, v7, %19, v14\n\t"
    "v_lshrrev_b32 v14, 1, v14\n\t"
    "v_and_or_b32 v14, v8, %19, v14\n\t"
    "s_waitcnt lgkmcnt(0)\n\t"
    "v_perm_b32 v4, v25, v24, %16\n\t"
    "v_perm_b32 v11, v29, v28, %16\n\t"
    "v_perm_b32 v0, v25, v24, %17\n\t"
    "v_perm_b32 v7, v29, v28, %17\n\t"
    "v_accvgpr_write_b32 %6, v4\n\t"
    "v_accvgpr_write_b32 %9, v11\n\t"
    "v_perm_b32 v5, v27, v26, %16\n\t"
    "v_perm_b32 v12, v31, v30, %16\n\t"
    "v_perm_b32 v1, v27, v26, %17\n\t"
    "v_perm_b32 v8, v31, v30, %17\n\t"
    "v_accvgpr_write_b32 %7, v5\n\t"
    "v_accvgpr_write_b32 %10, v12\n\t"
    "v_and_b32 v24, 0x7fff7fff, v24\n\t"
    "v_and_b32 v28, 0x7fff7fff, v28\n\t"
    "v_pk_add_u16 v2, v24, -1\n\t"
    "v_pk_add_u16 v9, v28, -1\n\t"
    "v_pk_sub_u16 v24, v24, %15 clamp\n\t"
    "v_pk_sub_u16 v28, v28, %15 clamp\n\t"
    "v_pk_min_u16 v32, v32, v2\n\t"
    "v_pk_min_u16 v32, v32, v9\n\t"
    "v_and_b32 v25, 0x7fff7fff, v25\n\t"
    "v_and_b32 v29, 0x7fff7fff, v29\n\t"
    "v_pk_add_u16 v3, v25, -1\n\t"
    "v_pk_add_u16 v10, v29, -1\n\t"
    "v_pk_sub_u16 v25, v25, %15 clamp\n\t"
    "v_pk_sub_u16 v29, v29, %15 clamp\n\t"
    "v_pk_min_u16 v32, v32, v3\n\t"
    "v_pk_min_u16 v32, v32, v10\n\t"
    "v_and_b32 v26, 0x7fff7fff, v26\n\t"
    "v_and_b32 v30, 0x7fff7fff, v30\n\t"
    "v_pk_add_u16 v2, v26, -1\n\t"
    "v_pk_add_u16 v9, v30, -1\n\t"
    "v_pk_sub_u16 v26, v26, %15 clamp\n\t"
    "v_pk_sub_u16 v30, v30, %15 clamp\n\t"
    "v_pk_min_u16 v32, v32, v2\n\t"
    "v_pk_min_u16 v32, v32, v9\n\t"
    "v_and_b32 v27, 0x7fff7fff, v27\n\t"
    "v_and_b32 v31, 0x7fff7fff, v31\n\t"
    "v_pk_add_u16 v3, v27, -1\n\t"
    "v_pk_add_u16 v10, v31, -1\n\t"
    "v_pk_sub_u16 v27, v27, %15 clamp\n\t"
    "v_pk_sub_u16 v31, v31, %15 clamp\n\t"
    "v_pk_min_u16 v32, v32, v3\n\t"
    "v_pk_min_u16 v32, v32, v10\n\t"
    "v_perm_b32 v2, v25, v24, %17\n\t"
    "v_perm_b32 v9, v29, v28, %17\n\t"
    "v_perm_b32 v3, v27, v26, %17\n\t"
    "v_perm_b32 v10, v31, v30, %17\n\t"
    "v_or3_b32 v33, v33, v2, v3\n\t"
    "v_or3_b32 v33, v33, v9, v10\n\t"
    "v_lshl_or_b32 v6, v3, 4, v2\n\t"
    "v_lshl_or_b32 v13, v10, 4, v9\n\t"
    "v_accvgpr_write_b32 %8, v6\n\t"
    "v_accvgpr_write_b32 %11, v13\n\t"
    "v_lshrrev_b32 v14, 1, v14\n\t"
    "v_and_or_b32 v14, v0, %19, v14\n\t"
    "v_lshrrev_b32 v14, 1, v14\n\t"
    "v_and_or_b32 v14, v1, %19, v14\n\t"
    "v_lshrrev_b32 v14, 1, v14\n\t"
    "v_and_or_b32 v14, v7, %19, v14\n\t"
    "v_lshrrev_b32 v14, 1, v14\n\t"
    "v_and_or_b32 v14, v8, %19, v14\n\t"
    "v_accvgpr_write_b32 %12, v14\n\t"
    "v_pk_sub_u16 v32, %18, v32 clamp\n\t"
    "v_and_b32 v33, 0xf0f0f0f0, v33\n\t"
    "v_or3_b32 %13, %13, v32, v33\n\t"
    : "=&a"(pk[5][0]), "=&a"(pk[5][1]), "=&a"(pk[5][2]), "=&a"(pk[5][3]), "=&a"(pk[5][4]), "=&a"(pk[5][5]), "=&a"(pk[5][6]), "=&a"(pk[5][7]), "=&a"(pk[5][8]), "=&a"(pk[5][9]), "=&a"(pk[5][10]), "=&a"(pk[5][11]), "=&a"(pk[5][12]), "+v"(badv)
    : "v"(laddr), "s"(base2v), "s"(0x06040200u), "s"(0x07050301u), "s"(base2v + 0x00ff00ffu), "s"(0x80808080u), "v"(vofr), "s"(vbase), "s"(ring)
    : "memory", "scc", "v0", "v1", "v2", "v3", "v4", "v5", "v6", "v7", "v8", "v9", "v10", "v11", "v12", "v13", "v14", "v15", "v16", "v17", "v18", "v19", "v20", "v21", "v22", "v23", "v24", "v25", "v26", "v27", "v28", "v29", "v30", "v31", "v32", "v33");
  badmask |= (__any(badv != 0) ? 1u : 0u) << 2; badv = 0;
  ld4(wor[4], off_wo + 4096u, p.wo);
  ld4(wor[5], off_wo + 5120u, p.wo);
  ld4(wor[6], off_wo + 6144u, p.wo);
  ld4(wor[7], off_wo + 7168u, p.wo);
  pace(tp, 671);
  asm volatile(
    "s_waitcnt vmcnt(20)\n\t"
    "ds_read_b128 v[16:19], %14 offset:24576\n\t"
    "ds_read_b128 v[20:23], %14 offset:25600\n\t"
    "ds_read_b128 v[24:27], %14 offset:26624\n\t"
    "ds_read_b128 v[28:31], %14 offset:27648\n\t"
    "v_xor_b32 v15, 81920, %20\n\t"
    "s_add_u32 m0, %22, 8192\n\t"
    "s_nop 0\n\t"
    "global_load_lds_dwordx4 v15, %21 nt\n\t"
    "global_load_lds_dwordx4 v15, %21 offset:1024 nt\n\t"
    "global_load_lds_dwordx4 v15, %21 offset:2048 nt\n\t"
    "global_load_lds_dwordx4 v15, %21 offset:3072 nt\n\t"
    "v_mov_b32 v32, -1\n\t"
    "v_mov_b32 v33, 0\n\t"
    "s_waitcnt lgkmcnt(2)\n\t"
    "v_perm_b32 v4, v17, v16, %16\n\t"
    "v_perm_b32 v11, v21, v20, %16\n\t"
    "v_perm_b32 v0, v17, v16, %17\n\t"
    "v_perm_b32 v7, v21, v20, %17\n\t"
    "v_accvgpr_write_b32 %0, v4\n\t"
    "v_accvgpr_write_b32 %3, v11\n\t"
    "v_perm_b32 v5, v19, v18, %16\n\t"
    "v_perm_b32 v12, v23, v22, %16\n\t"
    "v_perm_b32 v1, v19, v18, %17\n\t"
    "v_perm_b32 v8, v23, v22, %17\n\t"
    "v_accvgpr_write_b32 %1, v5\n\t"
    "v_accvgpr_write_b32 %4, v12\n\t"
    "v_and_b32 v16, 0x7fff7fff, v16\n\t"
    "v_and_b32 v20, 0x7fff7fff, v20\n\t"
    "v_pk_add_u16 v2, v16, -1\n\t"
    "v_pk_add_u16 v9, v20, -1\n\t"
    "v_pk_sub_u16 v16, v16, %15 clamp\n\t"
    "v_pk_sub_u16 v20, v20, %15 clamp\n\t"
    "v_pk_min_u16 v32, v32, v2\n\t"
    "v_pk_min_u16 v32, v32, v9\n\t"
    "v_and_b32 v17, 0x7fff7fff, v17\n\t"
    "v_and_b32 v21, 0x7fff7fff, v21\n\t"
    "v_pk_add_u16 v3, v17, -1\n\t"
    "v_pk_add_u16 v10, v21, -1\n\t"
    "v_pk_sub_u16 v17, v17, %15 clamp\n\t"
    "v_pk_sub_u16 v21, v21, %15 clamp\n\t"
    "v_pk_min_u16 v32, v32, v3\n\t"
    "v_pk_min_u16 v32, v32, v10\n\t"
    "v_and_b32 v18, 0x7fff7fff, v18\n\t"
    "v_and_b32 v22, 0x7fff7fff, v22\n\t"
    "v_pk_add_u16 v2, v18, -1\n\t"
    "v_pk_add_u16 v9, v22, -1\n\t"
    "v_pk_sub_u16 v18, v18, %15 clamp\n\t"
    "v_pk_sub_u16 v22, v22, %15 clamp\n\t"
    "v_pk_min_u16 v32, v32, v2\n\t"
    "v_pk_min_u16 v32, v32, v9\n\t"
    "v_and_b32 v19, 0x7fff7fff, v19\n\t"
    "v_and_b32 v23, 0x7fff7fff, v23\n\t"
    "v_pk_add_u16 v3, v19, -1\n\t"
    "v_pk_add_u16 v10, v23, -1\n\t"
    "v_pk_sub_u16 v19, v19, %15 clamp\n\t"
    "v_pk_sub_u16 v23, v23, %15 clamp\n\t"
    "v_pk_min_u16 v32, v32, v3\n\t"
    "v_pk_min_u16 v32, v32, v10\n\t"
    "v_perm_b32 v2, v17, v16, %17\n\t"
    "v_perm_b32 v9, v21, v20, %17\n\t"
    "v_perm_b32 v3, v19, v18, %17\n\t"
    "v_perm_b32 v10, v23, v22, %17\n\t"
    "v_or3_b32 v33, v33, v2, v3\n\t"
    "v_or3_b32 v33, v33, v9, v10\n\t"
    "v_lshl_or_b32 v6, v3, 4, v2\n\t"
    "v_lshl_or_b32 v13, v10, 4, v9\n\t"
    "v_accvgpr_write_b32 %2, v6\n\t"
    "v_accvgpr_write_b32 %5, v13\n\t"
    "v_and_b32 v14, %19, v0\n\t"
    "v_lshrrev_b32 v14, 1, v14\n\t"
    "v_and_or_b32 v14, v1, %19, v14\n\t"
    "v_lshrrev_b32 v14, 1, v14\n\t"
    "v_and_or_b32 v14, v7, %19, v14\n\t"
    "v_lshrrev_b32 v14, 1, v14\n\t"
    "v_and_or_b32 v14, v8, %19, v14\n\t"
    "s_waitcnt lgkmcnt(0)\n\t"
    "v_perm_b32 v4, v25, v24, %16\n\t"
    "v_perm_b32 v11, v29, v28, %16\n\t"
    "v_perm_b32 v0, v25, v24, %17\n\t"
    "v_perm_b32 v7, v29, v28, %17\n\t"
    "v_accvgpr_write_b32 %6, v4\n\t"
    "v_accvgpr_write_b32 %9, v11\n\t"
    "v_perm_b32 v5, v27, v26, %16\n\t"
    "v_perm_b32 v12, v31, v30, %16\n\t"
    "v_perm_b32 v1, v27, v26, %17\n\t"
    "v_perm_b32 v8, v31, v30, %17\n\t"
    "v_accvgpr_write_b32 %7, v5\n\t"
    "v_accvgpr_write_b32 %10, v12\n\t"
    "v_and_b32 v24, 0x7fff7fff, v24\n\t"
    "v_and_b32 v28, 0x7fff7fff, v28\n\t"
    "v_pk_add_u16 v2, v24, -1\n\t"
    "v_pk_add_u16 v9, v28, -1\n\t"
    "v_pk_sub_u16 v24, v24, %15 clamp\n\t"
    "v_pk_sub_u16 v28, v28, %15 clamp\n\t"
    "v_pk_min_u16 v32, v32, v2\n\t"
    "v_pk_min_u16 v32, v32, v9\n\t"
    "v_and_b32 v25, 0x7fff7fff, v25\n\t"
    "v_and_b32 v29, 0x7fff7fff, v29\n\t"
    "v_pk_add_u16 v3, v25, -1\n\t"
    "v_pk_add_u16 v10, v29, -1\n\t"
    "v_pk_sub_u16 v25, v25, %15 clamp\n\t"
    "v_pk_sub_u16 v29, v29, %15 clamp\n\t"
    "v_pk_min_u16 v32, v32, v3\n\t"
    "v_pk_min_u16 v32, v32, v10\n\t"
    "v_and_b32 v26, 0x7fff7fff, v26\n\t"
    "v_and_b32 v30, 0x7fff7fff, v30\n\t"
    "v_pk_add_u16 v2, v26, -1\n\t"
    "v_pk_add_u16 v9, v30, -1\n\t"
    "v_pk_sub_u16 v26, v26, %15 clamp\n\t"
    "v_pk_sub_u16 v30, v30, %15 clamp\n\t"
    "v_pk_min_u16 v32, v32, v2\n\t"
    "v_pk_min_u16 v32, v32, v9\n\t"
    "v_and_b32 v27, 0x7fff7fff, v27\n\t"
    "v_and_b32 v31, 0x7fff7fff, v31\n\t"
    "v_pk_add_u16 v3, v27, -1\n\t"
    "v_pk_add_u16 v10, v31, -1\n\t"
    "v_pk_sub_u16 v27, v27, %15 clamp\n\t"
    "v_pk_sub_u16 v31, v31, %15 clamp\n\t"
    "v_pk_min_u16 v32, v32, v3\n\t"
    "v_pk_min_u16 v32, v32, v10\n\t"
    "v_perm_b32 v2, v25, v24, %17\n\t"
    "v_perm_b32 v9, v29, v28, %17\n\t"
    "v_perm_b32 v3, v27, v26, %17\n\t"
    "v_perm_b32 v10, v31, v30, %17\n\t"
    "v_or3_b32 v33, v33, v2, v3\n\t"
    "v_or3_b32 v33, v33, v9, v10\n\t"
    "v_lshl_or_b32 v6, v3, 4, v2\n\t"
    "v_lshl_or_b32 v13, v10, 4, v9\n\t"
    "v_accvgpr_write_b32 %8, v6\n\t"
    "v_accvgpr_write_b32 %11, v13\n\t"
    "v_lshrrev_b32 v14, 1, v14\n\t"
    "v_and_or_b32 v14, v0, %19, v14\n\t"
    "v_lshrrev_b32 v14, 1, v14\n\t"
    "v_and_or_b32 v14, v1, %19, v14\n\t"
    "v_lshrrev_b32 v14, 1, v14\n\t"
    "v_and_or_b32 v14, v7, %19, v14\n\t"
    "v_lshrrev_b32 v14, 1, v14\n\t"
    "v_and_or_b32 v14, v8, %19, v14\n\t"
    "v_accvgpr_write_b32 %12, v14\n\t"
    "v_pk_sub_u16 v32, %18, v32 clamp\n\t"
    "v_and_b32 v33, 0xf0f0f0f0, v33\n\t"
    "v_or3_b32 %13, %13, v32, v33\n\t"
    : "=&a"(pk[6][0]), "=&a"(pk[6][1]), "=&a"(pk[6][2]), "=&a"(pk[6][3]), "=&a"(pk[6][4]), "=&a"(pk[6][5]), "=&a"(pk[6][6]), "=&a"(pk[6][7]), "=&a"(pk[6][8]), "=&a"(pk[6][9]), "=&a"(pk[6][10]), "=&a"(pk[6][11]), "=&a"(pk[6][12]), "+v"(badv)
    : "v"(laddr), "s"(base2k), "s"(0x06040200u), "s"(0x07050301u), "s"(base2k + 0x00ff00ffu), "s"(0x80808080u), "v"(koff), "s"(kbase), "s"(ring)
    : "memory", "scc", "v0", "v1", "v2", "v3", "v4", "v5", "v6", "v7", "v8", "v9", "v10", "v11", "v12", "v13", "v14", "v15", "v16", "v17", "v18", "v19", "v20", "v21", "v22", "v23", "v24", "v25", "v26", "v27", "v28", "v29", "v30", "v31", "v32", "v33");
  ld4(wor[8], off_wo + 8192u, p.wo);
  ld4(wor[9], off_wo + 9216u, p.wo);
  ld4(wor[10], off_wo + 10240u, p.wo);
  ld4(wor[11], off_wo + 11264u, p.wo);
  pace(tp, 762);
  asm volatile(
    "s_waitcnt vmcnt(24)\n\t"
    "ds_read_b128 v[16:19], %14 offset:28672\n\t"
    "ds_read_b128 v[20:23], %14 offset:29696\n\t"
    "ds_read_b128 v[24:27], %14 offset:30720\n\t"
    "ds_read_b128 v[28:31], %14 offset:31744\n\t"
    "v_xor_b32 v15, 81920, %20\n\t"
    "s_add_u32 m0, %22, 12288\n\t"
    "s_nop 0\n\t"
    "global_load_lds_dwordx4 v15, %21 nt\n\t"
    "global_load_lds_dwordx4 v15, %21 offset:1024 nt\n\t"
    "global_load_lds_dwordx4 v15, %21 offset:2048 nt\n\t"
    "global_load_lds_dwordx4 v15, %21 offset:3072 nt\n\t"
    "v_mov_b32 v32, -1\n\t"
    "v_mov_b32 v33, 0\n\t"
    "s_waitcnt lgkmcnt(2)\n\t"
    "v_perm_b32 v4, v17, v16, %16\n\t"
    "v_perm_b32 v11, v21, v20, %16\n\t"
    "v_perm_b32 v0, v17, v16, %17\n\t"
    "v_perm_b32 v7, v21, v20, %17\n\t"
    "v_accvgpr_write_b32 %0, v4\n\t"
    "v_accvgpr_write_b32 %3, v11\n\t"
    "v_perm_b32 v5, v19, v18, %16\n\t"
    "v_perm_b32 v12, v23, v22, %16\n\t"
    "v_perm_b32 v1, v19, v18, %17\n\t"
    "v_perm_b32 v8, v23, v22, %17\n\t"
    "v_accvgpr_write_b32 %1, v5\n\t"
    "v_accvgpr_write_b32 %4, v12\n\t"
    "v_and_b32 v16, 0x7fff7fff, v16\n\t"
    "v_and_b32 v20, 0x7fff7fff, v20\n\t"
    "v_pk_add_u16 v2, v16, -1\n\t"
    "v_pk_add_u16 v9, v20, -1\n\t"
    "v_pk_sub_u16 v16, v16, %15 clamp\n\t"
    "v_pk_sub_u16 v20, v20, %15 clamp\n\t"
    "v_pk_min_u16 v32, v32, v2\n\t"
    "v_pk_min_u16 v32, v32, v9\n\t"
    "v_and_b32 v17, 0x7fff7fff, v17\n\t"
    "v_and_b32 v21, 0x7fff7fff, v21\n\t"
    "v_pk_add_u16 v3, v17, -1\n\t"
    "v_pk_add_u16 v10, v21, -1\n\t"
    "v_pk_sub_u16 v17, v17, %15 clamp\n\t"
    "v_pk_sub_u16 v21, v21, %15 clamp\n\t"
    "v_pk_min_u16 v32, v32, v3\n\t"
    "v_pk_min_u16 v32, v32, v10\n\t"
    "v_and_b32 v18, 0x7fff7fff, v18\n\t"
    "v_and_b32 v22, 0x7fff7fff, v22\n\t"
    "v_pk_add_u16 v2, v18, -1\n\t"
    "v_pk_add_u16 v9, v22, -1\n\t"
    "v_pk_sub_u16 v18, v18, %15 clamp\n\t"
    "v_pk_sub_u16 v22, v22, %15 clamp\n\t"
    "v_pk_min_u16 v32, v32, v2\n\t"
    "v_pk_min_u16 v32, v32, v9\n\t"
    "v_and_b32 v19, 0x7fff7fff, v19\n\t"
    "v_and_b32 v23, 0x7fff7fff, v23\n\t"
    "v_pk_add_u16 v3, v19, -1\n\t"
    "v_pk_add_u16 v10, v23, -1\n\t"
    "v_pk_sub_u16 v19, v19, %15 clamp\n\t"
    "v_pk_sub_u16 v23, v23, %15 clamp\n\t"
    "v_pk_min_u16 v32, v32, v3\n\t"
    "v_pk_min_u16 v32, v32, v10\n\t"
    "v_perm_b32 v2, v17, v16, %17\n\t"
    "v_perm_b32 v9, v21, v20, %17\n\t"
    "v_perm_b32 v3, v19, v18, %17\n\t"
    "v_perm_b32 v10, v23, v22, %17\n\t"
    "v_or3_b32 v33, v33, v2, v3\n\t"
    "v_or3_b32 v33, v33, v9, v10\n\t"
    "v_lshl_or_b32 v6, v3, 4, v2\n\t"
    "v_lshl_or_b32 v13, v10, 4, v9\n\t"
    "v_accvgpr_write_b32 %2, v6\n\t"
    "v_accvgpr_write_b32 %5, v13\n\t"
    "v_and_b32 v14, %19, v0\n\t"
    "v_lshrrev_b32 v14, 1, v14\n\t"
    "v_and_or_b32 v14, v1, %19, v14\n\t"
    "v_lshrrev_b32 v14, 1, v14\n\t"
    "v_and_or_b32 v14, v7, %19, v14\n\t"
    "v_lshrrev_b32 v14, 1, v14\n\t"
    "v_and_or_b32 v14, v8, %19, v14\n\t"
    "s_waitcnt lgkmcnt(0)\n\t"
    "v_perm_b32 v4, v25, v24, %16\n\t"
    "v_perm_b32 v11, v29, v28, %16\n\t"
    "v_perm_b32 v0, v25, v24, %17\n\t"
    "v_perm_b32 v7, v29, v28, %17\n\t"
    "v_accvgpr_write_b32 %6, v4\n\t"
    "v_accvgpr_write_b32 %9, v11\n\t"
    "v_perm_b32 v5, v27, v26, %16\n\t"
    "v_perm_b32 v12, v31, v30, %16\n\t"
    "v_perm_b32 v1, v27, v26, %17\n\t"
    "v_perm_b32 v8, v31, v30, %17\n\t"
    "v_accvgpr_write_b32 %7, v5\n\t"
    "v_accvgpr_write_b32 %10, v12\n\t"
    "v_and_b32 v24, 0x7fff7fff, v24\n\t"
    "v_and_b32 v28, 0x7fff7fff, v28\n\t"
    "v_pk_add_u16 v2, v24, -1\n\t"
    "v_pk_add_u16 v9, v28, -1\n\t"
    "v_pk_sub_u16 v24, v24, %15 clamp\n\t"
    "v_pk_sub_u16 v28, v28, %15 clamp\n\t"
    "v_pk_min_u16 v32, v32, v2\n\t"
    "v_pk_min_u16 v32, v32, v9\n\t"
    "v_and_b32 v25, 0x7fff7fff, v25\n\t"
    "v_and_b32 v29, 0x7fff7fff, v29\n\t"
    "v_pk_add_u16 v3, v25, -1\n\t"
    "v_pk_add_u16 v10, v29, -1\n\t"
    "v_pk_sub_u16 v25, v25, %15 clamp\n\t"
    "v_pk_sub_u16 v29, v29, %15 clamp\n\t"
    "v_pk_min_u16 v32, v32, v3\n\t"
    "v_pk_min_u16 v32, v32, v10\n\t"
    "v_and_b32 v26, 0x7fff7fff, v26\n\t"
    "v_and_b32 v30, 0x7fff7fff, v30\n\t"
    "v_pk_add_u16 v2, v26, -1\n\t"
    "v_pk_add_u16 v9, v30, -1\n\t"
    "v_pk_sub_u16 v26, v26, %15 clamp\n\t"
    "v_pk_sub_u16 v30, v30, %15 clamp\n\t"
    "v_pk_min_u16 v32, v32, v2\n\t"
    "v_pk_min_u16 v32, v32, v9\n\t"
    "v_and_b32 v27, 0x7fff7fff, v27\n\t"
    "v_and_b32 v31, 0x7fff7fff, v31\n\t"
    "v_pk_add_u16 v3, v27, -1\n\t"
    "v_pk_add_u16 v10, v31, -1\n\t"
    "v_pk_sub_u16 v27, v27, %15 clamp\n\t"
    "v_pk_sub_u16 v31, v31, %15 clamp\n\t"
    "v_pk_min_u16 v32, v32, v3\n\t"
    "v_pk_min_u16 v32, v32, v10\n\t"
    "v_perm_b32 v2, v25, v24, %17\n\t"
    "v_perm_b32 v9, v29, v28, %17\n\t"
    "v_perm_b32 v3, v27, v26, %17\n\t"
    "v_perm_b32 v10, v31, v30, %17\n\t"
    "v_or3_b32 v33, v33, v2, v3\n\t"
    "v_or3_b32 v33, v33, v9, v10\n\t"
    "v_lshl_or_b32 v6, v3, 4, v2\n\t"
    "v_lshl_or_b32 v13, v10, 4, v9\n\t"
    "v_accvgpr_write_b32 %8, v6\n\t"
    "v_accvgpr_write_b32 %11, v13\n\t"
    "v_lshrrev_b32 v14, 1, v14\n\t"
    "v_and_or_b32 v14, v0, %19, v14\n\t"
    "v_lshrrev_b32 v14, 1, v14\n\t"
    "v_and_or_b32 v14, v1, %19, v14\n\t"
    "v_lshrrev_b32 v14, 1, v14\n\t"
    "v_and_or_b32 v14, v7, %19, v14\n\t"
    "v_lshrrev_b32 v14, 1, v14\n\t"
    "v_and_or_b32 v14, v8, %19, v14\n\t"
    "v_accvgpr_write_b32 %12, v14\n\t"
    "v_pk_sub_u16 v32, %18, v32 clamp\n\t"
    "v_and_b32 v33, 0xf0f0f0f0, v33\n\t"
    "v_or3_b32 %13, %13, v32, v33\n\t"
    : "=&a"(pk[7][0]), "=&a"(pk[7][1]), "=&a"(pk[7][2]), "=&a"(pk[7][3]), "=&a"(pk[7][4]), "=&a"(pk[7][5]), "=&a"(pk[7][6]), "=&a"(pk[7][7]), "=&a"(pk[7][8]), "=&a"(pk[7][9]), "=&a"(pk[7][10]), "=&a"(pk[7][11]), "=&a"(pk[7][12]), "+v"(badv)
    : "v"(laddr), "s"(base2v), "s"(0x06040200u), "s"(0x07050301u), "s"(base2v + 0x00ff00ffu), "s"(0x80808080u), "v"(vofr), "s"(vbase), "s"(ring)
    : "memory", "scc", "v0", "v1", "v2", "v3", "v4", "v5", "v6", "v7", "v8", "v9", "v10", "v11", "v12", "v13", "v14", "v15", "v16", "v17", "v18", "v19", "v20", "v21", "v22", "v23", "v24", "v25", "v26", "v27", "v28", "v29", "v30", "v31", "v32", "v33");
  badmask |= (__any(badv != 0) ? 1u : 0u) << 3; badv = 0;
  if (tid == 0) st8_sc1(OFF_R + c * 8, u32x2{(u32)tagR, (u32)(tagR >> 32)}, ws);
  ld4(wor[12], off_wo + 12288u, p.wo);
  ld4(wor[13], off_wo + 13312u, p.wo);
  ld4(wor[14], off_wo + 14336u, p.wo);
  ld4(wor[15], off_wo + 15360u, p.wo);
  pace(tp, 854);
  asm volatile(
    "s_waitcnt vmcnt(28)\n\t"
    "ds_read_b128 v[16:19], %14 offset:0\n\t"
    "ds_read_b128 v[20:23], %14 offset:1024\n\t"
    "ds_read_b128 v[24:27], %14 offset:2048\n\t"
    "ds_read_b128 v[28:31], %14 offset:3072\n\t"
    "v_xor_b32 v15, 98304, %20\n\t"
    "s_add_u32 m0, %22, 16384\n\t"
    "s_nop 0\n\t"
    "global_load_lds_dwordx4 v15, %21 nt\n\t"
    "global_load_lds_dwordx4 v15, %21 offset:1024 nt\n\t"
    "global_load_lds_dwordx4 v15, %21 offset:2048 nt\n\t"
    "global_load_lds_dwordx4 v15, %21 offset:3072 nt\n\t"
    "v_mov_b32 v32, -1\n\t"
    "v_mov_b32 v33, 0\n\t"
    "s_waitcnt lgkmcnt(2)\n\t"
    "v_perm_b32 v4, v17, v16, %16\n\t"
    "v_perm_b32 v11, v21, v20, %16\n\t"
    "v_perm_b32 v0, v17, v16, %17\n\t"
    "v_perm_b32 v7, v21, v20, %17\n\t"
    "v_accvgpr_write_b32 %0, v4\n\t"
    "v_accvgpr_write_b32 %3, v11\n\t"
    "v_perm_b32 v5, v19, v18, %16\n\t"
    "v_perm_b32 v12, v23, v22, %16\n\t"
    "v_perm_b32 v1, v19, v18, %17\n\t"
    "v_perm_b32 v8, v23, v22, %17\n\t"
    "v_accvgpr_write_b32 %1, v5\n\t"
    "v_accvgpr_write_b32 %4, v12\n\t"
    "v_and_b32 v16, 0x7fff7fff, v16\n\t"
    "v_and_b32 v20, 0x7fff7fff, v20\n\t"
    "v_pk_add_u16 v2, v16, -1\n\t"
    "v_pk_add_u16 v9, v20, -1\n\t"
    "v_pk_sub_u16 v16, v16, %15 clamp\n\t"
    "v_pk_sub_u16 v20, v20, %15 clamp\n\t"
    "v_pk_min_u16 v32, v32, v2\n\t"
    "v_pk_min_u16 v32, v32, v9\n\t"
    "v_and_b32 v17, 0x7fff7fff, v17\n\t"
    "v_and_b32 v21, 0x7fff7fff, v21\n\t"
    "v_pk_add_u16 v3, v17, -1\n\t"
    "v_pk_add_u16 v10, v21, -1\n\t"
    "v_pk_sub_u16 v17, v17, %15 clamp\n\t"
    "v_pk_sub_u16 v21, v21, %15 clamp\n\t"
    "v_pk_min_u16 v32, v32, v3\n\t"
    "v_pk_min_u16 v32, v32, v10\n\t"
    "v_and_b32 v18, 0x7fff7fff, v18\n\t"
    "v_and_b32 v22, 0x7fff7fff, v22\n\t"
    "v_pk_add_u16 v2, v18, -1\n\t"
    "v_pk_add_u16 v9, v22, -1\n\t"
    "v_pk_sub_u16 v18, v18, %15 clamp\n\t"
    "v_pk_sub_u16 v22, v22, %15 clamp\n\t"
    "v_pk_min_u16 v32, v32, v2\n\t"
    "v_pk_min_u16 v32, v32, v9\n\t"
    "v_and_b32 v19, 0x7fff7fff, v19\n\t"
    "v_and_b32 v23, 0x7fff7fff, v23\n\t"
    "v_pk_add_u16 v3, v19, -1\n\t"
    "v_pk_add_u16 v10, v23, -1\n\t"
    "v_pk_sub_u16 v19, v19, %15 clamp\n\t"
    "v_pk_sub_u16 v23, v23, %15 clamp\n\t"
    "v_pk_min_u16 v32, v32, v3\n\t"
    "v_pk_min_u16 v32, v32, v10\n\t"
    "v_perm_b32 v2, v17, v16, %17\n\t"
    "v_perm_b32 v9, v21, v20, %17\n\t"
    "v_perm_b32 v3, v19, v18, %17\n\t"
    "v_perm_b32 v10, v23, v22, %17\n\t"
    "v_or3_b32 v33, v33, v2, v3\n\t"
    "v_or3_b32 v33, v33, v9, v10\n\t"
    "v_lshl_or_b32 v6, v3, 4, v2\n\t"
    "v_lshl_or_b32 v13, v10, 4, v9\n\t"
    "v_accvgpr_write_b32 %2, v6\n\t"
    "v_accvgpr_write_b32 %5, v13\n\t"
    "v_and_b32 v14, %19, v0\n\t"
    "v_lshrrev_b32 v14, 1, v14\n\t"
    "v_and_or_b32 v14, v1, %19, v14\n\t"
    "v_lshrrev_b32 v14, 1, v14\n\t"
    "v_and_or_b32 v14, v7, %19, v14\n\t"
    "v_lshrrev_b32 v14, 1, v14\n\t"
    "v_and_or_b32 v14, v8, %19, v14\n\t"
    "s_waitcnt lgkmcnt(0)\n\t"
    "v_perm_b32 v4, v25, v24, %16\n\t"
    "v_perm_b32 v11, v29, v28, %16\n\t"
    "v_perm_b32 v0, v25, v24, %17\n\t"
    "v_perm_b32 v7, v29, v28, %17\n\t"
    "v_accvgpr_write_b32 %6, v4\n\t"
    "v_accvgpr_write_b32 %9, v11\n\t"
    "v_perm_b32 v5, v27, v26, %16\n\t"
    "v_perm_b32 v12, v31, v30, %16\n\t"
    "v_perm_b32 v1, v27, v26, %17\n\t"
    "v_perm_b32 v8, v31, v30, %17\n\t"
    "v_accvgpr_write_b32 %7, v5\n\t"
    "v_accvgpr_write_b32 %10, v12\n\t"
    "v_and_b32 v24, 0x7fff7fff, v24\n\t"
    "v_and_b32 v28, 0x7fff7fff, v28\n\t"
    "v_pk_add_u16 v2, v24, -1\n\t"
    "v_pk_add_u16 v9, v28, -1\n\t"
    "v_pk_sub_u16 v24, v24, %15 clamp\n\t"
    "v_pk_sub_u16 v28, v28, %15 clamp\n\t"
    "v_pk_min_u16 v32, v32, v2\n\t"
    "v_pk_min_u16 v32, v32, v9\n\t"
    "v_and_b32 v25, 0x7fff7fff, v25\n\t"
    "v_and_b32 v29, 0x7fff7fff, v29\n\t"
    "v_pk_add_u16 v3, v25, -1\n\t"
    "v_pk_add_u16 v10, v29, -1\n\t"
    "v_pk_sub_u16 v25, v25, %15 clamp\n\t"
    "v_pk_sub_u16 v29, v29, %15 clamp\n\t"
    "v_pk_min_u16 v32, v32, v3\n\t"
    "v_pk_min_u16 v32, v32, v10\n\t"
    "v_and_b32 v26, 0x7fff7fff, v26\n\t"
    "v_and_b32 v30, 0x7fff7fff, v30\n\t"
    "v_pk_add_u16 v2, v26, -1\n\t"
    "v_pk_add_u16 v9, v30, -1\n\t"
    "v_pk_sub_u16 v26, v26, %15 clamp\n\t"
    "v_pk_sub_u16 v30, v30, %15 clamp\n\t"
    "v_pk_min_u16 v32, v32, v2\n\t"
    "v_pk_min_u16 v32, v32, v9\n\t"
    "v_and_b32 v27, 0x7fff7fff, v27\n\t"
    "v_and_b32 v31, 0x7fff7fff, v31\n\t"
    "v_pk_add_u16 v3, v27, -1\n\t"
    "v_pk_add_u16 v10, v31, -1\n\t"
    "v_pk_sub_u16 v27, v27, %15 clamp\n\t"
    "v_pk_sub_u16 v31, v31, %15 clamp\n\t"
    "v_pk_min_u16 v32, v32, v3\n\t"
    "v_pk_min_u16 v32, v32, v10\n\t"
    "v_perm_b32 v2, v25, v24, %17\n\t"
    "v_perm_b32 v9, v29, v28, %17\n\t"
    "v_perm_b32 v3, v27, v26, %17\n\t"
    "v_perm_b32 v10, v31, v30, %17\n\t"
    "v_or3_b32 v33, v33, v2, v3\n\t"
    "v_or3_b32 v33, v33, v9, v10\n\t"
    "v_lshl_or_b32 v6, v3, 4, v2\n\t"
    "v_lshl_or_b32 v13, v10, 4, v9\n\t"
    "v_accvgpr_write_b32 %8, v6\n\t"
    "v_accvgpr_write_b32 %11, v13\n\t"
    "v_lshrrev_b32 v14, 1, v14\n\t"
    "v_and_or_b32 v14, v0, %19, v14\n\t"
    "v_lshrrev_b32 v14, 1, v14\n\t"
    "v_and_or_b32 v14, v1, %19, v14\n\t"
    "v_lshrrev_b32 v14, 1, v14\n\t"
    "v_and_or_b32 v14, v7, %19, v14\n\t"
    "v_lshrrev_b32 v14, 1, v14\n\t"
    "v_and_or_b32 v14, v8, %19, v14\n\t"
    "v_accvgpr_write_b32 %12, v14\n\t"
    "v_pk_sub_u16 v32, %18, v32 clamp\n\t"
    "v_and_b32 v33, 0xf0f0f0f0, v33\n\t"
    "v_or3_b32 %13, %13, v32, v33\n\t"
    : "=&a"(pk[8][0]), "=&a"(pk[8][1]), "=&a"(pk[8][2]), "=&a"(pk[8][3]), "=&a"(pk[8][4]), "=&a"(pk[8][5]), "=&a"(pk[8][6]), "=&a"(pk[8][7]), "=&a"(pk[8][8]), "=&a"(pk[8][9]), "=&a"(pk[8][10]), "=&a"(pk[8][11]), "=&a"(pk[8][12]), "+v"(badv)
    : "v"(laddr), "s"(base2k), "s"(0x06040200u), "s"(0x07050301u), "s"(base2k + 0x00ff00ffu), "s"(0x80808080u), "v"(koff), "s"(kbase), "s"(ring)
    : "memory", "scc", "v0", "v1", "v2", "v3", "v4", "v5", "v6", "v7", "v8", "v9", "v10", "v11", "v12", "v13", "v14", "v15", "v16", "v17", "v18", "v19", "v20", "v21", "v22", "v23", "v24", "v25", "v26", "v27", "v28", "v29", "v30", "v31", "v32", "v33");
  ld4(wdr[0], off_wd + 0u, p.wd);
  ld4(wdr[1], off_wd + 1024u, p.wd);
  ld4(wdr[2], off_wd + 2048u, p.wd);
  ld4(wdr[3], off_wd + 3072u, p.wd);
  pace(tp, 915);
  asm volatile(
    "s_waitcnt vmcnt(28)\n\t"
    "ds_read_b128 v[16:19], %14 offset:4096\n\t"
    "ds_read_b128 v[20:23], %14 offset:5120\n\t"
    "ds_read_b128 v[24:27], %14 offset:6144\n\t"
    "ds_read_b128 v[28:31], %14 offset:7168\n\t"
    "v_xor_b32 v15, 98304, %20\n\t"
    "s_add_u32 m0, %22, 20480\n\t"
    "s_nop 0\n\t"
    "global_load_lds_dwordx4 v15, %21 nt\n\t"
    "global_load_lds_dwordx4 v15, %21 offset:1024 nt\n\t"
    "global_load_lds_dwordx4 v15, %21 offset:2048 nt\n\t"
    "global_load_lds_dwordx4 v15, %21 offset:3072 nt\n\t"
    "v_mov_b32 v32, -1\n\t"
    "v_mov_b32 v33, 0\n\t"
    "s_waitcnt lgkmcnt(2)\n\t"
    "v_perm_b32 v4, v17, v16, %16\n\t"
    "v_perm_b32 v11, v21, v20, %16\n\t"
    "v_perm_b32 v0, v17, v16, %17\n\t"
    "v_perm_b32 v7, v21, v20, %17\n\t"
    "v_accvgpr_write_b32 %0, v4\n\t"
    "v_accvgpr_write_b32 %3, v11\n\t"
    "v_perm_b32 v5, v19, v18, %16\n\t"
    "v_perm_b32 v12, v23, v22, %16\n\t"
    "v_perm_b32 v1, v19, v18, %17\n\t"
    "v_perm_b32 v8, v23, v22, %17\n\t"
    "v_accvgpr_write_b32 %1, v5\n\t"
    "v_accvgpr_write_b32 %4, v12\n\t"
    "v_and_b32 v16, 0x7fff7fff, v16\n\t"
    "v_and_b32 v20, 0x7fff7fff, v20\n\t"
    "v_pk_add_u16 v2, v16, -1\n\t"
    "v_pk_add_u16 v9, v20, -1\n\t"
    "v_pk_sub_u16 v16, v16, %15 clamp\n\t"
    "v_pk_sub_u16 v20, v20, %15 clamp\n\t"
    "v_pk_min_u16 v32, v32, v2\n\t"
    "v_pk_min_u16 v32, v32, v9\n\t"
    "v_and_b32 v17, 0x7fff7fff, v17\n\t"
    "v_and_b32 v21, 0x7fff7fff, v21\n\t"
    "v_pk_add_u16 v3, v17, -1\n\t"
    "v_pk_add_u16 v10, v21, -1\n\t"
    "v_pk_sub_u16 v17, v17, %15 clamp\n\t"
    "v_pk_sub_u16 v21, v21, %15 clamp\n\t"
    "v_pk_min_u16 v32, v32, v3\n\t"
    "v_pk_min_u16 v32, v32, v10\n\t"
    "v_and_b32 v18, 0x7fff7fff, v18\n\t"
    "v_and_b32 v22, 0x7fff7fff, v22\n\t"
    "v_pk_add_u16 v2, v18, -1\n\t"
    "v_pk_add_u16 v9, v22, -1\n\t"
    "v_pk_sub_u16 v18, v18, %15 clamp\n\t"
    "v_pk_sub_u16 v22, v22, %15 clamp\n\t"
    "v_pk_min_u16 v32, v32, v2\n\t"
    "v_pk_min_u16 v32, v32, v9\n\t"
    "v_and_b32 v19, 0x7fff7fff, v19\n\t"
    "v_and_b32 v23, 0x7fff7fff, v23\n\t"
    "v_pk_add_u16 v3, v19, -1\n\t"
    "v_pk_add_u16 v10, v23, -1\n\t"
    "v_pk_sub_u16 v19, v19, %15 clamp\n\t"
    "v_pk_sub_u16 v23, v23, %15 clamp\n\t"
    "v_pk_min_u16 v32, v32, v3\n\t"
    "v_pk_min_u16 v32, v32, v10\n\t"
    "v_perm_b32 v2, v17, v16, %17\n\t"
    "v_perm_b32 v9, v21, v20, %17\n\t"
    "v_perm_b32 v3, v19, v18, %17\n\t"
    "v_perm_b32 v10, v23, v22, %17\n\t"
    "v_or3_b32 v33, v33, v2, v3\n\t"
    "v_or3_b32 v33, v33, v9, v10\n\t"
    "v_lshl_or_b32 v6, v3, 4, v2\n\t"
    "v_lshl_or_b32 v13, v10, 4, v9\n\t"
    "v_accvgpr_write_b32 %2, v6\n\t"
    "v_accvgpr_write_b32 %5, v13\n\t"
    "v_and_b32 v14, %19, v0\n\t"
    "v_lshrrev_b32 v14, 1, v14\n\t"
    "v_and_or_b32 v14, v1, %19, v14\n\t"
    "v_lshrrev_b32 v14, 1, v14\n\t"
    "v_and_or_b32 v14, v7, %19, v14\n\t"
    "v_lshrrev_b32 v14, 1, v14\n\t"
    "v_and_or_b32 v14, v8, %19, v14\n\t"
    "s_waitcnt lgkmcnt(0)\n\t"
    "v_perm_b32 v4, v25, v24, %16\n\t"
    "v_perm_b32 v11, v29, v28, %16\n\t"
    "v_perm_b32 v0, v25, v24, %17\n\t"
    "v_perm_b32 v7, v29, v28, %17\n\t"
    "v_accvgpr_write_b32 %6, v4\n\t"
    "v_accvgpr_write_b32 %9, v11\n\t"
    "v_perm_b32 v5, v27, v26, %16\n\t"
    "v_perm_b32 v12, v31, v30, %16\n\t"
    "v_perm_b32 v1, v27, v26, %17\n\t"
    "v_perm_b32 v8, v31, v30, %17\n\t"
    "v_accvgpr_write_b32 %7, v5\n\t"
    "v_accvgpr_write_b32 %10, v12\n\t"
    "v_and_b32 v24, 0x7fff7fff, v24\n\t"
    "v_and_b32 v28, 0x7fff7fff, v28\n\t"
    "v_pk_add_u16 v2, v24, -1\n\t"
    "v_pk_add_u16 v9, v28, -1\n\t"
    "v_pk_sub_u16 v24, v24, %15 clamp\n\t"
    "v_pk_sub_u16 v28, v28, %15 clamp\n\t"
    "v_pk_min_u16 v32, v32, v2\n\t"
    "v_pk_min_u16 v32, v32, v9\n\t"
    "v_and_b32 v25, 0x7fff7fff, v25\n\t"
    "v_and_b32 v29, 0x7fff7fff, v29\n\t"
    "v_pk_add_u16 v3, v25, -1\n\t"
    "v_pk_add_u16 v10, v29, -1\n\t"
    "v_pk_sub_u16 v25, v25, %15 clamp\n\t"
    "v_pk_sub_u16 v29, v29, %15 clamp\n\t"
    "v_pk_min_u16 v32, v32, v3\n\t"
    "v_pk_min_u16 v32, v32, v10\n\t"
    "v_and_b32 v26, 0x7fff7fff, v26\n\t"
    "v_and_b32 v30, 0x7fff7fff, v30\n\t"
    "v_pk_add_u16 v2, v26, -1\n\t"
    "v_pk_add_u16 v9, v30, -1\n\t"
    "v_pk_sub_u16 v26, v26, %15 clamp\n\t"
    "v_pk_sub_u16 v30, v30, %15 clamp\n\t"
    "v_pk_min_u16 v32, v32, v2\n\t"
    "v_pk_min_u16 v32, v32, v9\n\t"
    "v_and_b32 v27, 0x7fff7fff, v27\n\t"
    "v_and_b32 v31, 0x7fff7fff, v31\n\t"
    "v_pk_add_u16 v3, v27, -1\n\t"
    "v_pk_add_u16 v10, v31, -1\n\t"
    "v_pk_sub_u16 v27, v27, %15 clamp\n\t"
    "v_pk_sub_u16 v31, v31, %15 clamp\n\t"
    "v_pk_min_u16 v32, v32, v3\n\t"
    "v_pk_min_u16 v32, v32, v10\n\t"
    "v_perm_b32 v2, v25, v24, %17\n\t"
    "v_perm_b32 v9, v29, v28, %17\n\t"
    "v_perm_b32 v3, v27, v26, %17\n\t"
    "v_perm_b32 v10, v31, v30, %17\n\t"
    "v_or3_b32 v33, v33, v2, v3\n\t"
    "v_or3_b32 v33, v33, v9, v10\n\t"
    "v_lshl_or_b32 v6, v3, 4, v2\n\t"
    "v_lshl_or_b32 v13, v10, 4, v9\n\t"
    "v_accvgpr_write_b32 %8, v6\n\t"
    "v_accvgpr_write_b32 %11, v13\n\t"
    "v_lshrrev_b32 v14, 1, v14\n\t"
    "v_and_or_b32 v14, v0, %19, v14\n\t"
    "v_lshrrev_b32 v14, 1, v14\n\t"
    "v_and_or_b32 v14, v1, %19, v14\n\t"
    "v_lshrrev_b32 v14, 1, v14\n\t"
    "v_and_or_b32 v14, v7, %19, v14\n\t"
    "v_lshrrev_b32 v14, 1, v14\n\t"
    "v_and_or_b32 v14, v8, %19, v14\n\t"
    "v_accvgpr_write_b32 %12, v14\n\t"
    "v_pk_sub_u16 v32, %18, v32 clamp\n\t"
    "v_and_b32 v33, 0xf0f0f0f0, v33\n\t"
    "v_or3_b32 %13, %13, v32, v33\n\t"
    : "=&a"(pk[9][0]), "=&a"(pk[9][1]), "=&a"(pk[9][2]), "=&a"(pk[9][3]), "=&a"(pk[9][4]), "=&a"(pk[9][5]), "=&a"(pk[9][6]), "=&a"(pk[9][7]), "=&a"(pk[9][8]), "=&a"(pk[9][9]), "=&a"(pk[9][10]), "=&a"(pk[9][11]), "=&a"(pk[9][12]), "+v"(badv)
    : "v"(laddr), "s"(base2v), "s"(0x06040200u), "s"(0x07050301u), "s"(base2v + 0x00ff00ffu), "s"(0x80808080u), "v"(vofr), "s"(vbase), "s"(ring)
    : "memory", "scc", "v0", "v1", "v2", "v3", "v4", "v5", "v6", "v7", "v8", "v9", "v10", "v11", "v12", "v13", "v14", "v15", "v16", "v17", "v18", "v19", "v20", "v21", "v22", "v23", "v24", "v25", "v26", "v27", "v28", "v29", "v30", "v31", "v32", "v33");
  badmask |= (__any(badv != 0) ? 1u : 0u) << 4; badv = 0;
  ld4(wdr[4], off_wd + 4096u, p.wd);
  ld4(wdr[5], off_wd + 5120u, p.wd);
  ld4(wdr[6], off_wd + 6144u, p.wd);
  ld4(wdr[7], off_wd + 7168u, p.wd);
  pace(tp, 976);
  asm volatile(
    "s_waitcnt vmcnt(28)\n\t"
    "ds_read_b128 v[16:19], %14 offset:8192\n\t"
    "ds_read_b128 v[20:23], %14 offset:9216\n\t"
    "ds_read_b128 v[24:27], %14 offset:10240\n\t"
    "ds_read_b128 v[28:31], %14 offset:11264\n\t"
    "v_xor_b32 v15, 114688, %20\n\t"
    "s_add_u32 m0, %22, 24576\n\t"
    "s_nop 0\n\t"
    "global_load_lds_dwordx4 v15, %21 nt\n\t"
    "global_load_lds_dwordx4 v15, %21 offset:1024 nt\n\t"
    "global_load_lds_dwordx4 v15, %21 offset:2048 nt\n\t"
    "global_load_lds_dwordx4 v15, %21 offset:3072 nt\n\t"
    "v_mov_b32 v32, -1\n\t"
    "v_mov_b32 v33, 0\n\t"
    "s_waitcnt lgkmcnt(2)\n\t"
    "v_perm_b32 v4, v17, v16, %16\n\t"
    "v_perm_b32 v11, v21, v20, %16\n\t"
    "v_perm_b32 v0, v17, v16, %17\n\t"
    "v_perm_b32 v7, v21, v20, %17\n\t"
    "v_accvgpr_write_b32 %0, v4\n\t"
    "v_accvgpr_write_b32 %3, v11\n\t"
    "v_perm_b32 v5, v19, v18, %16\n\t"
    "v_perm_b32 v12, v23, v22, %16\n\t"
    "v_perm_b32 v1, v19, v18, %17\n\t"
    "v_perm_b32 v8, v23, v22, %17\n\t"
    "v_accvgpr_write_b32 %1, v5\n\t"
    "v_accvgpr_write_b32 %4, v12\n\t"
    "v_and_b32 v16, 0x7fff7fff, v16\n\t"
    "v_and_b32 v20, 0x7fff7fff, v20\n\t"
    "v_pk_add_u16 v2, v16, -1\n\t"
    "v_pk_add_u16 v9, v20, -1\n\t"
    "v_pk_sub_u16 v16, v16, %15 clamp\n\t"
    "v_pk_sub_u16 v20, v20, %15 clamp\n\t"
    "v_pk_min_u16 v32, v32, v2\n\t"
    "v_pk_min_u16 v32, v32, v9\n\t"
    "v_and_b32 v17, 0x7fff7fff, v17\n\t"
    "v_and_b32 v21, 0x7fff7fff, v21\n\t"
    "v_pk_add_u16 v3, v17, -1\n\t"
    "v_pk_add_u16 v10, v21, -1\n\t"
    "v_pk_sub_u16 v17, v17, %15 clamp\n\t"
    "v_pk_sub_u16 v21, v21, %15 clamp\n\t"
    "v_pk_min_u16 v32, v32, v3\n\t"
    "v_pk_min_u16 v32, v32, v10\n\t"
    "v_and_b32 v18, 0x7fff7fff, v18\n\t"
    "v_and_b32 v22, 0x7fff7fff, v22\n\t"
    "v_pk_add_u16 v2, v18, -1\n\t"
    "v_pk_add_u16 v9, v22, -1\n\t"
    "v_pk_sub_u16 v18, v18, %15 clamp\n\t"
    "v_pk_sub_u16 v22, v22, %15 clamp\n\t"
    "v_pk_min_u16 v32, v32, v2\n\t"
    "v_pk_min_u16 v32, v32, v9\n\t"
    "v_and_b32 v19, 0x7fff7fff, v19\n\t"
    "v_and_b32 v23, 0x7fff7fff, v23\n\t"
    "v_pk_add_u16 v3, v19, -1\n\t"
    "v_pk_add_u16 v10, v23, -1\n\t"
    "v_pk_sub_u16 v19, v19, %15 clamp\n\t"
    "v_pk_sub_u16 v23, v23, %15 clamp\n\t"
    "v_pk_min_u16 v32, v32, v3\n\t"
    "v_pk_min_u16 v32, v32, v10\n\t"
    "v_perm_b32 v2, v17, v16, %17\n\t"
    "v_perm_b32 v9, v21, v20, %17\n\t"
    "v_perm_b32 v3, v19, v18, %17\n\t"
    "v_perm_b32 v10, v23, v22, %17\n\t"
    "v_or3_b32 v33, v33, v2, v3\n\t"
    "v_or3_b32 v33, v33, v9, v10\n\t"
    "v_lshl_or_b32 v6, v3, 4, v2\n\t"
    "v_lshl_or_b32 v13, v10, 4, v9\n\t"
    "v_accvgpr_write_b32 %2, v6\n\t"
    "v_accvgpr_write_b32 %5, v13\n\t"
    "v_and_b32 v14, %19, v0\n\t"
    "v_lshrrev_b32 v14, 1, v14\n\t"
    "v_and_or_b32 v14, v1, %19, v14\n\t"
    "v_lshrrev_b32 v14, 1, v14\n\t"
    "v_and_or_b32 v14, v7, %19, v14\n\t"
    "v_lshrrev_b32 v14, 1, v14\n\t"
    "v_and_or_b32 v14, v8, %19, v14\n\t"
    "s_waitcnt lgkmcnt(0)\n\t"
    "v_perm_b32 v4, v25, v24, %16\n\t"
    "v_perm_b32 v11, v29, v28, %16\n\t"
    "v_perm_b32 v0, v25, v24, %17\n\t"
    "v_perm_b32 v7, v29, v28, %17\n\t"
    "v_accvgpr_write_b32 %6, v4\n\t"
    "v_accvgpr_write_b32 %9, v11\n\t"
    "v_perm_b32 v5, v27, v26, %16\n\t"
    "v_perm_b32 v12, v31, v30, %16\n\t"
    "v_perm_b32 v1, v27, v26, %17\n\t"
    "v_perm_b32 v8, v31, v30, %17\n\t"
    "v_accvgpr_write_b32 %7, v5\n\t"
    "v_accvgpr_write_b32 %10, v12\n\t"
    "v_and_b32 v24, 0x7fff7fff, v24\n\t"
    "v_and_b32 v28, 0x7fff7fff, v28\n\t"
    "v_pk_add_u16 v2, v24, -1\n\t"
    "v_pk_add_u16 v9, v28, -1\n\t"
    "v_pk_sub_u16 v24, v24, %15 clamp\n\t"
    "v_pk_sub_u16 v28, v28, %15 clamp\n\t"
    "v_pk_min_u16 v32, v32, v2\n\t"
    "v_pk_min_u16 v32, v32, v9\n\t"
    "v_and_b32 v25, 0x7fff7fff, v25\n\t"
    "v_and_b32 v29, 0x7fff7fff, v29\n\t"
    "v_pk_add_u16 v3, v25, -1\n\t"
    "v_pk_add_u16 v10, v29, -1\n\t"
    "v_pk_sub_u16 v25, v25, %15 clamp\n\t"
    "v_pk_sub_u16 v29, v29, %15 clamp\n\t"
    "v_pk_min_u16 v32, v32, v3\n\t"
    "v_pk_min_u16 v32, v32, v10\n\t"
    "v_and_b32 v26, 0x7fff7fff, v26\n\t"
    "v_and_b32 v30, 0x7fff7fff, v30\n\t"
    "v_pk_add_u16 v2, v26, -1\n\t"
    "v_pk_add_u16 v9, v30, -1\n\t"
    "v_pk_sub_u16 v26, v26, %15 clamp\n\t"
    "v_pk_sub_u16 v30, v30, %15 clamp\n\t"
    "v_pk_min_u16 v32, v32, v2\n\t"
    "v_pk_min_u16 v32, v32, v9\n\t"
    "v_and_b32 v27, 0x7fff7fff, v27\n\t"
    "v_and_b32 v31, 0x7fff7fff, v31\n\t"
    "v_pk_add_u16 v3, v27, -1\n\t"
    "v_pk_add_u16 v10, v31, -1\n\t"
    "v_pk_sub_u16 v27, v27, %15 clamp\n\t"
    "v_pk_sub_u16 v31, v31, %15 clamp\n\t"
    "v_pk_min_u16 v32, v32, v3\n\t"
    "v_pk_min_u16 v32, v32, v10\n\t"
    "v_perm_b32 v2, v25, v24, %17\n\t"
    "v_perm_b32 v9, v29, v28, %17\n\t"
    "v_perm_b32 v3, v27, v26, %17\n\t"
    "v_perm_b32 v10, v31, v30, %17\n\t"
    "v_or3_b32 v33, v33, v2, v3\n\t"
    "v_or3_b32 v33, v33, v9, v10\n\t"
    "v_lshl_or_b32 v6, v3, 4, v2\n\t"
    "v_lshl_or_b32 v13, v10, 4, v9\n\t"
    "v_accvgpr_write_b32 %8, v6\n\t"
    "v_accvgpr_write_b32 %11, v13\n\t"
    "v_lshrrev_b32 v14, 1, v14\n\t"
    "v_and_or_b32 v14, v0, %19, v14\n\t"
    "v_lshrrev_b32 v14, 1, v14\n\t"
    "v_and_or_b32 v14, v1, %19, v14\n\t"
    "v_lshrrev_b32 v14, 1, v14\n\t"
    "v_and_or_b32 v14, v7, %19, v14\n\t"
    "v_lshrrev_b32 v14, 1, v14\n\t"
    "v_and_or_b32 v14, v8, %19, v14\n\t"
    "v_accvgpr_write_b32 %12, v14\n\t"
    "v_pk_sub_u16 v32, %18, v32 clamp\n\t"
    "v_and_b32 v33, 0xf0f0f0f0, v33\n\t"
    "v_or3_b32 %13, %13, v32, v33\n\t"
    : "=&a"(pk[10][0]), "=&a"(pk[10][1]), "=&a"(pk[10][2]), "=&a"(pk[10][3]), "=&a"(pk[10][4]), "=&a"(pk[10][5]), "=&a"(pk[10][6]), "=&a"(pk[10][7]), "=&a"(pk[10][8]), "=&a"(pk[10][9]), "=&a"(pk[10][10]), "=&a"(pk[10][11]), "=&a"(pk[10][12]), "+v"(badv)
    : "v"(laddr), "s"(base2k), "s"(0x06040200u), "s"(0x07050301u), "s"(base2k + 0x00ff00ffu), "s"(0x80808080u), "v"(koff), "s"(kbase), "s"(ring)
    : "memory", "scc", "v0", "v1", "v2", "v3", "v4", "v5", "v6", "v7", "v8", "v9", "v10", "v11", "v12", "v13", "v14", "v15", "v16", "v17", "v18", "v19", "v20", "v21", "v22", "v23", "v24", "v25", "v26", "v27", "v28", "v29", "v30", "v31", "v32", "v33");
  ld16_if(gur[0], off_gu + 0, p.wg, gph, 0);
  ld16_if(gur[1], off_gu + 0, p.wu, gph, 0);
  pace(tp, 1037);
  asm volatile(
    "s_waitcnt vmcnt(24)\n\t"
    "ds_read_b128 v[16:19], %14 offset:12288\n\t"
    "ds_read_b128 v[20:23], %14 offset:13312\n\t"
    "ds_read_b128 v[24:27], %14 offset:14336\n\t"
    "ds_read_b128 v[28:31], %14 offset:15360\n\t"
    "v_xor_b32 v15, 114688, %20\n\t"
    "s_add_u32 m0, %22, 28672\n\t"
    "s_nop 0\n\t"
    "global_load_lds_dwordx4 v15, %21 nt\n\t"
    "global_load_lds_dwordx4 v15, %21 offset:1024 nt\n\t"
    "global_load_lds_dwordx4 v15, %21 offset:2048 nt\n\t"
    "global_load_lds_dwordx4 v15, %21 offset:3072 nt\n\t"
    "v_mov_b32 v32, -1\n\t"
    "v_mov_b32 v33, 0\n\t"
    "s_waitcnt lgkmcnt(2)\n\t"
    "v_perm_b32 v4, v17, v16, %16\n\t"
    "v_perm_b32 v11, v21, v20, %16\n\t"
    "v_perm_b32 v0, v17, v16, %17\n\t"
    "v_perm_b32 v7, v21, v20, %17\n\t"
    "v_accvgpr_write_b32 %0, v4\n\t"
    "v_accvgpr_write_b32 %3, v11\n\t"
    "v_perm_b32 v5, v19, v18, %16\n\t"
    "v_perm_b32 v12, v23, v22, %16\n\t"
    "v_perm_b32 v1, v19, v18, %17\n\t"
    "v_perm_b32 v8, v23, v22, %17\n\t"
    "v_accvgpr_write_b32 %1, v5\n\t"
    "v_accvgpr_write_b32 %4, v12\n\t"
    "v_and_b32 v16, 0x7fff7fff, v16\n\t"
    "v_and_b32 v20, 0x7fff7fff, v20\n\t"
    "v_pk_add_u16 v2, v16, -1\n\t"
    "v_pk_add_u16 v9, v20, -1\n\t"
    "v_pk_sub_u16 v16, v16, %15 clamp\n\t"
    "v_pk_sub_u16 v20, v20, %15 clamp\n\t"
    "v_pk_min_u16 v32, v32, v2\n\t"
    "v_pk_min_u16 v32, v32, v9\n\t"
    "v_and_b32 v17, 0x7fff7fff, v17\n\t"
    "v_and_b32 v21, 0x7fff7fff, v21\n\t"
    "v_pk_add_u16 v3, v17, -1\n\t"
    "v_pk_add_u16 v10, v21, -1\n\t"
    "v_pk_sub_u16 v17, v17, %15 clamp\n\t"
    "v_pk_sub_u16 v21, v21, %15 clamp\n\t"
    "v_pk_min_u16 v32, v32, v3\n\t"
    "v_pk_min_u16 v32, v32, v10\n\t"
    "v_and_b32 v18, 0x7fff7fff, v18\n\t"
    "v_and_b32 v22, 0x7fff7fff, v22\n\t"
    "v_pk_add_u16 v2, v18, -1\n\t"
    "v_pk_add_u16 v9, v22, -1\n\t"
    "v_pk_sub_u16 v18, v18, %15 clamp\n\t"
    "v_pk_sub_u16 v22, v22, %15 clamp\n\t"
    "v_pk_min_u16 v32, v32, v2\n\t"
    "v_pk_min_u16 v32, v32, v9\n\t"
    "v_and_b32 v19, 0x7fff7fff, v19\n\t"
    "v_and_b32 v23, 0x7fff7fff, v23\n\t"
    "v_pk_add_u16 v3, v19, -1\n\t"
    "v_pk_add_u16 v10, v23, -1\n\t"
    "v_pk_sub_u16 v19, v19, %15 clamp\n\t"
    "v_pk_sub_u16 v23, v23, %15 clamp\n\t"
    "v_pk_min_u16 v32, v32, v3\n\t"
    "v_pk_min_u16 v32, v32, v10\n\t"
    "v_perm_b32 v2, v17, v16, %17\n\t"
    "v_perm_b32 v9, v21, v20, %17\n\t"
    "v_perm_b32 v3, v19, v18, %17\n\t"
    "v_perm_b32 v10, v23, v22, %17\n\t"
    "v_or3_b32 v33, v33, v2, v3\n\t"
    "v_or3_b32 v33, v33, v9, v10\n\t"
    "v_lshl_or_b32 v6, v3, 4, v2\n\t"
    "v_lshl_or_b32 v13, v10, 4, v9\n\t"
    "v_accvgpr_write_b32 %2, v6\n\t"
    "v_accvgpr_write_b32 %5, v13\n\t"
    "v_and_b32 v14, %19, v0\n\t"
    "v_lshrrev_b32 v14, 1, v14\n\t"
    "v_and_or_b32 v14, v1, %19, v14\n\t"
    "v_lshrrev_b32 v14, 1, v14\n\t"
    "v_and_or_b32 v14, v7, %19, v14\n\t"
    "v_lshrrev_b32 v14, 1, v14\n\t"
    "v_and_or_b32 v14, v8, %19, v14\n\t"
    "s_waitcnt lgkmcnt(0)\n\t"
    "v_perm_b32 v4, v25, v24, %16\n\t"
    "v_perm_b32 v11, v29, v28, %16\n\t"
    "v_perm_b32 v0, v25, v24, %17\n\t"
    "v_perm_b32 v7, v29, v28, %17\n\t"
    "v_accvgpr_write_b32 %6, v4\n\t"
    "v_accvgpr_write_b32 %9, v11\n\t"
    "v_perm_b32 v5, v27, v26, %16\n\t"
    "v_perm_b32 v12, v31, v30, %16\n\t"
    "v_perm_b32 v1, v27, v26, %17\n\t"
    "v_perm_b32 v8, v31, v30, %17\n\t"
    "v_accvgpr_write_b32 %7, v5\n\t"
    "v_accvgpr_write_b32 %10, v12\n\t"
    "v_and_b32 v24, 0x7fff7fff, v24\n\t"
    "v_and_b32 v28, 0x7fff7fff, v28\n\t"
    "v_pk_add_u16 v2, v24, -1\n\t"
    "v_pk_add_u16 v9, v28, -1\n\t"
    "v_pk_sub_u16 v24, v24, %15 clamp\n\t"
    "v_pk_sub_u16 v28, v28, %15 clamp\n\t"
    "v_pk_min_u16 v32, v32, v2\n\t"
    "v_pk_min_u16 v32, v32, v9\n\t"
    "v_and_b32 v25, 0x7fff7fff, v25\n\t"
    "v_and_b32 v29, 0x7fff7fff, v29\n\t"
    "v_pk_add_u16 v3, v25, -1\n\t"
    "v_pk_add_u16 v10, v29, -1\n\t"
    "v_pk_sub_u16 v25, v25, %15 clamp\n\t"
    "v_pk_sub_u16 v29, v29, %15 clamp\n\t"
    "v_pk_min_u16 v32, v32, v3\n\t"
    "v_pk_min_u16 v32, v32, v10\n\t"
    "v_and_b32 v26, 0x7fff7fff, v26\n\t"
    "v_and_b32 v30, 0x7fff7fff, v30\n\t"
    "v_pk_add_u16 v2, v26, -1\n\t"
    "v_pk_add_u16 v9, v30, -1\n\t"
    "v_pk_sub_u16 v26, v26, %15 clamp\n\t"
    "v_pk_sub_u16 v30, v30, %15 clamp\n\t"
    "v_pk_min_u16 v32, v32, v2\n\t"
    "v_pk_min_u16 v32, v32, v9\n\t"
    "v_and_b32 v27, 0x7fff7fff, v27\n\t"
    "v_and_b32 v31, 0x7fff7fff, v31\n\t"
    "v_pk_add_u16 v3, v27, -1\n\t"
    "v_pk_add_u16 v10, v31, -1\n\t"
    "v_pk_sub_u16 v27, v27, %15 clamp\n\t"
    "v_pk_sub_u16 v31, v31, %15 clamp\n\t"
    "v_pk_min_u16 v32, v32, v3\n\t"
    "v_pk_min_u16 v32, v32, v10\n\t"
    "v_perm_b32 v2, v25, v24, %17\n\t"
    "v_perm_b32 v9, v29, v28, %17\n\t"
    "v_perm_b32 v3, v27, v26, %17\n\t"
    "v_perm_b32 v10, v31, v30, %17\n\t"
    "v_or3_b32 v33, v33, v2, v3\n\t"
    "v_or3_b32 v33, v33, v9, v10\n\t"
    "v_lshl_or_b32 v6, v3, 4, v2\n\t"
    "v_lshl_or_b32 v13, v10, 4, v9\n\t"
    "v_accvgpr_write_b32 %8, v6\n\t"
    "v_accvgpr_write_b32 %11, v13\n\t"
    "v_lshrrev_b32 v14, 1, v14\n\t"
    "v_and_or_b32 v14, v0, %19, v14\n\t"
    "v_lshrrev_b32 v14, 1, v14\n\t"
    "v_and_or_b32 v14, v1, %19, v14\n\t"
    "v_lshrrev_b32 v14, 1, v14\n\t"
    "v_and_or_b32 v14, v7, %19, v14\n\t"
    "v_lshrrev_b32 v14, 1, v14\n\t"
    "v_and_or_b32 v14, v8, %19, v14\n\t"
    "v_accvgpr_write_b32 %12, v14\n\t"
    "v_pk_sub_u16 v32, %18, v32 clamp\n\t"
    "v_and_b32 v33, 0xf0f0f0f0, v33\n\t"
    "v_or3_b32 %13, %13, v32, v33\n\t"
    : "=&a"(pk[11][0]), "=&a"(pk[11][1]), "=&a"(pk[11][2]), "=&a"(pk[11][3]), "=&a"(pk[11][4]), "=&a"(pk[11][5]), "=&a"(pk[11][6]), "=&a"(pk[11][7]), "=&a"(pk[11][8]), "=&a"(pk[11][9]), "=&a"(pk[11][10]), "=&a"(pk[11][11]), "=&a"(pk[11][12]), "+v"(badv)
    : "v"(laddr), "s"(base2v), "s"(0x06040200u), "s"(0x07050301u), "s"(base2v + 0x00ff00ffu), "s"(0x80808080u), "v"(vofr), "s"(vbase), "s"(ring)
    : "memory", "scc", "v0", "v1", "v2", "v3", "v4", "v5", "v6", "v7", "v8", "v9", "v10", "v11", "v12", "v13", "v14", "v15", "v16", "v17", "v18", "v19", "v20", "v21", "v22", "v23", "v24", "v25", "v26", "v27", "v28", "v29", "v30", "v31", "v32", "v33");
  badmask |= (__any(badv != 0) ? 1u : 0u) << 5; badv = 0;
  ld16_if(gur[2], off_gu + 3584, p.wg, gph, 0);
  ld16_if(gur[3], off_gu + 3584, p.wu, gph, 0);
  pace(tp, 1098);
  asm volatile(
    "s_waitcnt vmcnt(20)\n\t"
    "ds_read_b128 v[16:19], %14 offset:16384\n\t"
    "ds_read_b128 v[20:23], %14 offset:17408\n\t"
    "ds_read_b128 v[24:27], %14 offset:18432\n\t"
    "ds_read_b128 v[28:31], %14 offset:19456\n\t"
    "v_xor_b32 v15, 131072, %20\n\t"
    "s_add_u32 m0, %22, 0\n\t"
    "s_nop 0\n\t"
    "global_load_lds_dwordx4 v15, %21 nt\n\t"
    "global_load_lds_dwordx4 v15, %21 offset:1024 nt\n\t"
    "global_load_lds_dwordx4 v15, %21 offset:2048 nt\n\t"
    "global_load_lds_dwordx4 v15, %21 offset:3072 nt\n\t"
    "v_mov_b32 v32, -1\n\t"
    "v_mov_b32 v33, 0\n\t"
    "s_waitcnt lgkmcnt(2)\n\t"
    "v_perm_b32 v4, v17, v16, %16\n\t"
    "v_perm_b32 v11, v21, v20, %16\n\t"
    "v_perm_b32 v0, v17, v16, %17\n\t"
    "v_perm_b32 v7, v21, v20, %17\n\t"
    "v_accvgpr_write_b32 %0, v4\n\t"
    "v_accvgpr_write_b32 %3, v11\n\t"
    "v_perm_b32 v5, v19, v18, %16\n\t"
    "v_perm_b32 v12, v23, v22, %16\n\t"
    "v_perm_b32 v1, v19, v18, %17\n\t"
    "v_perm_b32 v8, v23, v22, %17\n\t"
    "v_accvgpr_write_b32 %1, v5\n\t"
    "v_accvgpr_write_b32 %4, v12\n\t"
    "v_and_b32 v16, 0x7fff7fff, v16\n\t"
    "v_and_b32 v20, 0x7fff7fff, v20\n\t"
    "v_pk_add_u16 v2, v16, -1\n\t"
    "v_pk_add_u16 v9, v20, -1\n\t"
    "v_pk_sub_u16 v16, v16, %15 clamp\n\t"
    "v_pk_sub_u16 v20, v20, %15 clamp\n\t"
    "v_pk_min_u16 v32, v32, v2\n\t"
    "v_pk_min_u16 v32, v32, v9\n\t"
    "v_and_b32 v17, 0x7fff7fff, v17\n\t"
    "v_and_b32 v21, 0x7fff7fff, v21\n\t"
    "v_pk_add_u16 v3, v17, -1\n\t"
    "v_pk_add_u16 v10, v21, -1\n\t"
    "v_pk_sub_u16 v17, v17, %15 clamp\n\t"
    "v_pk_sub_u16 v21, v21, %15 clamp\n\t"
    "v_pk_min_u16 v32, v32, v3\n\t"
    "v_pk_min_u16 v32, v32, v10\n\t"
    "v_and_b32 v18, 0x7fff7fff, v18\n\t"
    "v_and_b32 v22, 0x7fff7fff, v22\n\t"
    "v_pk_add_u16 v2, v18, -1\n\t"
    "v_pk_add_u16 v9, v22, -1\n\t"
    "v_pk_sub_u16 v18, v18, %15 clamp\n\t"
    "v_pk_sub_u16 v22, v22, %15 clamp\n\t"
    "v_pk_min_u16 v32, v32, v2\n\t"
    "v_pk_min_u16 v32, v32, v9\n\t"
    "v_and_b32 v19, 0x7fff7fff, v19\n\t"
    "v_and_b32 v23, 0x7fff7fff, v23\n\t"
    "v_pk_add_u16 v3, v19, -1\n\t"
    "v_pk_add_u16 v10, v23, -1\n\t"
    "v_pk_sub_u16 v19, v19, %15 clamp\n\t"
    "v_pk_sub_u16 v23, v23, %15 clamp\n\t"
    "v_pk_min_u16 v32, v32, v3\n\t"
    "v_pk_min_u16 v32, v32, v10\n\t"
    "v_perm_b32 v2, v17, v16, %17\n\t"
    "v_perm_b32 v9, v21, v20, %17\n\t"
    "v_perm_b32 v3, v19, v18, %17\n\t"
    "v_perm_b32 v10, v23, v22, %17\n\t"
    "v_or3_b32 v33, v33, v2, v3\n\t"
    "v_or3_b32 v33, v33, v9, v10\n\t"
    "v_lshl_or_b32 v6, v3, 4, v2\n\t"
    "v_lshl_or_b32 v13, v10, 4, v9\n\t"
    "v_accvgpr_write_b32 %2, v6\n\t"
    "v_accvgpr_write_b32 %5, v13\n\t"
    "v_and_b32 v14, %19, v0\n\t"
    "v_lshrrev_b32 v14, 1, v14\n\t"
    "v_and_or_b32 v14, v1, %19, v14\n\t"
    "v_lshrrev_b32 v14, 1, v14\n\t"
    "v_and_or_b32 v14, v7, %19, v14\n\t"
    "v_lshrrev_b32 v14, 1, v14\n\t"
    "v_and_or_b32 v14, v8, %19, v14\n\t"
    "s_waitcnt lgkmcnt(0)\n\t"
    "v_perm_b32 v4, v25, v24, %16\n\t"
    "v_perm_b32 v11, v29, v28, %16\n\t"
    "v_perm_b32 v0, v25, v24, %17\n\t"
    "v_perm_b32 v7, v29, v28, %17\n\t"
    "v_accvgpr_write_b32 %6, v4\n\t"
    "v_accvgpr_write_b32 %9, v11\n\t"
    "v_perm_b32 v5, v27, v26, %16\n\t"
    "v_perm_b32 v12, v31, v30, %16\n\t"
    "v_perm_b32 v1, v27, v26, %17\n\t"
    "v_perm_b32 v8, v31, v30, %17\n\t"
    "v_accvgpr_write_b32 %7, v5\n\t"
    "v_accvgpr_write_b32 %10, v12\n\t"
    "v_and_b32 v24, 0x7fff7fff, v24\n\t"
    "v_and_b32 v28, 0x7fff7fff, v28\n\t"
    "v_pk_add_u16 v2, v24, -1\n\t"
    "v_pk_add_u16 v9, v28, -1\n\t"
    "v_pk_sub_u16 v24, v24, %15 clamp\n\t"
    "v_pk_sub_u16 v28, v28, %15 clamp\n\t"
    "v_pk_min_u16 v32, v32, v2\n\t"
    "v_pk_min_u16 v32, v32, v9\n\t"
    "v_and_b32 v25, 0x7fff7fff, v25\n\t"
    "v_and_b32 v29, 0x7fff7fff, v29\n\t"
    "v_pk_add_u16 v3, v25, -1\n\t"
    "v_pk_add_u16 v10, v29, -1\n\t"
    "v_pk_sub_u16 v25, v25, %15 clamp\n\t"
    "v_pk_sub_u16 v29, v29, %15 clamp\n\t"
    "v_pk_min_u16 v32, v32, v3\n\t"
    "v_pk_min_u16 v32, v32, v10\n\t"
    "v_and_b32 v26, 0x7fff7fff, v26\n\t"
    "v_and_b32 v30, 0x7fff7fff, v30\n\t"
    "v_pk_add_u16 v2, v26, -1\n\t"
    "v_pk_add_u16 v9, v30, -1\n\t"
    "v_pk_sub_u16 v26, v26, %15 clamp\n\t"
    "v_pk_sub_u16 v30, v30, %15 clamp\n\t"
    "v_pk_min_u16 v32, v32, v2\n\t"
    "v_pk_min_u16 v32, v32, v9\n\t"
    "v_and_b32 v27, 0x7fff7fff, v27\n\t"
    "v_and_b32 v31, 0x7fff7fff, v31\n\t"
    "v_pk_add_u16 v3, v27, -1\n\t"
    "v_pk_add_u16 v10, v31, -1\n\t"
    "v_pk_sub_u16 v27, v27, %15 clamp\n\t"
    "v_pk_sub_u16 v31, v31, %15 clamp\n\t"
    "v_pk_min_u16 v32, v32, v3\n\t"
    "v_pk_min_u16 v32, v32, v10\n\t"
    "v_perm_b32 v2, v25, v24, %17\n\t"
    "v_perm_b32 v9, v29, v28, %17\n\t"
    "v_perm_b32 v3, v27, v26, %17\n\t"
    "v_perm_b32 v10, v31, v30, %17\n\t"
    "v_or3_b32 v33, v33, v2, v3\n\t"
    "v_or3_b32 v33, v33, v9, v10\n\t"
    "v_lshl_or_b32 v6, v3, 4, v2\n\t"
    "v_lshl_or_b32 v13, v10, 4, v9\n\t"
    "v_accvgpr_write_b32 %8, v6\n\t"
    "v_accvgpr_write_b32 %11, v13\n\t"
    "v_lshrrev_b32 v14, 1, v14\n\t"
    "v_and_or_b32 v14, v0, %19, v14\n\t"
    "v_lshrrev_b32 v14, 1, v14\n\t"
    "v_and_or_b32 v14, v1, %19, v14\n\t"
    "v_lshrrev_b32 v14, 1, v14\n\t"
    "v_and_or_b32 v14, v7, %19, v14\n\t"
    "v_lshrrev_b32 v14, 1, v14\n\t"
    "v_and_or_b32 v14, v8, %19, v14\n\t"
    "v_accvgpr_write_b32 %12, v14\n\t"
    "v_pk_sub_u16 v32, %18, v32 clamp\n\t"
    "v_and_b32 v33, 0xf0f0f0f0, v33\n\t"
    "v_or3_b32 %13, %13, v32, v33\n\t"
    : "=&a"(pk[12][0]), "=&a"(pk[12][1]), "=&a"(pk[12][2]), "=&a"(pk[12][3]), "=&a"(pk[12][4]), "=&a"(pk[12][5]), "=&a"(pk[12][6]), "=&a"(pk[12][7]), "=&a"(pk[12][8]), "=&a"(pk[12][9]), "=&a"(pk[12][10]), "=&a"(pk[12][11]), "=&a"(pk[12][12]), "+v"(badv)
    : "v"(laddr), "s"(base2k), "s"(0x06040200u), "s"(0x07050301u), "s"(base2k + 0x00ff00ffu), "s"(0x80808080u), "v"(koff), "s"(kbase), "s"(ring)
    : "memory", "scc", "v0", "v1", "v2", "v3", "v4", "v5", "v6", "v7", "v8", "v9", "v10", "v11", "v12", "v13", "v14", "v15", "v16", "v17", "v18", "v19", "v20", "v21", "v22", "v23", "v24", "v25", "v26", "v27", "v28", "v29", "v30", "v31", "v32", "v33");
  ld16_if(gur[0], off_gu + 0, p.wg, gph, 1);
  ld16_if(gur[1], off_gu + 0, p.wu, gph, 1);
  pace(tp, 1159);
  asm volatile(
    "s_waitcnt vmcnt(16)\n\t"
    "ds_read_b128 v[16:19], %14 offset:20480\n\t"
    "ds_read_b128 v[20:23], %14 offset:21504\n\t"
    "ds_read_b128 v[24:27], %14 offset:22528\n\t"
    "ds_read_b128 v[28:31], %14 offset:23552\n\t"
    "v_xor_b32 v15, 131072, %20\n\t"
    "s_add_u32 m0, %22, 4096\n\t"
    "s_nop 0\n\t"
    "global_load_lds_dwordx4 v15, %21 nt\n\t"
    "global_load_lds_dwordx4 v15, %21 offset:1024 nt\n\t"
    "global_load_lds_dwordx4 v15, %21 offset:2048 nt\n\t"
    "global_load_lds_dwordx4 v15, %21 offset:3072 nt\n\t"
    "v_mov_b32 v32, -1\n\t"
    "v_mov_b32 v33, 0\n\t"
    "s_waitcnt lgkmcnt(2)\n\t"
    "v_perm_b32 v4, v17, v16, %16\n\t"
    "v_perm_b32 v11, v21, v20, %16\n\t"
    "v_perm_b32 v0, v17, v16, %17\n\t"
    "v_perm_b32 v7, v21, v20, %17\n\t"
    "v_accvgpr_write_b32 %0, v4\n\t"
    "v_accvgpr_write_b32 %3, v11\n\t"
    "v_perm_b32 v5, v19, v18, %16\n\t"
    "v_perm_b32 v12, v23, v22, %16\n\t"
    "v_perm_b32 v1, v19, v18, %17\n\t"
    "v_perm_b32 v8, v23, v22, %17\n\t"
    "v_accvgpr_write_b32 %1, v5\n\t"
    "v_accvgpr_write_b32 %4, v12\n\t"
    "v_and_b32 v16, 0x7fff7fff, v16\n\t"
    "v_and_b32 v20, 0x7fff7fff, v20\n\t"
    "v_pk_add_u16 v2, v16, -1\n\t"
    "v_pk_add_u16 v9, v20, -1\n\t"
    "v_pk_sub_u16 v16, v16, %15 clamp\n\t"
    "v_pk_sub_u16 v20, v20, %15 clamp\n\t"
    "v_pk_min_u16 v32, v32, v2\n\t"
    "v_pk_min_u16 v32, v32, v9\n\t"
    "v_and_b32 v17, 0x7fff7fff, v17\n\t"
    "v_and_b32 v21, 0x7fff7fff, v21\n\t"
    "v_pk_add_u16 v3, v17, -1\n\t"
    "v_pk_add_u16 v10, v21, -1\n\t"
    "v_pk_sub_u16 v17, v17, %15 clamp\n\t"
    "v_pk_sub_u16 v21, v21, %15 clamp\n\t"
    "v_pk_min_u16 v32, v32, v3\n\t"
    "v_pk_min_u16 v32, v32, v10\n\t"
    "v_and_b32 v18, 0x7fff7fff, v18\n\t"
    "v_and_b32 v22, 0x7fff7fff, v22\n\t"
    "v_pk_add_u16 v2, v18, -1\n\t"
    "v_pk_add_u16 v9, v22, -1\n\t"
    "v_pk_sub_u16 v18, v18, %15 clamp\n\t"
    "v_pk_sub_u16 v22, v22, %15 clamp\n\t"
    "v_pk_min_u16 v32, v32, v2\n\t"
    "v_pk_min_u16 v32, v32, v9\n\t"
    "v_and_b32 v19, 0x7fff7fff, v19\n\t"
    "v_and_b32 v23, 0x7fff7fff, v23\n\t"
    "v_pk_add_u16 v3, v19, -1\n\t"
    "v_pk_add_u16 v10, v23, -1\n\t"
    "v_pk_sub_u16 v19, v19, %15 clamp\n\t"
    "v_pk_sub_u16 v23, v23, %15 clamp\n\t"
    "v_pk_min_u16 v32, v32, v3\n\t"
    "v_pk_min_u16 v32, v32, v10\n\t"
    "v_perm_b32 v2, v17, v16, %17\n\t"
    "v_perm_b32 v9, v21, v20, %17\n\t"
    "v_perm_b32 v3, v19, v18, %17\n\t"
    "v_perm_b32 v10, v23, v22, %17\n\t"
    "v_or3_b32 v33, v33, v2, v3\n\t"
    "v_or3_b32 v33, v33, v9, v10\n\t"
    "v_lshl_or_b32 v6, v3, 4, v2\n\t"
    "v_lshl_or_b32 v13, v10, 4, v9\n\t"
    "v_accvgpr_write_b32 %2, v6\n\t"
    "v_accvgpr_write_b32 %5, v13\n\t"
    "v_and_b32 v14, %19, v0\n\t"
    "v_lshrrev_b32 v14, 1, v14\n\t"
    "v_and_or_b32 v14, v1, %19, v14\n\t"
    "v_lshrrev_b32 v14, 1, v14\n\t"
    "v_and_or_b32 v14, v7, %19, v14\n\t"
    "v_lshrrev_b32 v14, 1, v14\n\t"
    "v_and_or_b32 v14, v8, %19, v14\n\t"
    "s_waitcnt lgkmcnt(0)\n\t"
    "v_perm_b32 v4, v25, v24, %16\n\t"
    "v_perm_b32 v11, v29, v28, %16\n\t"
    "v_perm_b32 v0, v25, v24, %17\n\t"
    "v_perm_b32 v7, v29, v28, %17\n\t"
    "v_accvgpr_write_b32 %6, v4\n\t"
    "v_accvgpr_write_b32 %9, v11\n\t"
    "v_perm_b32 v5, v27, v26, %16\n\t"
    "v_perm_b32 v12, v31, v30, %16\n\t"
    "v_perm_b32 v1, v27, v26, %17\n\t"
    "v_perm_b32 v8, v31, v30, %17\n\t"
    "v_accvgpr_write_b32 %7, v5\n\t"
    "v_accvgpr_write_b32 %10, v12\n\t"
    "v_and_b32 v24, 0x7fff7fff, v24\n\t"
    "v_and_b32 v28, 0x7fff7fff, v28\n\t"
    "v_pk_add_u16 v2, v24, -1\n\t"
    "v_pk_add_u16 v9, v28, -1\n\t"
    "v_pk_sub_u16 v24, v24, %15 clamp\n\t"
    "v_pk_sub_u16 v28, v28, %15 clamp\n\t"
    "v_pk_min_u16 v32, v32, v2\n\t"
    "v_pk_min_u16 v32, v32, v9\n\t"
    "v_and_b32 v25, 0x7fff7fff, v25\n\t"
    "v_and_b32 v29, 0x7fff7fff, v29\n\t"
    "v_pk_add_u16 v3, v25, -1\n\t"
    "v_pk_add_u16 v10, v29, -1\n\t"
    "v_pk_sub_u16 v25, v25, %15 clamp\n\t"
    "v_pk_sub_u16 v29, v29, %15 clamp\n\t"
    "v_pk_min_u16 v32, v32, v3\n\t"
    "v_pk_min_u16 v32, v32, v10\n\t"
    "v_and_b32 v26, 0x7fff7fff, v26\n\t"
    "v_and_b32 v30, 0x7fff7fff, v30\n\t"
    "v_pk_add_u16 v2, v26, -1\n\t"
    "v_pk_add_u16 v9, v30, -1\n\t"
    "v_pk_sub_u16 v26, v26, %15 clamp\n\t"
    "v_pk_sub_u16 v30, v30, %15 clamp\n\t"
    "v_pk_min_u16 v32, v32, v2\n\t"
    "v_pk_min_u16 v32, v32, v9\n\t"
    "v_and_b32 v27, 0x7fff7fff, v27\n\t"
    "v_and_b32 v31, 0x7fff7fff, v31\n\t"
    "v_pk_add_u16 v3, v27, -1\n\t"
    "v_pk_add_u16 v10, v31, -1\n\t"
    "v_pk_sub_u16 v27, v27, %15 clamp\n\t"
    "v_pk_sub_u16 v31, v31, %15 clamp\n\t"
    "v_pk_min_u16 v32, v32, v3\n\t"
    "v_pk_min_u16 v32, v32, v10\n\t"
    "v_perm_b32 v2, v25, v24, %17\n\t"
    "v_perm_b32 v9, v29, v28, %17\n\t"
    "v_perm_b32 v3, v27, v26, %17\n\t"
    "v_perm_b32 v10, v31, v30, %17\n\t"
    "v_or3_b32 v33, v33, v2, v3\n\t"
    "v_or3_b32 v33, v33, v9, v10\n\t"
    "v_lshl_or_b32 v6, v3, 4, v2\n\t"
    "v_lshl_or_b32 v13, v10, 4, v9\n\t"
    "v_accvgpr_write_b32 %8, v6\n\t"
    "v_accvgpr_write_b32 %11, v13\n\t"
    "v_lshrrev_b32 v14, 1, v14\n\t"
    "v_and_or_b32 v14, v0, %19, v14\n\t"
    "v_lshrrev_b32 v14, 1, v14\n\t"
    "v_and_or_b32 v14, v1, %19, v14\n\t"
    "v_lshrrev_b32 v14, 1, v14\n\t"
    "v_and_or_b32 v14, v7, %19, v14\n\t"
    "v_lshrrev_b32 v14, 1, v14\n\t"
    "v_and_or_b32 v14, v8, %19, v14\n\t"
    "v_accvgpr_write_b32 %12, v14\n\t"
    "v_pk_sub_u16 v32, %18, v32 clamp\n\t"
    "v_and_b32 v33, 0xf0f0f0f0, v33\n\t"
    "v_or3_b32 %13, %13, v32, v33\n\t"
    : "=&a"(pk[13][0]), "=&a"(pk[13][1]), "=&a"(pk[13][2]), "=&a"(pk[13][3]), "=&a"(pk[13][4]), "=&a"(pk[13][5]), "=&a"(pk[13][6]), "=&a"(pk[13][7]), "=&a"(pk[13][8]), "=&a"(pk[13][9]), "=&a"(pk[13][10]), "=&a"(pk[13][11]), "=&a"(pk[13][12]), "+v"(badv)
    : "v"(laddr), "s"(base2v), "s"(0x06040200u), "s"(0x07050301u), "s"(base2v + 0x00ff00ffu), "s"(0x80808080u), "v"(vofr), "s"(vbase), "s"(ring)
    : "memory", "scc", "v0", "v1", "v2", "v3", "v4", "v5", "v6", "v7", "v8", "v9", "v10", "v11", "v12", "v13", "v14", "v15", "v16", "v17", "v18", "v19", "v20", "v21", "v22", "v23", "v24", "v25", "v26", "v27", "v28", "v29", "v30", "v31", "v32", "v33");
  badmask |= (__any(badv != 0) ? 1u : 0u) << 6; badv = 0;
  ld16_if(gur[2], off_gu + 3584, p.wg, gph, 1);
  ld16_if(gur[3], off_gu + 3584, p.wu, gph, 1);
  pace(tp, 1220);
  asm volatile(
    "s_waitcnt vmcnt(12)\n\t"
    "ds_read_b128 v[16:19], %14 offset:24576\n\t"
    "ds_read_b128 v[20:23], %14 offset:25600\n\t"
    "ds_read_b128 v[24:27], %14 offset:26624\n\t"
    "ds_read_b128 v[28:31], %14 offset:27648\n\t"
    "v_xor_b32 v15, 147456, %20\n\t"
    "s_add_u32 m0, %22, 8192\n\t"
    "s_nop 0\n\t"
    "global_load_lds_dwordx4 v15, %21 nt\n\t"
    "global_load_lds_dwordx4 v15, %21 offset:1024 nt\n\t"
    "global_load_lds_dwordx4 v15, %21 offset:2048 nt\n\t"
    "global_load_lds_dwordx4 v15, %21 offset:3072 nt\n\t"
    "v_mov_b32 v32, -1\n\t"
    "v_mov_b32 v33, 0\n\t"
    "s_waitcnt lgkmcnt(2)\n\t"
    "v_perm_b32 v4, v17, v16, %16\n\t"
    "v_perm_b32 v11, v21, v20, %16\n\t"
    "v_perm_b32 v0, v17, v16, %17\n\t"
    "v_perm_b32 v7, v21, v20, %17\n\t"
    "v_accvgpr_write_b32 %0, v4\n\t"
    "v_accvgpr_write_b32 %3, v11\n\t"
    "v_perm_b32 v5, v19, v18, %16\n\t"
    "v_perm_b32 v12, v23, v22, %16\n\t"
    "v_perm_b32 v1, v19, v18, %17\n\t"
    "v_perm_b32 v8, v23, v22, %17\n\t"
    "v_accvgpr_write_b32 %1, v5\n\t"
    "v_accvgpr_write_b32 %4, v12\n\t"
    "v_and_b32 v16, 0x7fff7fff, v16\n\t"
    "v_and_b32 v20, 0x7fff7fff, v20\n\t"
    "v_pk_add_u16 v2, v16, -1\n\t"
    "v_pk_add_u16 v9, v20, -1\n\t"
    "v_pk_sub_u16 v16, v16, %15 clamp\n\t"
    "v_pk_sub_u16 v20, v20, %15 clamp\n\t"
    "v_pk_min_u16 v32, v32, v2\n\t"
    "v_pk_min_u16 v32, v32, v9\n\t"
    "v_and_b32 v17, 0x7fff7fff, v17\n\t"
    "v_and_b32 v21, 0x7fff7fff, v21\n\t"
    "v_pk_add_u16 v3, v17, -1\n\t"
    "v_pk_add_u16 v10, v21, -1\n\t"
    "v_pk_sub_u16 v17, v17, %15 clamp\n\t"
    "v_pk_sub_u16 v21, v21, %15 clamp\n\t"
    "v_pk_min_u16 v32, v32, v3\n\t"
    "v_pk_min_u16 v32, v32, v10\n\t"
    "v_and_b32 v18, 0x7fff7fff, v18\n\t"
    "v_and_b32 v22, 0x7fff7fff, v22\n\t"
    "v_pk_add_u16 v2, v18, -1\n\t"
    "v_pk_add_u16 v9, v22, -1\n\t"
    "v_pk_sub_u16 v18, v18, %15 clamp\n\t"
    "v_pk_sub_u16 v22, v22, %15 clamp\n\t"
    "v_pk_min_u16 v32, v32, v2\n\t"
    "v_pk_min_u16 v32, v32, v9\n\t"
    "v_and_b32 v19, 0x7fff7fff, v19\n\t"
    "v_and_b32 v23, 0x7fff7fff, v23\n\t"
    "v_pk_add_u16 v3, v19, -1\n\t"
    "v_pk_add_u16 v10, v23, -1\n\t"
    "v_pk_sub_u16 v19, v19, %15 clamp\n\t"
    "v_pk_sub_u16 v23, v23, %15 clamp\n\t"
    "v_pk_min_u16 v32, v32, v3\n\t"
    "v_pk_min_u16 v32, v32, v10\n\t"
    "v_perm_b32 v2, v17, v16, %17\n\t"
    "v_perm_b32 v9, v21, v20, %17\n\t"
    "v_perm_b32 v3, v19, v18, %17\n\t"
    "v_perm_b32 v10, v23, v22, %17\n\t"
    "v_or3_b32 v33, v33, v2, v3\n\t"
    "v_or3_b32 v33, v33, v9, v10\n\t"
    "v_lshl_or_b32 v6, v3, 4, v2\n\t"
    "v_lshl_or_b32 v13, v10, 4, v9\n\t"
    "v_accvgpr_write_b32 %2, v6\n\t"
    "v_accvgpr_write_b32 %5, v13\n\t"
    "v_and_b32 v14, %19, v0\n\t"
    "v_lshrrev_b32 v14, 1, v14\n\t"
    "v_and_or_b32 v14, v1, %19, v14\n\t"
    "v_lshrrev_b32 v14, 1, v14\n\t"
    "v_and_or_b32 v14, v7, %19, v14\n\t"
    "v_lshrrev_b32 v14, 1, v14\n\t"
    "v_and_or_b32 v14, v8, %19, v14\n\t"
    "s_waitcnt lgkmcnt(0)\n\t"
    "v_perm_b32 v4, v25, v24, %16\n\t"
    "v_perm_b32 v11, v29, v28, %16\n\t"
    "v_perm_b32 v0, v25, v24, %17\n\t"
    "v_perm_b32 v7, v29, v28, %17\n\t"
    "v_accvgpr_write_b32 %6, v4\n\t"
    "v_accvgpr_write_b32 %9, v11\n\t"
    "v_perm_b32 v5, v27, v26, %16\n\t"
    "v_perm_b32 v12, v31, v30, %16\n\t"
    "v_perm_b32 v1, v27, v26, %17\n\t"
    "v_perm_b32 v8, v31, v30, %17\n\t"
    "v_accvgpr_write_b32 %7, v5\n\t"
    "v_accvgpr_write_b32 %10, v12\n\t"
    "v_and_b32 v24, 0x7fff7fff, v24\n\t"
    "v_and_b32 v28, 0x7fff7fff, v28\n\t"
    "v_pk_add_u16 v2, v24, -1\n\t"
    "v_pk_add_u16 v9, v28, -1\n\t"
    "v_pk_sub_u16 v24, v24, %15 clamp\n\t"
    "v_pk_sub_u16 v28, v28, %15 clamp\n\t"
    "v_pk_min_u16 v32, v32, v2\n\t"
    "v_pk_min_u16 v32, v32, v9\n\t"
    "v_and_b32 v25, 0x7fff7fff, v25\n\t"
    "v_and_b32 v29, 0x7fff7fff, v29\n\t"
    "v_pk_add_u16 v3, v25, -1\n\t"
    "v_pk_add_u16 v10, v29, -1\n\t"
    "v_pk_sub_u16 v25, v25, %15 clamp\n\t"
    "v_pk_sub_u16 v29, v29, %15 clamp\n\t"
    "v_pk_min_u16 v32, v32, v3\n\t"
    "v_pk_min_u16 v32, v32, v10\n\t"
    "v_and_b32 v26, 0x7fff7fff, v26\n\t"
    "v_and_b32 v30, 0x7fff7fff, v30\n\t"
    "v_pk_add_u16 v2, v26, -1\n\t"
    "v_pk_add_u16 v9, v30, -1\n\t"
    "v_pk_sub_u16 v26, v26, %15 clamp\n\t"
    "v_pk_sub_u16 v30, v30, %15 clamp\n\t"
    "v_pk_min_u16 v32, v32, v2\n\t"
    "v_pk_min_u16 v32, v32, v9\n\t"
    "v_and_b32 v27, 0x7fff7fff, v27\n\t"
    "v_and_b32 v31, 0x7fff7fff, v31\n\t"
    "v_pk_add_u16 v3, v27, -1\n\t"
    "v_pk_add_u16 v10, v31, -1\n\t"
    "v_pk_sub_u16 v27, v27, %15 clamp\n\t"
    "v_pk_sub_u16 v31, v31, %15 clamp\n\t"
    "v_pk_min_u16 v32, v32, v3\n\t"
    "v_pk_min_u16 v32, v32, v10\n\t"
    "v_perm_b32 v2, v25, v24, %17\n\t"
    "v_perm_b32 v9, v29, v28, %17\n\t"
    "v_perm_b32 v3, v27, v26, %17\n\t"
    "v_perm_b32 v10, v31, v30, %17\n\t"
    "v_or3_b32 v33, v33, v2, v3\n\t"
    "v_or3_b32 v33, v33, v9, v10\n\t"
    "v_lshl_or_b32 v6, v3, 4, v2\n\t"
    "v_lshl_or_b32 v13, v10, 4, v9\n\t"
    "v_accvgpr_write_b32 %8, v6\n\t"
    "v_accvgpr_write_b32 %11, v13\n\t"
    "v_lshrrev_b32 v14, 1, v14\n\t"
    "v_and_or_b32 v14, v0, %19, v14\n\t"
    "v_lshrrev_b32 v14, 1, v14\n\t"
    "v_and_or_b32 v14, v1, %19, v14\n\t"
    "v_lshrrev_b32 v14, 1, v14\n\t"
    "v_and_or_b32 v14, v7, %19, v14\n\t"
    "v_lshrrev_b32 v14, 1, v14\n\t"
    "v_and_or_b32 v14, v8, %19, v14\n\t"
    "v_accvgpr_write_b32 %12, v14\n\t"
    "v_pk_sub_u16 v32, %18, v32 clamp\n\t"
    "v_and_b32 v33, 0xf0f0f0f0, v33\n\t"
    "v_or3_b32 %13, %13, v32, v33\n\t"
    : "=&a"(pk[14][0]), "=&a"(pk[14][1]), "=&a"(pk[14][2]), "=&a"(pk[14][3]), "=&a"(pk[14][4]), "=&a"(pk[14][5]), "=&a"(pk[14][6]), "=&a"(pk[14][7]), "=&a"(pk[14][8]), "=&a"(pk[14][9]), "=&a"(pk[14][10]), "=&a"(pk[14][11]), "=&a"(pk[14][12]), "+v"(badv)
    : "v"(laddr), "s"(base2k), "s"(0x06040200u), "s"(0x07050301u), "s"(base2k + 0x00ff00ffu), "s"(0x80808080u), "v"(koff), "s"(kbase), "s"(ring)
    : "memory", "scc", "v0", "v1", "v2", "v3", "v4", "v5", "v6", "v7", "v8", "v9", "v10", "v11", "v12", "v13", "v14", "v15", "v16", "v17", "v18", "v19", "v20", "v21", "v22", "v23", "v24", "v25", "v26", "v27", "v28", "v29", "v30", "v31", "v32", "v33");
  ld16_if(gur[0], off_gu + 0, p.wg, gph, 2);
  ld16_if(gur[1], off_gu + 0, p.wu, gph, 2);
  pace(tp, 1281);
  asm volatile(
    "s_waitcnt vmcnt(12)\n\t"
    "ds_read_b128 v[16:19], %14 offset:28672\n\t"
    "ds_read_b128 v[20:23], %14 offset:29696\n\t"
    "ds_read_b128 v[24:27], %14 offset:30720\n\t"
    "ds_read_b128 v[28:31], %14 offset:31744\n\t"
    "v_xor_b32 v15, 147456, %20\n\t"
    "s_add_u32 m0, %22, 12288\n\t"
    "s_nop 0\n\t"
    "global_load_lds_dwordx4 v15, %21 nt\n\t"
    "global_load_lds_dwordx4 v15, %21 offset:1024 nt\n\t"
    "global_load_lds_dwordx4 v15, %21 offset:2048 nt\n\t"
    "global_load_lds_dwordx4 v15, %21 offset:3072 nt\n\t"
    "v_mov_b32 v32, -1\n\t"
    "v_mov_b32 v33, 0\n\t"
    "s_waitcnt lgkmcnt(2)\n\t"
    "v_perm_b32 v4, v17, v16, %16\n\t"
    "v_perm_b32 v11, v21, v20, %16\n\t"
    "v_perm_b32 v0, v17, v16, %17\n\t"
    "v_perm_b32 v7, v21, v20, %17\n\t"
    "v_accvgpr_write_b32 %0, v4\n\t"
    "v_accvgpr_write_b32 %3, v11\n\t"
    "v_perm_b32 v5, v19, v18, %16\n\t"
    "v_perm_b32 v12, v23, v22, %16\n\t"
    "v_perm_b32 v1, v19, v18, %17\n\t"
    "v_perm_b32 v8, v23, v22, %17\n\t"
    "v_accvgpr_write_b32 %1, v5\n\t"
    "v_accvgpr_write_b32 %4, v12\n\t"
    "v_and_b32 v16, 0x7fff7fff, v16\n\t"
    "v_and_b32 v20, 0x7fff7fff, v20\n\t"
    "v_pk_add_u16 v2, v16, -1\n\t"
    "v_pk_add_u16 v9, v20, -1\n\t"
    "v_pk_sub_u16 v16, v16, %15 clamp\n\t"
    "v_pk_sub_u16 v20, v20, %15 clamp\n\t"
    "v_pk_min_u16 v32, v32, v2\n\t"
    "v_pk_min_u16 v32, v32, v9\n\t"
    "v_and_b32 v17, 0x7fff7fff, v17\n\t"
    "v_and_b32 v21, 0x7fff7fff, v21\n\t"
    "v_pk_add_u16 v3, v17, -1\n\t"
    "v_pk_add_u16 v10, v21, -1\n\t"
    "v_pk_sub_u16 v17, v17, %15 clamp\n\t"
    "v_pk_sub_u16 v21, v21, %15 clamp\n\t"
    "v_pk_min_u16 v32, v32, v3\n\t"
    "v_pk_min_u16 v32, v32, v10\n\t"
    "v_and_b32 v18, 0x7fff7fff, v18\n\t"
    "v_and_b32 v22, 0x7fff7fff, v22\n\t"
    "v_pk_add_u16 v2, v18, -1\n\t"
    "v_pk_add_u16 v9, v22, -1\n\t"
    "v_pk_sub_u16 v18, v18, %15 clamp\n\t"
    "v_pk_sub_u16 v22, v22, %15 clamp\n\t"
    "v_pk_min_u16 v32, v32, v2\n\t"
    "v_pk_min_u16 v32, v32, v9\n\t"
    "v_and_b32 v19, 0x7fff7fff, v19\n\t"
    "v_and_b32 v23, 0x7fff7fff, v23\n\t"
    "v_pk_add_u16 v3, v19, -1\n\t"
    "v_pk_add_u16 v10, v23, -1\n\t"
    "v_pk_sub_u16 v19, v19, %15 clamp\n\t"
    "v_pk_sub_u16 v23, v23, %15 clamp\n\t"
    "v_pk_min_u16 v32, v32, v3\n\t"
    "v_pk_min_u16 v32, v32, v10\n\t"
    "v_perm_b32 v2, v17, v16, %17\n\t"
    "v_perm_b32 v9, v21, v20, %17\n\t"
    "v_perm_b32 v3, v19, v18, %17\n\t"
    "v_perm_b32 v10, v23, v22, %17\n\t"
    "v_or3_b32 v33, v33, v2, v3\n\t"
    "v_or3_b32 v33, v33, v9, v10\n\t"
    "v_lshl_or_b32 v6, v3, 4, v2\n\t"
    "v_lshl_or_b32 v13, v10, 4, v9\n\t"
    "v_accvgpr_write_b32 %2, v6\n\t"
    "v_accvgpr_write_b32 %5, v13\n\t"
    "v_and_b32 v14, %19, v0\n\t"
    "v_lshrrev_b32 v14, 1, v14\n\t"
    "v_and_or_b32 v14, v1, %19, v14\n\t"
    "v_lshrrev_b32 v14, 1, v14\n\t"
    "v_and_or_b32 v14, v7, %19, v14\n\t"
    "v_lshrrev_b32 v14, 1, v14\n\t"
    "v_and_or_b32 v14, v8, %19, v14\n\t"
    "s_waitcnt lgkmcnt(0)\n\t"
    "v_perm_b32 v4, v25, v24, %16\n\t"
    "v_perm_b32 v11, v29, v28, %16\n\t"
    "v_perm_b32 v0, v25, v24, %17\n\t"
    "v_perm_b32 v7, v29, v28, %17\n\t"
    "v_accvgpr_write_b32 %6, v4\n\t"
    "v_accvgpr_write_b32 %9, v11\n\t"
    "v_perm_b32 v5, v27, v26, %16\n\t"
    "v_perm_b32 v12, v31, v30, %16\n\t"
    "v_perm_b32 v1, v27, v26, %17\n\t"
    "v_perm_b32 v8, v31, v30, %17\n\t"
    "v_accvgpr_write_b32 %7, v5\n\t"
    "v_accvgpr_write_b32 %10, v12\n\t"
    "v_and_b32 v24, 0x7fff7fff, v24\n\t"
    "v_and_b32 v28, 0x7fff7fff, v28\n\t"
    "v_pk_add_u16 v2, v24, -1\n\t"
    "v_pk_add_u16 v9, v28, -1\n\t"
    "v_pk_sub_u16 v24, v24, %15 clamp\n\t"
    "v_pk_sub_u16 v28, v28, %15 clamp\n\t"
    "v_pk_min_u16 v32, v32, v2\n\t"
    "v_pk_min_u16 v32, v32, v9\n\t"
    "v_and_b32 v25, 0x7fff7fff, v25\n\t"
    "v_and_b32 v29, 0x7fff7fff, v29\n\t"
    "v_pk_add_u16 v3, v25, -1\n\t"
    "v_pk_add_u16 v10, v29, -1\n\t"
    "v_pk_sub_u16 v25, v25, %15 clamp\n\t"
    "v_pk_sub_u16 v29, v29, %15 clamp\n\t"
    "v_pk_min_u16 v32, v32, v3\n\t"
    "v_pk_min_u16 v32, v32, v10\n\t"
    "v_and_b32 v26, 0x7fff7fff, v26\n\t"
    "v_and_b32 v30, 0x7fff7fff, v30\n\t"
    "v_pk_add_u16 v2, v26, -1\n\t"
    "v_pk_add_u16 v9, v30, -1\n\t"
    "v_pk_sub_u16 v26, v26, %15 clamp\n\t"
    "v_pk_sub_u16 v30, v30, %15 clamp\n\t"
    "v_pk_min_u16 v32, v32, v2\n\t"
    "v_pk_min_u16 v32, v32, v9\n\t"
    "v_and_b32 v27, 0x7fff7fff, v27\n\t"
    "v_and_b32 v31, 0x7fff7fff, v31\n\t"
    "v_pk_add_u16 v3, v27, -1\n\t"
    "v_pk_add_u16 v10, v31, -1\n\t"
    "v_pk_sub_u16 v27, v27, %15 clamp\n\t"
    "v_pk_sub_u16 v31, v31, %15 clamp\n\t"
    "v_pk_min_u16 v32, v32, v3\n\t"
    "v_pk_min_u16 v32, v32, v10\n\t"
    "v_perm_b32 v2, v25, v24, %17\n\t"
    "v_perm_b32 v9, v29, v28, %17\n\t"
    "v_perm_b32 v3, v27, v26, %17\n\t"
    "v_perm_b32 v10, v31, v30, %17\n\t"
    "v_or3_b32 v33, v33, v2, v3\n\t"
    "v_or3_b32 v33, v33, v9, v10\n\t"
    "v_lshl_or_b32 v6, v3, 4, v2\n\t"
    "v_lshl_or_b32 v13, v10, 4, v9\n\t"
    "v_accvgpr_write_b32 %8, v6\n\t"
    "v_accvgpr_write_b32 %11, v13\n\t"
    "v_lshrrev_b32 v14, 1, v14\n\t"
    "v_and_or_b32 v14, v0, %19, v14\n\t"
    "v_lshrrev_b32 v14, 1, v14\n\t"
    "v_and_or_b32 v14, v1, %19, v14\n\t"
    "v_lshrrev_b32 v14, 1, v14\n\t"
    "v_and_or_b32 v14, v7, %19, v14\n\t"
    "v_lshrrev_b32 v14, 1, v14\n\t"
    "v_and_or_b32 v14, v8, %19, v14\n\t"
    "v_accvgpr_write_b32 %12, v14\n\t"
    "v_pk_sub_u16 v32, %18, v32 clamp\n\t"
    "v_and_b32 v33, 0xf0f0f0f0, v33\n\t"
    "v_or3_b32 %13, %13, v32, v33\n\t"
    : "=&a"(pk[15][0]), "=&a"(pk[15][1]), "=&a"(pk[15][2]), "=&a"(pk[15][3]), "=&a"(pk[15][4]), "=&a"(pk[15][5]), "=&a"(pk[15][6]), "=&a"(pk[15][7]), "=&a"(pk[15][8]), "=&a"(pk[15][9]), "=&a"(pk[15][10]), "=&a"(pk[15][11]), "=&a"(pk[15][12]), "+v"(badv)
    : "v"(laddr), "s"(base2v), "s"(0x06040200u), "s"(0x07050301u), "s"(base2v + 0x00ff00ffu), "s"(0x80808080u), "v"(vofr), "s"(vbase), "s"(ring)
    : "memory", "scc", "v0", "v1", "v2", "v3", "v4", "v5", "v6", "v7", "v8", "v9", "v10", "v11", "v12", "v13", "v14", "v15", "v16", "v17", "v18", "v19", "v20", "v21", "v22", "v23", "v24", "v25", "v26", "v27", "v28", "v29", "v30", "v31", "v32", "v33");
  badmask |= (__any(badv != 0) ? 1u : 0u) << 7; badv = 0;
  ld16_if(gur[2], off_gu + 3584, p.wg, gph, 2);
  ld16_if(gur[3], off_gu + 3584, p.wu, gph, 2);
  pace(tp, 1342);
  asm volatile(
    "s_waitcnt vmcnt(12)\n\t"
    "ds_read_b128 v[16:19], %14 offset:0\n\t"
    "ds_read_b128 v[20:23], %14 offset:1024\n\t"
    "ds_read_b128 v[24:27], %14 offset:2048\n\t"
    "ds_read_b128 v[28:31], %14 offset:3072\n\t"
    "v_xor_b32 v15, 163840, %20\n\t"
    "s_add_u32 m0, %22, 16384\n\t"
    "s_nop 0\n\t"
    "global_load_lds_dwordx4 v15, %21 nt\n\t"
    "global_load_lds_dwordx4 v15, %21 offset:1024 nt\n\t"
    "global_load_lds_dwordx4 v15, %21 offset:2048 nt\n\t"
    "global_load_lds_dwordx4 v15, %21 offset:3072 nt\n\t"
    "v_mov_b32 v32, -1\n\t"
    "v_mov_b32 v33, 0\n\t"
    "s_waitcnt lgkmcnt(2)\n\t"
    "v_perm_b32 v4, v17, v16, %16\n\t"
    "v_perm_b32 v11, v21, v20, %16\n\t"
    "v_perm_b32 v0, v17, v16, %17\n\t"
    "v_perm_b32 v7, v21, v20, %17\n\t"
    "v_accvgpr_write_b32 %0, v4\n\t"
    "v_accvgpr_write_b32 %3, v11\n\t"
    "v_perm_b32 v5, v19, v18, %16\n\t"
    "v_perm_b32 v12, v23, v22, %16\n\t"
    "v_perm_b32 v1, v19, v18, %17\n\t"
    "v_perm_b32 v8, v23, v22, %17\n\t"
    "v_accvgpr_write_b32 %1, v5\n\t"
    "v_accvgpr_write_b32 %4, v12\n\t"
    "v_and_b32 v16, 0x7fff7fff, v16\n\t"
    "v_and_b32 v20, 0x7fff7fff, v20\n\t"
    "v_pk_add_u16 v2, v16, -1\n\t"
    "v_pk_add_u16 v9, v20, -1\n\t"
    "v_pk_sub_u16 v16, v16, %15 clamp\n\t"
    "v_pk_sub_u16 v20, v20, %15 clamp\n\t"
    "v_pk_min_u16 v32, v32, v2\n\t"
    "v_pk_min_u16 v32, v32, v9\n\t"
    "v_and_b32 v17, 0x7fff7fff, v17\n\t"
    "v_and_b32 v21, 0x7fff7fff, v21\n\t"
    "v_pk_add_u16 v3, v17, -1\n\t"
    "v_pk_add_u16 v10, v21, -1\n\t"
    "v_pk_sub_u16 v17, v17, %15 clamp\n\t"
    "v_pk_sub_u16 v21, v21, %15 clamp\n\t"
    "v_pk_min_u16 v32, v32, v3\n\t"
    "v_pk_min_u16 v32, v32, v10\n\t"
    "v_and_b32 v18, 0x7fff7fff, v18\n\t"
    "v_and_b32 v22, 0x7fff7fff, v22\n\t"
    "v_pk_add_u16 v2, v18, -1\n\t"
    "v_pk_add_u16 v9, v22, -1\n\t"
    "v_pk_sub_u16 v18, v18, %15 clamp\n\t"
    "v_pk_sub_u16 v22, v22, %15 clamp\n\t"
    "v_pk_min_u16 v32, v32, v2\n\t"
    "v_pk_min_u16 v32, v32, v9\n\t"
    "v_and_b32 v19, 0x7fff7fff, v19\n\t"
    "v_and_b32 v23, 0x7fff7fff, v23\n\t"
    "v_pk_add_u16 v3, v19, -1\n\t"
    "v_pk_add_u16 v10, v23, -1\n\t"
    "v_pk_sub_u16 v19, v19, %15 clamp\n\t"
    "v_pk_sub_u16 v23, v23, %15 clamp\n\t"
    "v_pk_min_u16 v32, v32, v3\n\t"
    "v_pk_min_u16 v32, v32, v10\n\t"
    "v_perm_b32 v2, v17, v16, %17\n\t"
    "v_perm_b32 v9, v21, v20, %17\n\t"
    "v_perm_b32 v3, v19, v18, %17\n\t"
    "v_perm_b32 v10, v23, v22, %17\n\t"
    "v_or3_b32 v33, v33, v2, v3\n\t"
    "v_or3_b32 v33, v33, v9, v10\n\t"
    "v_lshl_or_b32 v6, v3, 4, v2\n\t"
    "v_lshl_or_b32 v13, v10, 4, v9\n\t"
    "v_accvgpr_write_b32 %2, v6\n\t"
    "v_accvgpr_write_b32 %5, v13\n\t"
    "v_and_b32 v14, %19, v0\n\t"
    "v_lshrrev_b32 v14, 1, v14\n\t"
    "v_and_or_b32 v14, v1, %19, v14\n\t"
    "v_lshrrev_b32 v14, 1, v14\n\t"
    "v_and_or_b32 v14, v7, %19, v14\n\t"
    "v_lshrrev_b32 v14, 1, v14\n\t"
    "v_and_or_b32 v14, v8, %19, v14\n\t"
    "s_waitcnt lgkmcnt(0)\n\t"
    "v_perm_b32 v4, v25, v24, %16\n\t"
    "v_perm_b32 v11, v29, v28, %16\n\t"
    "v_perm_b32 v0, v25, v24, %17\n\t"
    "v_perm_b32 v7, v29, v28, %17\n\t"
    "v_accvgpr_write_b32 %6, v4\n\t"
    "v_accvgpr_write_b32 %9, v11\n\t"
    "v_perm_b32 v5, v27, v26, %16\n\t"
    "v_perm_b32 v12, v31, v30, %16\n\t"
    "v_perm_b32 v1, v27, v26, %17\n\t"
    "v_perm_b32 v8, v31, v30, %17\n\t"
    "v_accvgpr_write_b32 %7, v5\n\t"
    "v_accvgpr_write_b32 %10, v12\n\t"
    "v_and_b32 v24, 0x7fff7fff, v24\n\t"
    "v_and_b32 v28, 0x7fff7fff, v28\n\t"
    "v_pk_add_u16 v2, v24, -1\n\t"
    "v_pk_add_u16 v9, v28, -1\n\t"
    "v_pk_sub_u16 v24, v24, %15 clamp\n\t"
    "v_pk_sub_u16 v28, v28, %15 clamp\n\t"
    "v_pk_min_u16 v32, v32, v2\n\t"
    "v_pk_min_u16 v32, v32, v9\n\t"
    "v_and_b32 v25, 0x7fff7fff, v25\n\t"
    "v_and_b32 v29, 0x7fff7fff, v29\n\t"
    "v_pk_add_u16 v3, v25, -1\n\t"
    "v_pk_add_u16 v10, v29, -1\n\t"
    "v_pk_sub_u16 v25, v25, %15 clamp\n\t"
    "v_pk_sub_u16 v29, v29, %15 clamp\n\t"
    "v_pk_min_u16 v32, v32, v3\n\t"
    "v_pk_min_u16 v32, v32, v10\n\t"
    "v_and_b32 v26, 0x7fff7fff, v26\n\t"
    "v_and_b32 v30, 0x7fff7fff, v30\n\t"
    "v_pk_add_u16 v2, v26, -1\n\t"
    "v_pk_add_u16 v9, v30, -1\n\t"
    "v_pk_sub_u16 v26, v26, %15 clamp\n\t"
    "v_pk_sub_u16 v30, v30, %15 clamp\n\t"
    "v_pk_min_u16 v32, v32, v2\n\t"
    "v_pk_min_u16 v32, v32, v9\n\t"
    "v_and_b32 v27, 0x7fff7fff, v27\n\t"
    "v_and_b32 v31, 0x7fff7fff, v31\n\t"
    "v_pk_add_u16 v3, v27, -1\n\t"
    "v_pk_add_u16 v10, v31, -1\n\t"
    "v_pk_sub_u16 v27, v27, %15 clamp\n\t"
    "v_pk_sub_u16 v31, v31, %15 clamp\n\t"
    "v_pk_min_u16 v32, v32, v3\n\t"
    "v_pk_min_u16 v32, v32, v10\n\t"
    "v_perm_b32 v2, v25, v24, %17\n\t"
    "v_perm_b32 v9, v29, v28, %17\n\t"
    "v_perm_b32 v3, v27, v26, %17\n\t"
    "v_perm_b32 v10, v31, v30, %17\n\t"
    "v_or3_b32 v33, v33, v2, v3\n\t"
    "v_or3_b32 v33, v33, v9, v10\n\t"
    "v_lshl_or_b32 v6, v3, 4, v2\n\t"
    "v_lshl_or_b32 v13, v10, 4, v9\n\t"
    "v_accvgpr_write_b32 %8, v6\n\t"
    "v_accvgpr_write_b32 %11, v13\n\t"
    "v_lshrrev_b32 v14, 1, v14\n\t"
    "v_and_or_b32 v14, v0, %19, v14\n\t"
    "v_lshrrev_b32 v14, 1, v14\n\t"
    "v_and_or_b32 v14, v1, %19, v14\n\t"
    "v_lshrrev_b32 v14, 1, v14\n\t"
    "v_and_or_b32 v14, v7, %19, v14\n\t"
    "v_lshrrev_b32 v14, 1, v14\n\t"
    "v_and_or_b32 v14, v8, %19, v14\n\t"
    "v_accvgpr_write_b32 %12, v14\n\t"
    "v_pk_sub_u16 v32, %18, v32 clamp\n\t"
    "v_and_b32 v33, 0xf0f0f0f0, v33\n\t"
    "v_or3_b32 %13, %13, v32, v33\n\t"
    : "=&a"(pk[16][0]), "=&a"(pk[16][1]), "=&a"(pk[16][2]), "=&a"(pk[16][3]), "=&a"(pk[16][4]), "=&a"(pk[16][5]), "=&a"(pk[16][6]), "=&a"(pk[16][7]), "=&a"(pk[16][8]), "=&a"(pk[16][9]), "=&a"(pk[16][10]), "=&a"(pk[16][11]), "=&a"(pk[16][12]), "+v"(badv)
    : "v"(laddr), "s"(base2k), "s"(0x06040200u), "s"(0x07050301u), "s"(base2k + 0x00ff00ffu), "s"(0x80808080u), "v"(koff), "s"(kbase), "s"(ring)
    : "memory", "scc", "v0", "v1", "v2", "v3", "v4", "v5", "v6", "v7", "v8", "v9", "v10", "v11", "v12", "v13", "v14", "v15", "v16", "v17", "v18", "v19", "v20", "v21", "v22", "v23", "v24", "v25", "v26", "v27", "v28", "v29", "v30", "v31", "v32", "v33");
  ld16_if(gur[0], off_gu + 0, p.wg, gph, 3);
  ld16_if(gur[1], off_gu + 0, p.wu, gph, 3);
  ld16_sc1(fl0, OFF_R + lane * 16, ws, 0);
  ld16_sc1(fl1, OFF_R + lane * 16, ws, 1024);
  pace(tp, 1403);
  asm volatile(
    "s_waitcnt vmcnt(14)\n\t"
    "ds_read_b128 v[16:19], %14 offset:4096\n\t"
    "ds_read_b128 v[20:23], %14 offset:5120\n\t"
    "ds_read_b128 v[24:27], %14 offset:6144\n\t"
    "ds_read_b128 v[28:31], %14 offset:7168\n\t"
    "v_xor_b32 v15, 163840, %20\n\t"
    "s_add_u32 m0, %22, 20480\n\t"
    "s_nop 0\n\t"
    "global_load_lds_dwordx4 v15, %21 nt\n\t"
    "global_load_lds_dwordx4 v15, %21 offset:1024 nt\n\t"
    "global_load_lds_dwordx4 v15, %21 offset:2048 nt\n\t"
    "global_load_lds_dwordx4 v15, %21 offset:3072 nt\n\t"
    "v_mov_b32 v32, -1\n\t"
    "v_mov_b32 v33, 0\n\t"
    "s_waitcnt lgkmcnt(2)\n\t"
    "v_perm_b32 v4, v17, v16, %16\n\t"
    "v_perm_b32 v11, v21, v20, %16\n\t"
    "v_perm_b32 v0, v17, v16, %17\n\t"
    "v_perm_b32 v7, v21, v20, %17\n\t"
    "v_accvgpr_write_b32 %0, v4\n\t"
    "v_accvgpr_write_b32 %3, v11\n\t"
    "v_perm_b32 v5, v19, v18, %16\n\t"
    "v_perm_b32 v12, v23, v22, %16\n\t"
    "v_perm_b32 v1, v19, v18, %17\n\t"
    "v_perm_b32 v8, v23, v22, %17\n\t"
    "v_accvgpr_write_b32 %1, v5\n\t"
    "v_accvgpr_write_b32 %4, v12\n\t"
    "v_and_b32 v16, 0x7fff7fff, v16\n\t"
    "v_and_b32 v20, 0x7fff7fff, v20\n\t"
    "v_pk_add_u16 v2, v16, -1\n\t"
    "v_pk_add_u16 v9, v20, -1\n\t"
    "v_pk_sub_u16 v16, v16, %15 clamp\n\t"
    "v_pk_sub_u16 v20, v20, %15 clamp\n\t"
    "v_pk_min_u16 v32, v32, v2\n\t"
    "v_pk_min_u16 v32, v32, v9\n\t"
    "v_and_b32 v17, 0x7fff7fff, v17\n\t"
    "v_and_b32 v21, 0x7fff7fff, v21\n\t"
    "v_pk_add_u16 v3, v17, -1\n\t"
    "v_pk_add_u16 v10, v21, -1\n\t"
    "v_pk_sub_u16 v17, v17, %15 clamp\n\t"
    "v_pk_sub_u16 v21, v21, %15 clamp\n\t"
    "v_pk_min_u16 v32, v32, v3\n\t"
    "v_pk_min_u16 v32, v32, v10\n\t"
    "v_and_b32 v18, 0x7fff7fff, v18\n\t"
    "v_and_b32 v22, 0x7fff7fff, v22\n\t"
    "v_pk_add_u16 v2, v18, -1\n\t"
    "v_pk_add_u16 v9, v22, -1\n\t"
    "v_pk_sub_u16 v18, v18, %15 clamp\n\t"
    "v_pk_sub_u16 v22, v22, %15 clamp\n\t"
    "v_pk_min_u16 v32, v32, v2\n\t"
    "v_pk_min_u16 v32, v32, v9\n\t"
    "v_and_b32 v19, 0x7fff7fff, v19\n\t"
    "v_and_b32 v23, 0x7fff7fff, v23\n\t"
    "v_pk_add_u16 v3, v19, -1\n\t"
    "v_pk_add_u16 v10, v23, -1\n\t"
    "v_pk_sub_u16 v19, v19, %15 clamp\n\t"
    "v_pk_sub_u16 v23, v23, %15 clamp\n\t"
    "v_pk_min_u16 v32, v32, v3\n\t"
    "v_pk_min_u16 v32, v32, v10\n\t"
    "v_perm_b32 v2, v17, v16, %17\n\t"
    "v_perm_b32 v9, v21, v20, %17\n\t"
    "v_perm_b32 v3, v19, v18, %17\n\t"
    "v_perm_b32 v10, v23, v22, %17\n\t"
    "v_or3_b32 v33, v33, v2, v3\n\t"
    "v_or3_b32 v33, v33, v9, v10\n\t"
    "v_lshl_or_b32 v6, v3, 4, v2\n\t"
    "v_lshl_or_b32 v13, v10, 4, v9\n\t"
    "v_accvgpr_write_b32 %2, v6\n\t"
    "v_accvgpr_write_b32 %5, v13\n\t"
    "v_and_b32 v14, %19, v0\n\t"
    "v_lshrrev_b32 v14, 1, v14\n\t"
    "v_and_or_b32 v14, v1, %19, v14\n\t"
    "v_lshrrev_b32 v14, 1, v14\n\t"
    "v_and_or_b32 v14, v7, %19, v14\n\t"
    "v_lshrrev_b32 v14, 1, v14\n\t"
    "v_and_or_b32 v14, v8, %19, v14\n\t"
    "s_waitcnt lgkmcnt(0)\n\t"
    "v_perm_b32 v4, v25, v24, %16\n\t"
    "v_perm_b32 v11, v29, v28, %16\n\t"
    "v_perm_b32 v0, v25, v24, %17\n\t"
    "v_perm_b32 v7, v29, v28, %17\n\t"
    "v_accvgpr_write_b32 %6, v4\n\t"
    "v_accvgpr_write_b32 %9, v11\n\t"
    "v_perm_b32 v5, v27, v26, %16\n\t"
    "v_perm_b32 v12, v31, v30, %16\n\t"
    "v_perm_b32 v1, v27, v26, %17\n\t"
    "v_perm_b32 v8, v31, v30, %17\n\t"
    "v_accvgpr_write_b32 %7, v5\n\t"
    "v_accvgpr_write_b32 %10, v12\n\t"
    "v_and_b32 v24, 0x7fff7fff, v24\n\t"
    "v_and_b32 v28, 0x7fff7fff, v28\n\t"
    "v_pk_add_u16 v2, v24, -1\n\t"
    "v_pk_add_u16 v9, v28, -1\n\t"
    "v_pk_sub_u16 v24, v24, %15 clamp\n\t"
    "v_pk_sub_u16 v28, v28, %15 clamp\n\t"
    "v_pk_min_u16 v32, v32, v2\n\t"
    "v_pk_min_u16 v32, v32, v9\n\t"
    "v_and_b32 v25, 0x7fff7fff, v25\n\t"
    "v_and_b32 v29, 0x7fff7fff, v29\n\t"
    "v_pk_add_u16 v3, v25, -1\n\t"
    "v_pk_add_u16 v10, v29, -1\n\t"
    "v_pk_sub_u16 v25, v25, %15 clamp\n\t"
    "v_pk_sub_u16 v29, v29, %15 clamp\n\t"
    "v_pk_min_u16 v32, v32, v3\n\t"
    "v_pk_min_u16 v32, v32, v10\n\t"
    "v_and_b32 v26, 0x7fff7fff, v26\n\t"
    "v_and_b32 v30, 0x7fff7fff, v30\n\t"
    "v_pk_add_u16 v2, v26, -1\n\t"
    "v_pk_add_u16 v9, v30, -1\n\t"
    "v_pk_sub_u16 v26, v26, %15 clamp\n\t"
    "v_pk_sub_u16 v30, v30, %15 clamp\n\t"
    "v_pk_min_u16 v32, v32, v2\n\t"
    "v_pk_min_u16 v32, v32, v9\n\t"
    "v_and_b32 v27, 0x7fff7fff, v27\n\t"
    "v_and_b32 v31, 0x7fff7fff, v31\n\t"
    "v_pk_add_u16 v3, v27, -1\n\t"
    "v_pk_add_u16 v10, v31, -1\n\t"
    "v_pk_sub_u16 v27, v27, %15 clamp\n\t"
    "v_pk_sub_u16 v31, v31, %15 clamp\n\t"
    "v_pk_min_u16 v32, v32, v3\n\t"
    "v_pk_min_u16 v32, v32, v10\n\t"
    "v_perm_b32 v2, v25, v24, %17\n\t"
    "v_perm_b32 v9, v29, v28, %17\n\t"
    "v_perm_b32 v3, v27, v26, %17\n\t"
    "v_perm_b32 v10, v31, v30, %17\n\t"
    "v_or3_b32 v33, v33, v2, v3\n\t"
    "v_or3_b32 v33, v33, v9, v10\n\t"
    "v_lshl_or_b32 v6, v3, 4, v2\n\t"
    "v_lshl_or_b32 v13, v10, 4, v9\n\t"
    "v_accvgpr_write_b32 %8, v6\n\t"
    "v_accvgpr_write_b32 %11, v13\n\t"
    "v_lshrrev_b32 v14, 1, v14\n\t"
    "v_and_or_b32 v14, v0, %19, v14\n\t"
    "v_lshrrev_b32 v14, 1, v14\n\t"
    "v_and_or_b32 v14, v1, %19, v14\n\t"
    "v_lshrrev_b32 v14, 1, v14\n\t"
    "v_and_or_b32 v14, v7, %19, v14\n\t"
    "v_lshrrev_b32 v14, 1, v14\n\t"
    "v_and_or_b32 v14, v8, %19, v14\n\t"
    "v_accvgpr_write_b32 %12, v14\n\t"
    "v_pk_sub_u16 v32, %18, v32 clamp\n\t"
    "v_and_b32 v33, 0xf0f0f0f0, v33\n\t"
    "v_or3_b32 %13, %13, v32, v33\n\t"
    : "=&a"(pk[17][0]), "=&a"(pk[17][1]), "=&a"(pk[17][2]), "=&a"(pk[17][3]), "=&a"(pk[17][4]), "=&a"(pk[17][5]), "=&a"(pk[17][6]), "=&a"(pk[17][7]), "=&a"(pk[17][8]), "=&a"(pk[17][9]), "=&a"(pk[17][10]), "=&a"(pk[17][11]), "=&a"(pk[17][12]), "+v"(badv)
    : "v"(laddr), "s"(base2v), "s"(0x06040200u), "s"(0x07050301u), "s"(base2v + 0x00ff00ffu), "s"(0x80808080u), "v"(vofr), "s"(vbase), "s"(ring)
    : "memory", "scc", "v0", "v1", "v2", "v3", "v4", "v5", "v6", "v7", "v8", "v9", "v10", "v11", "v12", "v13", "v14", "v15", "v16", "v17", "v18", "v19", "v20", "v21", "v22", "v23", "v24", "v25", "v26", "v27", "v28", "v29", "v30", "v31", "v32", "v33");
  badmask |= (__any(badv != 0) ? 1u : 0u) << 8; badv = 0;
  ld16_if(gur[2], off_gu + 3584, p.wg, gph, 3);
  ld16_if(gur[3], off_gu + 3584, p.wu, gph, 3);
  pace(tp, 1464);
  asm volatile(
    "s_waitcnt vmcnt(14)\n\t"
    "ds_read_b128 v[16:19], %14 offset:8192\n\t"
    "ds_read_b128 v[20:23], %14 offset:9216\n\t"
    "ds_read_b128 v[24:27], %14 offset:10240\n\t"
    "ds_read_b128 v[28:31], %14 offset:11264\n\t"
    "v_xor_b32 v15, 180224, %20\n\t"
    "s_add_u32 m0, %22, 24576\n\t"
    "s_nop 0\n\t"
    "global_load_lds_dwordx4 v15, %21 nt\n\t"
    "global_load_lds_dwordx4 v15, %21 offset:1024 nt\n\t"
    "global_load_lds_dwordx4 v15, %21 offset:2048 nt\n\t"
    "global_load_lds_dwordx4 v15, %21 offset:3072 nt\n\t"
    "v_mov_b32 v32, -1\n\t"
    "v_mov_b32 v33, 0\n\t"
    "s_waitcnt lgkmcnt(2)\n\t"
    "v_perm_b32 v4, v17, v16, %16\n\t"
    "v_perm_b32 v11, v21, v20, %16\n\t"
    "v_perm_b32 v0, v17, v16, %17\n\t"
    "v_perm_b32 v7, v21, v20, %17\n\t"
    "v_accvgpr_write_b32 %0, v4\n\t"
    "v_accvgpr_write_b32 %3, v11\n\t"
    "v_perm_b32 v5, v19, v18, %16\n\t"
    "v_perm_b32 v12, v23, v22, %16\n\t"
    "v_perm_b32 v1, v19, v18, %17\n\t"
    "v_perm_b32 v8, v23, v22, %17\n\t"
    "v_accvgpr_write_b32 %1, v5\n\t"
    "v_accvgpr_write_b32 %4, v12\n\t"
    "v_and_b32 v16, 0x7fff7fff, v16\n\t"
    "v_and_b32 v20, 0x7fff7fff, v20\n\t"
    "v_pk_add_u16 v2, v16, -1\n\t"
    "v_pk_add_u16 v9, v20, -1\n\t"
    "v_pk_sub_u16 v16, v16, %15 clamp\n\t"
    "v_pk_sub_u16 v20, v20, %15 clamp\n\t"
    "v_pk_min_u16 v32, v32, v2\n\t"
    "v_pk_min_u16 v32, v32, v9\n\t"
    "v_and_b32 v17, 0x7fff7fff, v17\n\t"
    "v_and_b32 v21, 0x7fff7fff, v21\n\t"
    "v_pk_add_u16 v3, v17, -1\n\t"
    "v_pk_add_u16 v10, v21, -1\n\t"
    "v_pk_sub_u16 v17, v17, %15 clamp\n\t"
    "v_pk_sub_u16 v21, v21, %15 clamp\n\t"
    "v_pk_min_u16 v32, v32, v3\n\t"
    "v_pk_min_u16 v32, v32, v10\n\t"
    "v_and_b32 v18, 0x7fff7fff, v18\n\t"
    "v_and_b32 v22, 0x7fff7fff, v22\n\t"
    "v_pk_add_u16 v2, v18, -1\n\t"
    "v_pk_add_u16 v9, v22, -1\n\t"
    "v_pk_sub_u16 v18, v18, %15 clamp\n\t"
    "v_pk_sub_u16 v22, v22, %15 clamp\n\t"
    "v_pk_min_u16 v32, v32, v2\n\t"
    "v_pk_min_u16 v32, v32, v9\n\t"
    "v_and_b32 v19, 0x7fff7fff, v19\n\t"
    "v_and_b32 v23, 0x7fff7fff, v23\n\t"
    "v_pk_add_u16 v3, v19, -1\n\t"
    "v_pk_add_u16 v10, v23, -1\n\t"
    "v_pk_sub_u16 v19, v19, %15 clamp\n\t"
    "v_pk_sub_u16 v23, v23, %15 clamp\n\t"
    "v_pk_min_u16 v32, v32, v3\n\t"
    "v_pk_min_u16 v32, v32, v10\n\t"
    "v_perm_b32 v2, v17, v16, %17\n\t"
    "v_perm_b32 v9, v21, v20, %17\n\t"
    "v_perm_b32 v3, v19, v18, %17\n\t"
    "v_perm_b32 v10, v23, v22, %17\n\t"
    "v_or3_b32 v33, v33, v2, v3\n\t"
    "v_or3_b32 v33, v33, v9, v10\n\t"
    "v_lshl_or_b32 v6, v3, 4, v2\n\t"
    "v_lshl_or_b32 v13, v10, 4, v9\n\t"
    "v_accvgpr_write_b32 %2, v6\n\t"
    "v_accvgpr_write_b32 %5, v13\n\t"
    "v_and_b32 v14, %19, v0\n\t"
    "v_lshrrev_b32 v14, 1, v14\n\t"
    "v_and_or_b32 v14, v1, %19, v14\n\t"
    "v_lshrrev_b32 v14, 1, v14\n\t"
    "v_and_or_b32 v14, v7, %19, v14\n\t"
    "v_lshrrev_b32 v14, 1, v14\n\t"
    "v_and_or_b32 v14, v8, %19, v14\n\t"
    "s_waitcnt lgkmcnt(0)\n\t"
    "v_perm_b32 v4, v25, v24, %16\n\t"
    "v_perm_b32 v11, v29, v28, %16\n\t"
    "v_perm_b32 v0, v25, v24, %17\n\t"
    "v_perm_b32 v7, v29, v28, %17\n\t"
    "v_accvgpr_write_b32 %6, v4\n\t"
    "v_accvgpr_write_b32 %9, v11\n\t"
    "v_perm_b32 v5, v27, v26, %16\n\t"
    "v_perm_b32 v12, v31, v30, %16\n\t"
    "v_perm_b32 v1, v27, v26, %17\n\t"
    "v_perm_b32 v8, v31, v30, %17\n\t"
    "v_accvgpr_write_b32 %7, v5\n\t"
    "v_accvgpr_write_b32 %10, v12\n\t"
    "v_and_b32 v24, 0x7fff7fff, v24\n\t"
    "v_and_b32 v28, 0x7fff7fff, v28\n\t"
    "v_pk_add_u16 v2, v24, -1\n\t"
    "v_pk_add_u16 v9, v28, -1\n\t"
    "v_pk_sub_u16 v24, v24, %15 clamp\n\t"
    "v_pk_sub_u16 v28, v28, %15 clamp\n\t"
    "v_pk_min_u16 v32, v32, v2\n\t"
    "v_pk_min_u16 v32, v32, v9\n\t"
    "v_and_b32 v25, 0x7fff7fff, v25\n\t"
    "v_and_b32 v29, 0x7fff7fff, v29\n\t"
    "v_pk_add_u16 v3, v25, -1\n\t"
    "v_pk_add_u16 v10, v29, -1\n\t"
    "v_pk_sub_u16 v25, v25, %15 clamp\n\t"
    "v_pk_sub_u16 v29, v29, %15 clamp\n\t"
    "v_pk_min_u16 v32, v32, v3\n\t"
    "v_pk_min_u16 v32, v32, v10\n\t"
    "v_and_b32 v26, 0x7fff7fff, v26\n\t"
    "v_and_b32 v30, 0x7fff7fff, v30\n\t"
    "v_pk_add_u16 v2, v26, -1\n\t"
    "v_pk_add_u16 v9, v30, -1\n\t"
    "v_pk_sub_u16 v26, v26, %15 clamp\n\t"
    "v_pk_sub_u16 v30, v30, %15 clamp\n\t"
    "v_pk_min_u16 v32, v32, v2\n\t"
    "v_pk_min_u16 v32, v32, v9\n\t"
    "v_and_b32 v27, 0x7fff7fff, v27\n\t"
    "v_and_b32 v31, 0x7fff7fff, v31\n\t"
    "v_pk_add_u16 v3, v27, -1\n\t"
    "v_pk_add_u16 v10, v31, -1\n\t"
    "v_pk_sub_u16 v27, v27, %15 clamp\n\t"
    "v_pk_sub_u16 v31, v31, %15 clamp\n\t"
    "v_pk_min_u16 v32, v32, v3\n\t"
    "v_pk_min_u16 v32, v32, v10\n\t"
    "v_perm_b32 v2, v25, v24, %17\n\t"
    "v_perm_b32 v9, v29, v28, %17\n\t"
    "v_perm_b32 v3, v27, v26, %17\n\t"
    "v_perm_b32 v10, v31, v30, %17\n\t"
    "v_or3_b32 v33, v33, v2, v3\n\t"
    "v_or3_b32 v33, v33, v9, v10\n\t"
    "v_lshl_or_b32 v6, v3, 4, v2\n\t"
    "v_lshl_or_b32 v13, v10, 4, v9\n\t"
    "v_accvgpr_write_b32 %8, v6\n\t"
    "v_accvgpr_write_b32 %11, v13\n\t"
    "v_lshrrev_b32 v14, 1, v14\n\t"
    "v_and_or_b32 v14, v0, %19, v14\n\t"
    "v_lshrrev_b32 v14, 1, v14\n\t"
    "v_and_or_b32 v14, v1, %19, v14\n\t"
    "v_lshrrev_b32 v14, 1, v14\n\t"
    "v_and_or_b32 v14, v7, %19, v14\n\t"
    "v_lshrrev_b32 v14, 1, v14\n\t"
    "v_and_or_b32 v14, v8, %19, v14\n\t"
    "v_accvgpr_write_b32 %12, v14\n\t"
    "v_pk_sub_u16 v32, %18, v32 clamp\n\t"
    "v_and_b32 v33, 0xf0f0f0f0, v33\n\t"
    "v_or3_b32 %13, %13, v32, v33\n\t"
    : "=&a"(pk[18][0]), "=&a"(pk[18][1]), "=&a"(pk[18][2]), "=&a"(pk[18][3]), "=&a"(pk[18][4]), "=&a"(pk[18][5]), "=&a"(pk[18][6]), "=&a"(pk[18][7]), "=&a"(pk[18][8]), "=&a"(pk[18][9]), "=&a"(pk[18][10]), "=&a"(pk[18][11]), "=&a"(pk[18][12]), "+v"(badv)
    : "v"(laddr), "s"(base2k), "s"(0x06040200u), "s"(0x07050301u), "s"(base2k + 0x00ff00ffu), "s"(0x80808080u), "v"(koff), "s"(kbase), "s"(ring)
    : "memory", "scc", "v0", "v1", "v2", "v3", "v4", "v5", "v6", "v7", "v8", "v9", "v10", "v11", "v12", "v13", "v14", "v15", "v16", "v17", "v18", "v19", "v20", "v21", "v22", "v23", "v24", "v25", "v26", "v27", "v28", "v29", "v30", "v31", "v32", "v33");
  pace(tp, 1525);
  asm volatile(
    "s_waitcnt vmcnt(14)\n\t"
    "ds_read_b128 v[16:19], %14 offset:12288\n\t"
    "ds_read_b128 v[20:23], %14 offset:13312\n\t"
    "ds_read_b128 v[24:27], %14 offset:14336\n\t"
    "ds_read_b128 v[28:31], %14 offset:15360\n\t"
    "v_xor_b32 v15, 180224, %20\n\t"
    "s_add_u32 m0, %22, 28672\n\t"
    "s_nop 0\n\t"
    "global_load_lds_dwordx4 v15, %21 nt\n\t"
    "global_load_lds_dwordx4 v15, %21 offset:1024 nt\n\t"
    "global_load_lds_dwordx4 v15, %21 offset:2048 nt\n\t"
    "global_load_lds_dwordx4 v15, %21 offset:3072 nt\n\t"
    "v_mov_b32 v32, -1\n\t"
    "v_mov_b32 v33, 0\n\t"
    "s_waitcnt lgkmcnt(2)\n\t"
    "v_perm_b32 v4, v17, v16, %16\n\t"
    "v_perm_b32 v11, v21, v20, %16\n\t"
    "v_perm_b32 v0, v17, v16, %17\n\t"
    "v_perm_b32 v7, v21, v20, %17\n\t"
    "v_accvgpr_write_b32 %0, v4\n\t"
    "v_accvgpr_write_b32 %3, v11\n\t"
    "v_perm_b32 v5, v19, v18, %16\n\t"
    "v_perm_b32 v12, v23, v22, %16\n\t"
    "v_perm_b32 v1, v19, v18, %17\n\t"
    "v_perm_b32 v8, v23, v22, %17\n\t"
    "v_accvgpr_write_b32 %1, v5\n\t"
    "v_accvgpr_write_b32 %4, v12\n\t"
    "v_and_b32 v16, 0x7fff7fff, v16\n\t"
    "v_and_b32 v20, 0x7fff7fff, v20\n\t"
    "v_pk_add_u16 v2, v16, -1\n\t"
    "v_pk_add_u16 v9, v20, -1\n\t"
    "v_pk_sub_u16 v16, v16, %15 clamp\n\t"
    "v_pk_sub_u16 v20, v20, %15 clamp\n\t"
    "v_pk_min_u16 v32, v32, v2\n\t"
    "v_pk_min_u16 v32, v32, v9\n\t"
    "v_and_b32 v17, 0x7fff7fff, v17\n\t"
    "v_and_b32 v21, 0x7fff7fff, v21\n\t"
    "v_pk_add_u16 v3, v17, -1\n\t"
    "v_pk_add_u16 v10, v21, -1\n\t"
    "v_pk_sub_u16 v17, v17, %15 clamp\n\t"
    "v_pk_sub_u16 v21, v21, %15 clamp\n\t"
    "v_pk_min_u16 v32, v32, v3\n\t"
    "v_pk_min_u16 v32, v32, v10\n\t"
    "v_and_b32 v18, 0x7fff7fff, v18\n\t"
    "v_and_b32 v22, 0x7fff7fff, v22\n\t"
    "v_pk_add_u16 v2, v18, -1\n\t"
    "v_pk_add_u16 v9, v22, -1\n\t"
    "v_pk_sub_u16 v18, v18, %15 clamp\n\t"
    "v_pk_sub_u16 v22, v22, %15 clamp\n\t"
    "v_pk_min_u16 v32, v32, v2\n\t"
    "v_pk_min_u16 v32, v32, v9\n\t"
    "v_and_b32 v19, 0x7fff7fff, v19\n\t"
    "v_and_b32 v23, 0x7fff7fff, v23\n\t"
    "v_pk_add_u16 v3, v19, -1\n\t"
    "v_pk_add_u16 v10, v23, -1\n\t"
    "v_pk_sub_u16 v19, v19, %15 clamp\n\t"
    "v_pk_sub_u16 v23, v23, %15 clamp\n\t"
    "v_pk_min_u16 v32, v32, v3\n\t"
    "v_pk_min_u16 v32, v32, v10\n\t"
    "v_perm_b32 v2, v17, v16, %17\n\t"
    "v_perm_b32 v9, v21, v20, %17\n\t"
    "v_perm_b32 v3, v19, v18, %17\n\t"
    "v_perm_b32 v10, v23, v22, %17\n\t"
    "v_or3_b32 v33, v33, v2, v3\n\t"
    "v_or3_b32 v33, v33, v9, v10\n\t"
    "v_lshl_or_b32 v6, v3, 4, v2\n\t"
    "v_lshl_or_b32 v13, v10, 4, v9\n\t"
    "v_accvgpr_write_b32 %2, v6\n\t"
    "v_accvgpr_write_b32 %5, v13\n\t"
    "v_and_b32 %12, %19, v0\n\t"
    "v_lshrrev_b32 %12, 1, %12\n\t"
    "v_and_or_b32 %12, v1, %19, %12\n\t"
    "v_lshrrev_b32 %12, 1, %12\n\t"
    "v_and_or_b32 %12, v7, %19, %12\n\t"
    "v_lshrrev_b32 %12, 1, %12\n\t"
    "v_and_or_b32 %12, v8, %19, %12\n\t"
    "s_waitcnt lgkmcnt(0)\n\t"
    "v_perm_b32 v4, v25, v24, %16\n\t"
    "v_perm_b32 %9, v29, v28, %16\n\t"
    "v_perm_b32 v0, v25, v24, %17\n\t"
    "v_perm_b32 v7, v29, v28, %17\n\t"
    "v_accvgpr_write_b32 %6, v4\n\t"
    "v_perm_b32 %10, v31, v30, %16\n\t"
    "v_perm_b32 v5, v27, v26, %16\n\t"
    "v_perm_b32 v8, v31, v30, %17\n\t"
    "v_perm_b32 v1, v27, v26, %17\n\t"
    "v_and_b32 v28, 0x7fff7fff, v28\n\t"
    "v_accvgpr_write_b32 %7, v5\n\t"
    "v_pk_add_u16 v9, v28, -1\n\t"
    "v_and_b32 v24, 0x7fff7fff, v24\n\t"
    "v_pk_sub_u16 v28, v28, %15 clamp\n\t"
    "v_pk_add_u16 v2, v24, -1\n\t"
    "v_pk_min_u16 v32, v32, v9\n\t"
    "v_pk_sub_u16 v24, v24, %15 clamp\n\t"
    "v_and_b32 v29, 0x7fff7fff, v29\n\t"
    "v_pk_min_u16 v32, v32, v2\n\t"
    "v_pk_add_u16 v10, v29, -1\n\t"
    "v_and_b32 v25, 0x7fff7fff, v25\n\t"
    "v_pk_sub_u16 v29, v29, %15 clamp\n\t"
    "v_pk_add_u16 v3, v25, -1\n\t"
    "v_pk_min_u16 v32, v32, v10\n\t"
    "v_pk_sub_u16 v25, v25, %15 clamp\n\t"
    "v_and_b32 v30, 0x7fff7fff, v30\n\t"
    "v_pk_min_u16 v32, v32, v3\n\t"
    "v_pk_add_u16 v9, v30, -1\n\t"
    "v_and_b32 v26, 0x7fff7fff, v26\n\t"
    "v_pk_sub_u16 v30, v30, %15 clamp\n\t"
    "v_pk_add_u16 v2, v26, -1\n\t"
    "v_pk_min_u16 v32, v32, v9\n\t"
    "v_pk_sub_u16 v26, v26, %15 clamp\n\t"
    "v_and_b32 v31, 0x7fff7fff, v31\n\t"
    "v_pk_min_u16 v32, v32, v2\n\t"
    "v_pk_add_u16 v10, v31, -1\n\t"
    "v_and_b32 v27, 0x7fff7fff, v27\n\t"
    "v_pk_sub_u16 v31, v31, %15 clamp\n\t"
    "v_pk_add_u16 v3, v27, -1\n\t"
    "v_pk_min_u16 v32, v32, v10\n\t"
    "v_pk_sub_u16 v27, v27, %15 clamp\n\t"
    "v_perm_b32 v9, v29, v28, %17\n\t"
    "v_pk_min_u16 v32, v32, v3\n\t"
    "v_perm_b32 v10, v31, v30, %17\n\t"
    "v_perm_b32 v2, v25, v24, %17\n\t"
    "v_or3_b32 v33, v33, v9, v10\n\t"
    "v_perm_b32 v3, v27, v26, %17\n\t"
    "v_lshl_or_b32 %11, v10, 4, v9\n\t"
    "v_or3_b32 v33, v33, v2, v3\n\t"
    "v_lshl_or_b32 v6, v3, 4, v2\n\t"
    "v_accvgpr_write_b32 %8, v6\n\t"
    "v_lshrrev_b32 %12, 1, %12\n\t"
    "v_and_or_b32 %12, v0, %19, %12\n\t"
    "v_lshrrev_b32 %12, 1, %12\n\t"
    "v_and_or_b32 %12, v1, %19, %12\n\t"
    "v_lshrrev_b32 %12, 1, %12\n\t"
    "v_and_or_b32 %12, v7, %19, %12\n\t"
    "v_lshrrev_b32 %12, 1, %12\n\t"
    "v_and_or_b32 %12, v8, %19, %12\n\t"
    "v_pk_sub_u16 v32, %18, v32 clamp\n\t"
    "v_and_b32 v33, 0xf0f0f0f0, v33\n\t"
    "v_or3_b32 %13, %13, v32, v33\n\t"
    : "=&a"(pk[19][0]), "=&a"(pk[19][1]), "=&a"(pk[19][2]), "=&a"(pk[19][3]), "=&a"(pk[19][4]), "=&a"(pk[19][5]), "=&a"(pk[19][6]), "=&a"(pk[19][7]), "=&a"(pk[19][8]), "=&v"(pk[19][9]), "=&v"(pk[19][10]), "=&v"(pk[19][11]), "=&v"(pk[19][12]), "+v"(badv)
    : "v"(laddr), "s"(base2v), "s"(0x06040200u), "s"(0x07050301u), "s"(base2v + 0x00ff00ffu), "s"(0x80808080u), "v"(vofr), "s"(vbase), "s"(ring)
    : "memory", "scc", "v0", "v1", "v2", "v3", "v4", "v5", "v6", "v7", "v8", "v9", "v10", "v11", "v12", "v13", "v14", "v15", "v16", "v17", "v18", "v19", "v20", "v21", "v22", "v23", "v24", "v25", "v26", "v27", "v28", "v29", "v30", "v31", "v32", "v33");
  badmask |= (__any(badv != 0) ? 1u : 0u) << 9; badv = 0;
  pace(tp, 1586);
  asm volatile(
    "s_waitcnt vmcnt(14)\n\t"
    "ds_read_b128 v[16:19], %14 offset:16384\n\t"
    "ds_read_b128 v[20:23], %14 offset:17408\n\t"
    "ds_read_b128 v[24:27], %14 offset:18432\n\t"
    "ds_read_b128 v[28:31], %14 offset:19456\n\t"
    "v_xor_b32 v15, 196608, %20\n\t"
    "s_add_u32 m0, %22, 0\n\t"
    "s_nop 0\n\t"
    "global_load_lds_dwordx4 v15, %21 nt\n\t"
    "global_load_lds_dwordx4 v15, %21 offset:1024 nt\n\t"
    "global_load_lds_dwordx4 v15, %21 offset:2048 nt\n\t"
    "global_load_lds_dwordx4 v15, %21 offset:3072 nt\n\t"
    "v_mov_b32 v32, -1\n\t"
    "v_mov_b32 v33, 0\n\t"
    "s_waitcnt lgkmcnt(2)\n\t"
    "v_perm_b32 %0, v17, v16, %16\n\t"
    "v_perm_b32 %3, v21, v20, %16\n\t"
    "v_perm_b32 v0, v17, v16, %17\n\t"
    "v_perm_b32 v7, v21, v20, %17\n\t"
    "v_perm_b32 %1, v19, v18, %16\n\t"
    "v_perm_b32 %4, v23, v22, %16\n\t"
    "v_perm_b32 v1, v19, v18, %17\n\t"
    "v_perm_b32 v8, v23, v22, %17\n\t"
    "v_and_b32 v16, 0x7fff7fff, v16\n\t"
    "v_and_b32 v20, 0x7fff7fff, v20\n\t"
    "v_pk_add_u16 v2, v16, -1\n\t"
    "v_pk_add_u16 v9, v20, -1\n\t"
    "v_pk_sub_u16 v16, v16, %15 clamp\n\t"
    "v_pk_sub_u16 v20, v20, %15 clamp\n\t"
    "v_pk_min_u16 v32, v32, v2\n\t"
    "v_pk_min_u16 v32, v32, v9\n\t"
    "v_and_b32 v17, 0x7fff7fff, v17\n\t"
    "v_and_b32 v21, 0x7fff7fff, v21\n\t"
    "v_pk_add_u16 v3, v17, -1\n\t"
    "v_pk_add_u16 v10, v21, -1\n\t"
    "v_pk_sub_u16 v17, v17, %15 clamp\n\t"
    "v_pk_sub_u16 v21, v21, %15 clamp\n\t"
    "v_pk_min_u16 v32, v32, v3\n\t"
    "v_pk_min_u16 v32, v32, v10\n\t"
    "v_and_b32 v18, 0x7fff7fff, v18\n\t"
    "v_and_b32 v22, 0x7fff7fff, v22\n\t"
    "v_pk_add_u16 v2, v18, -1\n\t"
    "v_pk_add_u16 v9, v22, -1\n\t"
    "v_pk_sub_u16 v18, v18, %15 clamp\n\t"
    "v_pk_sub_u16 v22, v22, %15 clamp\n\t"
    "v_pk_min_u16 v32, v32, v2\n\t"
    "v_pk_min_u16 v32, v32, v9\n\t"
    "v_and_b32 v19, 0x7fff7fff, v19\n\t"
    "v_and_b32 v23, 0x7fff7fff, v23\n\t"
    "v_pk_add_u16 v3, v19, -1\n\t"
    "v_pk_add_u16 v10, v23, -1\n\t"
    "v_pk_sub_u16 v19, v19, %15 clamp\n\t"
    "v_pk_sub_u16 v23, v23, %15 clamp\n\t"
    "v_pk_min_u16 v32, v32, v3\n\t"
    "v_pk_min_u16 v32, v32, v10\n\t"
    "v_perm_b32 v2, v17, v16, %17\n\t"
    "v_perm_b32 v9, v21, v20, %17\n\t"
    "v_perm_b32 v3, v19, v18, %17\n\t"
    "v_perm_b32 v10, v23, v22, %17\n\t"
    "v_or3_b32 v33, v33, v2, v3\n\t"
    "v_or3_b32 v33, v33, v9, v10\n\t"
    "v_lshl_or_b32 %2, v3, 4, v2\n\t"
    "v_lshl_or_b32 %5, v10, 4, v9\n\t"
    "v_and_b32 %12, %19, v0\n\t"
    "v_lshrrev_b32 %12, 1, %12\n\t"
    "v_and_or_b32 %12, v1, %19, %12\n\t"
    "v_lshrrev_b32 %12, 1, %12\n\t"
    "v_and_or_b32 %12, v7, %19, %12\n\t"
    "v_lshrrev_b32 %12, 1, %12\n\t"
    "v_and_or_b32 %12, v8, %19, %12\n\t"
    "s_waitcnt lgkmcnt(0)\n\t"
    "v_perm_b32 %6, v25, v24, %16\n\t"
    "v_perm_b32 %9, v29, v28, %16\n\t"
    "v_perm_b32 v0, v25, v24, %17\n\t"
    "v_perm_b32 v7, v29, v28, %17\n\t"
    "v_perm_b32 %7, v27, v26, %16\n\t"
    "v_perm_b32 %10, v31, v30, %16\n\t"
    "v_perm_b32 v1, v27, v26, %17\n\t"
    "v_perm_b32 v8, v31, v30, %17\n\t"
    "v_and_b32 v24, 0x7fff7fff, v24\n\t"
    "v_and_b32 v28, 0x7fff7fff, v28\n\t"
    "v_pk_add_u16 v2, v24, -1\n\t"
    "v_pk_add_u16 v9, v28, -1\n\t"
    "v_pk_sub_u16 v24, v24, %15 clamp\n\t"
    "v_pk_sub_u16 v28, v28, %15 clamp\n\t"
    "v_pk_min_u16 v32, v32, v2\n\t"
    "v_pk_min_u16 v32, v32, v9\n\t"
    "v_and_b32 v25, 0x7fff7fff, v25\n\t"
    "v_and_b32 v29, 0x7fff7fff, v29\n\t"
    "v_pk_add_u16 v3, v25, -1\n\t"
    "v_pk_add_u16 v10, v29, -1\n\t"
    "v_pk_sub_u16 v25, v25, %15 clamp\n\t"
    "v_pk_sub_u16 v29, v29, %15 clamp\n\t"
    "v_pk_min_u16 v32, v32, v3\n\t"
    "v_pk_min_u16 v32, v32, v10\n\t"
    "v_and_b32 v26, 0x7fff7fff, v26\n\t"
    "v_and_b32 v30, 0x7fff7fff, v30\n\t"
    "v_pk_add_u16 v2, v26, -1\n\t"
    "v_pk_add_u16 v9, v30, -1\n\t"
    "v_pk_sub_u16 v26, v26, %15 clamp\n\t"
    "v_pk_sub_u16 v30, v30, %15 clamp\n\t"
    "v_pk_min_u16 v32, v32, v2\n\t"
    "v_pk_min_u16 v32, v32, v9\n\t"
    "v_and_b32 v27, 0x7fff7fff, v27\n\t"
    "v_and_b32 v31, 0x7fff7fff, v31\n\t"
    "v_pk_add_u16 v3, v27, -1\n\t"
    "v_pk_add_u16 v10, v31, -1\n\t"
    "v_pk_sub_u16 v27, v27, %15 clamp\n\t"
    "v_pk_sub_u16 v31, v31, %15 clamp\n\t"
    "v_pk_min_u16 v32, v32, v3\n\t"
    "v_pk_min_u16 v32, v32, v10\n\t"
    "v_perm_b32 v2, v25, v24, %17\n\t"
    "v_perm_b32 v9, v29, v28, %17\n\t"
    "v_perm_b32 v3, v27, v26, %17\n\t"
    "v_perm_b32 v10, v31, v30, %17\n\t"
    "v_or3_b32 v33, v33, v2, v3\n\t"
    "v_or3_b32 v33, v33, v9, v10\n\t"
    "v_lshl_or_b32 %8, v3, 4, v2\n\t"
    "v_lshl_or_b32 %11, v10, 4, v9\n\t"
    "v_lshrrev_b32 %12, 1, %12\n\t"
    "v_and_or_b32 %12, v0, %19, %12\n\t"
    "v_lshrrev_b32 %12, 1, %12\n\t"
    "v_and_or_b32 %12, v1, %19, %12\n\t"
    "v_lshrrev_b32 %12, 1, %12\n\t"
    "v_and_or_b32 %12, v7, %19, %12\n\t"
    "v_lshrrev_b32 %12, 1, %12\n\t"
    "v_and_or_b32 %12, v8, %19, %12\n\t"
    "v_pk_sub_u16 v32, %18, v32 clamp\n\t"
    "v_and_b32 v33, 0xf0f0f0f0, v33\n\t"
    "v_or3_b32 %13, %13, v32, v33\n\t"
    : "=&v"(pk[20][0]), "=&v"(pk[20][1]), "=&v"(pk[20][2]), "=&v"(pk[20][3]), "=&v"(pk[20][4]), "=&v"(pk[20][5]), "=&v"(pk[20][6]), "=&v"(pk[20][7]), "=&v"(pk[20][8]), "=&v"(pk[20][9]), "=&v"(pk[20][10]), "=&v"(pk[20][11]), "=&v"(pk[20][12]), "+v"(badv)
    : "v"(laddr), "s"(base2k), "s"(0x06040200u), "s"(0x07050301u), "s"(base2k + 0x00ff00ffu), "s"(0x80808080u), "v"(koff), "s"(kbase), "s"(ring)
    : "memory", "scc", "v0", "v1", "v2", "v3", "v4", "v5", "v6", "v7", "v8", "v9", "v10", "v11", "v12", "v13", "v14", "v15", "v16", "v17", "v18", "v19", "v20", "v21", "v22", "v23", "v24", "v25", "v26", "v27", "v28", "v29", "v30", "v31", "v32", "v33");
  asm volatile("s_waitcnt vmcnt(16)" : "+v"(fl0), "+v"(fl1) : : "memory");
  STAMP(4);
  flags_retry(fl0, fl1, tagR, lane, ws);
  STAMP(5);
  if (c == 0 && tid == 0) st8_sc1(0, u32x2{(u32)(E + 1), (u32)((E + 1) >> 32)}, ws);
  q_dma();
  pace(tp, 1647);
  asm volatile(
    "s_waitcnt vmcnt(14)\n\t"
    "ds_read_b128 v[16:19], %14 offset:20480\n\t"
    "ds_read_b128 v[20:23], %14 offset:21504\n\t"
    "ds_read_b128 v[24:27], %14 offset:22528\n\t"
    "ds_read_b128 v[28:31], %14 offset:23552\n\t"
    "v_xor_b32 v15, 196608, %20\n\t"
    "s_add_u32 m0, %22, 4096\n\t"
    "s_nop 0\n\t"
    "global_load_lds_dwordx4 v15, %21 nt\n\t"
    "global_load_lds_dwordx4 v15, %21 offset:1024 nt\n\t"
    "global_load_lds_dwordx4 v15, %21 offset:2048 nt\n\t"
    "global_load_lds_dwordx4 v15, %21 offset:3072 nt\n\t"
    "v_mov_b32 v32, -1\n\t"
    "v_mov_b32 v33, 0\n\t"
    "s_waitcnt lgkmcnt(2)\n\t"
    "v_perm_b32 %0, v17, v16, %16\n\t"
    "v_perm_b32 %3, v21, v20, %16\n\t"
    "v_perm_b32 v0, v17, v16, %17\n\t"
    "v_perm_b32 v7, v21, v20, %17\n\t"
    "v_perm_b32 %1, v19, v18, %16\n\t"
    "v_perm_b32 %4, v23, v22, %16\n\t"
    "v_perm_b32 v1, v19, v18, %17\n\t"
    "v_perm_b32 v8, v23, v22, %17\n\t"
    "v_and_b32 v16, 0x7fff7fff, v16\n\t"
    "v_and_b32 v20, 0x7fff7fff, v20\n\t"
    "v_pk_add_u16 v2, v16, -1\n\t"
    "v_pk_add_u16 v9, v20, -1\n\t"
    "v_pk_sub_u16 v16, v16, %15 clamp\n\t"
    "v_pk_sub_u16 v20, v20, %15 clamp\n\t"
    "v_pk_min_u16 v32, v32, v2\n\t"
    "v_pk_min_u16 v32, v32, v9\n\t"
    "v_and_b32 v17, 0x7fff7fff, v17\n\t"
    "v_and_b32 v21, 0x7fff7fff, v21\n\t"
    "v_pk_add_u16 v3, v17, -1\n\t"
    "v_pk_add_u16 v10, v21, -1\n\t"
    "v_pk_sub_u16 v17, v17, %15 clamp\n\t"
    "v_pk_sub_u16 v21, v21, %15 clamp\n\t"
    "v_pk_min_u16 v32, v32, v3\n\t"
    "v_pk_min_u16 v32, v32, v10\n\t"
    "v_and_b32 v18, 0x7fff7fff, v18\n\t"
    "v_and_b32 v22, 0x7fff7fff, v22\n\t"
    "v_pk_add_u16 v2, v18, -1\n\t"
    "v_pk_add_u16 v9, v22, -1\n\t"
    "v_pk_sub_u16 v18, v18, %15 clamp\n\t"
    "v_pk_sub_u16 v22, v22, %15 clamp\n\t"
    "v_pk_min_u16 v32, v32, v2\n\t"
    "v_pk_min_u16 v32, v32, v9\n\t"
    "v_and_b32 v19, 0x7fff7fff, v19\n\t"
    "v_and_b32 v23, 0x7fff7fff, v23\n\t"
    "v_pk_add_u16 v3, v19, -1\n\t"
    "v_pk_add_u16 v10, v23, -1\n\t"
    "v_pk_sub_u16 v19, v19, %15 clamp\n\t"
    "v_pk_sub_u16 v23, v23, %15 clamp\n\t"
    "v_pk_min_u16 v32, v32, v3\n\t"
    "v_pk_min_u16 v32, v32, v10\n\t"
    "v_perm_b32 v2, v17, v16, %17\n\t"
    "v_perm_b32 v9, v21, v20, %17\n\t"
    "v_perm_b32 v3, v19, v18, %17\n\t"
    "v_perm_b32 v10, v23, v22, %17\n\t"
    "v_or3_b32 v33, v33, v2, v3\n\t"
    "v_or3_b32 v33, v33, v9, v10\n\t"
    "v_lshl_or_b32 %2, v3, 4, v2\n\t"
    "v_lshl_or_b32 %5, v10, 4, v9\n\t"
    "v_and_b32 %12, %19, v0\n\t"
    "v_lshrrev_b32 %12, 1, %12\n\t"
    "v_and_or_b32 %12, v1, %19, %12\n\t"
    "v_lshrrev_b32 %12, 1, %12\n\t"
    "v_and_or_b32 %12, v7, %19, %12\n\t"
    "v_lshrrev_b32 %12, 1, %12\n\t"
    "v_and_or_b32 %12, v8, %19, %12\n\t"
    "s_waitcnt lgkmcnt(0)\n\t"
    "v_perm_b32 %6, v25, v24, %16\n\t"
    "v_perm_b32 %9, v29, v28, %16\n\t"
    "v_perm_b32 v0, v25, v24, %17\n\t"
    "v_perm_b32 v7, v29, v28, %17\n\t"
    "v_perm_b32 %7, v27, v26, %16\n\t"
    "v_perm_b32 %10, v31, v30, %16\n\t"
    "v_perm_b32 v1, v27, v26, %17\n\t"
    "v_perm_b32 v8, v31, v30, %17\n\t"
    "v_and_b32 v24, 0x7fff7fff, v24\n\t"
    "v_and_b32 v28, 0x7fff7fff, v28\n\t"
    "v_pk_add_u16 v2, v24, -1\n\t"
    "v_pk_add_u16 v9, v28, -1\n\t"
    "v_pk_sub_u16 v24, v24, %15 clamp\n\t"
    "v_pk_sub_u16 v28, v28, %15 clamp\n\t"
    "v_pk_min_u16 v32, v32, v2\n\t"
    "v_pk_min_u16 v32, v32, v9\n\t"
    "v_and_b32 v25, 0x7fff7fff, v25\n\t"
    "v_and_b32 v29, 0x7fff7fff, v29\n\t"
    "v_pk_add_u16 v3, v25, -1\n\t"
    "v_pk_add_u16 v10, v29, -1\n\t"
    "v_pk_sub_u16 v25, v25, %15 clamp\n\t"
    "v_pk_sub_u16 v29, v29, %15 clamp\n\t"
    "v_pk_min_u16 v32, v32, v3\n\t"
    "v_pk_min_u16 v32, v32, v10\n\t"
    "v_and_b32 v26, 0x7fff7fff, v26\n\t"
    "v_and_b32 v30, 0x7fff7fff, v30\n\t"
    "v_pk_add_u16 v2, v26, -1\n\t"
    "v_pk_add_u16 v9, v30, -1\n\t"
    "v_pk_sub_u16 v26, v26, %15 clamp\n\t"
    "v_pk_sub_u16 v30, v30, %15 clamp\n\t"
    "v_pk_min_u16 v32, v32, v2\n\t"
    "v_pk_min_u16 v32, v32, v9\n\t"
    "v_and_b32 v27, 0x7fff7fff, v27\n\t"
    "v_and_b32 v31, 0x7fff7fff, v31\n\t"
    "v_pk_add_u16 v3, v27, -1\n\t"
    "v_pk_add_u16 v10, v31, -1\n\t"
    "v_pk_sub_u16 v27, v27, %15 clamp\n\t"
    "v_pk_sub_u16 v31, v31, %15 clamp\n\t"
    "v_pk_min_u16 v32, v32, v3\n\t"
    "v_pk_min_u16 v32, v32, v10\n\t"
    "v_perm_b32 v2, v25, v24, %17\n\t"
    "v_perm_b32 v9, v29, v28, %17\n\t"
    "v_perm_b32 v3, v27, v26, %17\n\t"
    "v_perm_b32 v10, v31, v30, %17\n\t"
    "v_or3_b32 v33, v33, v2, v3\n\t"
    "v_or3_b32 v33, v33, v9, v10\n\t"
    "v_lshl_or_b32 %8, v3, 4, v2\n\t"
    "v_lshl_or_b32 %11, v10, 4, v9\n\t"
    "v_lshrrev_b32 %12, 1, %12\n\t"
    "v_and_or_b32 %12, v0, %19, %12\n\t"
    "v_lshrrev_b32 %12, 1, %12\n\t"
    "v_and_or_b32 %12, v1, %19, %12\n\t"
    "v_lshrrev_b32 %12, 1, %12\n\t"
    "v_and_or_b32 %12, v7, %19, %12\n\t"
    "v_lshrrev_b32 %12, 1, %12\n\t"
    "v_and_or_b32 %12, v8, %19, %12\n\t"
    "v_pk_sub_u16 v32, %18, v32 clamp\n\t"
    "v_and_b32 v33, 0xf0f0f0f0, v33\n\t"
    "v_or3_b32 %13, %13, v32, v33\n\t"
    : "=&v"(pk[21][0]), "=&v"(pk[21][1]), "=&v"(pk[21][2]), "=&v"(pk[21][3]), "=&v"(pk[21][4]), "=&v"(pk[21][5]), "=&v"(pk[21][6]), "=&v"(pk[21][7]), "=&v"(pk[21][8]), "=&v"(pk[21][9]), "=&v"(pk[21][10]), "=&v"(pk[21][11]), "=&v"(pk[21][12]), "+v"(badv)
    : "v"(laddr), "s"(base2v), "s"(0x06040200u), "s"(0x07050301u), "s"(base2v + 0x00ff00ffu), "s"(0x80808080u), "v"(vofr), "s"(vbase), "s"(ring)
    : "memory", "scc", "v0", "v1", "v2", "v3", "v4", "v5", "v6", "v7", "v8", "v9", "v10", "v11", "v12", "v13", "v14", "v15", "v16", "v17", "v18", "v19", "v20", "v21", "v22", "v23", "v24", "v25", "v26", "v27", "v28", "v29", "v30", "v31", "v32", "v33");
  badmask |= (__any(badv != 0) ? 1u : 0u) << 10; badv = 0;
  pace(tp, 1708);
  asm volatile(
    "s_waitcnt vmcnt(14)\n\t"
    "ds_read_b128 v[16:19], %14 offset:24576\n\t"
    "ds_read_b128 v[20:23], %14 offset:25600\n\t"
    "ds_read_b128 v[24:27], %14 offset:26624\n\t"
    "ds_read_b128 v[28:31], %14 offset:27648\n\t"
    "v_xor_b32 v15, 212992, %20\n\t"
    "s_add_u32 m0, %22, 8192\n\t"
    "s_nop 0\n\t"
    "global_load_lds_dwordx4 v15, %21 nt\n\t"
    "global_load_lds_dwordx4 v15, %21 offset:1024 nt\n\t"
    "global_load_lds_dwordx4 v15, %21 offset:2048 nt\n\t"
    "global_load_lds_dwordx4 v15, %21 offset:3072 nt\n\t"
    "v_mov_b32 v32, -1\n\t"
    "v_mov_b32 v33, 0\n\t"
    "s_waitcnt lgkmcnt(2)\n\t"
    "v_perm_b32 %0, v17, v16, %16\n\t"
    "v_perm_b32 %3, v21, v20, %16\n\t"
    "v_perm_b32 v0, v17, v16, %17\n\t"
    "v_perm_b32 v7, v21, v20, %17\n\t"
    "v_perm_b32 %1, v19, v18, %16\n\t"
    "v_perm_b32 %4, v23, v22, %16\n\t"
    "v_perm_b32 v1, v19, v18, %17\n\t"
    "v_perm_b32 v8, v23, v22, %17\n\t"
    "v_and_b32 v16, 0x7fff7fff, v16\n\t"
    "v_and_b32 v20, 0x7fff7fff, v20\n\t"
    "v_pk_add_u16 v2, v16, -1\n\t"
    "v_pk_add_u16 v9, v20, -1\n\t"
    "v_pk_sub_u16 v16, v16, %15 clamp\n\t"
    "v_pk_sub_u16 v20, v20, %15 clamp\n\t"
    "v_pk_min_u16 v32, v32, v2\n\t"
    "v_pk_min_u16 v32, v32, v9\n\t"
    "v_and_b32 v17, 0x7fff7fff, v17\n\t"
    "v_and_b32 v21, 0x7fff7fff, v21\n\t"
    "v_pk_add_u16 v3, v17, -1\n\t"
    "v_pk_add_u16 v10, v21, -1\n\t"
    "v_pk_sub_u16 v17, v17, %15 clamp\n\t"
    "v_pk_sub_u16 v21, v21, %15 clamp\n\t"
    "v_pk_min_u16 v32, v32, v3\n\t"
    "v_pk_min_u16 v32, v32, v10\n\t"
    "v_and_b32 v18, 0x7fff7fff, v18\n\t"
    "v_and_b32 v22, 0x7fff7fff, v22\n\t"
    "v_pk_add_u16 v2, v18, -1\n\t"
    "v_pk_add_u16 v9, v22, -1\n\t"
    "v_pk_sub_u16 v18, v18, %15 clamp\n\t"
    "v_pk_sub_u16 v22, v22, %15 clamp\n\t"
    "v_pk_min_u16 v32, v32, v2\n\t"
    "v_pk_min_u16 v32, v32, v9\n\t"
    "v_and_b32 v19, 0x7fff7fff, v19\n\t"
    "v_and_b32 v23, 0x7fff7fff, v23\n\t"
    "v_pk_add_u16 v3, v19, -1\n\t"
    "v_pk_add_u16 v10, v23, -1\n\t"
    "v_pk_sub_u16 v19, v19, %15 clamp\n\t"
    "v_pk_sub_u16 v23, v23, %15 clamp\n\t"
    "v_pk_min_u16 v32, v32, v3\n\t"
    "v_pk_min_u16 v32, v32, v10\n\t"
    "v_perm_b32 v2, v17, v16, %17\n\t"
    "v_perm_b32 v9, v21, v20, %17\n\t"
    "v_perm_b32 v3, v19, v18, %17\n\t"
    "v_perm_b32 v10, v23, v22, %17\n\t"
    "v_or3_b32 v33, v33, v2, v3\n\t"
    "v_or3_b32 v33, v33, v9, v10\n\t"
    "v_lshl_or_b32 %2, v3, 4, v2\n\t"
    "v_lshl_or_b32 %5, v10, 4, v9\n\t"
    "v_and_b32 %12, %19, v0\n\t"
    "v_lshrrev_b32 %12, 1, %12\n\t"
    "v_and_or_b32 %12, v1, %19, %12\n\t"
    "v_lshrrev_b32 %12, 1, %12\n\t"
    "v_and_or_b32 %12, v7, %19, %12\n\t"
    "v_lshrrev_b32 %12, 1, %12\n\t"
    "v_and_or_b32 %12, v8, %19, %12\n\t"
    "s_waitcnt lgkmcnt(0)\n\t"
    "v_perm_b32 %6, v25, v24, %16\n\t"
    "v_perm_b32 %9, v29, v28, %16\n\t"
    "v_perm_b32 v0, v25, v24, %17\n\t"
    "v_perm_b32 v7, v29, v28, %17\n\t"
    "v_perm_b32 %7, v27, v26, %16\n\t"
    "v_perm_b32 %10, v31, v30, %16\n\t"
    "v_perm_b32 v1, v27, v26, %17\n\t"
    "v_perm_b32 v8, v31, v30, %17\n\t"
    "v_and_b32 v24, 0x7fff7fff, v24\n\t"
    "v_and_b32 v28, 0x7fff7fff, v28\n\t"
    "v_pk_add_u16 v2, v24, -1\n\t"
    "v_pk_add_u16 v9, v28, -1\n\t"
    "v_pk_sub_u16 v24, v24, %15 clamp\n\t"
    "v_pk_sub_u16 v28, v28, %15 clamp\n\t"
    "v_pk_min_u16 v32, v32, v2\n\t"
    "v_pk_min_u16 v32, v32, v9\n\t"
    "v_and_b32 v25, 0x7fff7fff, v25\n\t"
    "v_and_b32 v29, 0x7fff7fff, v29\n\t"
    "v_pk_add_u16 v3, v25, -1\n\t"
    "v_pk_add_u16 v10, v29, -1\n\t"
    "v_pk_sub_u16 v25, v25, %15 clamp\n\t"
    "v_pk_sub_u16 v29, v29, %15 clamp\n\t"
    "v_pk_min_u16 v32, v32, v3\n\t"
    "v_pk_min_u16 v32, v32, v10\n\t"
    "v_and_b32 v26, 0x7fff7fff, v26\n\t"
    "v_and_b32 v30, 0x7fff7fff, v30\n\t"
    "v_pk_add_u16 v2, v26, -1\n\t"
    "v_pk_add_u16 v9, v30, -1\n\t"
    "v_pk_sub_u16 v26, v26, %15 clamp\n\t"
    "v_pk_sub_u16 v30, v30, %15 clamp\n\t"
    "v_pk_min_u16 v32, v32, v2\n\t"
    "v_pk_min_u16 v32, v32, v9\n\t"
    "v_and_b32 v27, 0x7fff7fff, v27\n\t"
    "v_and_b32 v31, 0x7fff7fff, v31\n\t"
    "v_pk_add_u16 v3, v27, -1\n\t"
    "v_pk_add_u16 v10, v31, -1\n\t"
    "v_pk_sub_u16 v27, v27, %15 clamp\n\t"
    "v_pk_sub_u16 v31, v31, %15 clamp\n\t"
    "v_pk_min_u16 v32, v32, v3\n\t"
    "v_pk_min_u16 v32, v32, v10\n\t"
    "v_perm_b32 v2, v25, v24, %17\n\t"
    "v_perm_b32 v9, v29, v28, %17\n\t"
    "v_perm_b32 v3, v27, v26, %17\n\t"
    "v_perm_b32 v10, v31, v30, %17\n\t"
    "v_or3_b32 v33, v33, v2, v3\n\t"
    "v_or3_b32 v33, v33, v9, v10\n\t"
    "v_lshl_or_b32 %8, v3, 4, v2\n\t"
    "v_lshl_or_b32 %11, v10, 4, v9\n\t"
    "v_lshrrev_b32 %12, 1, %12\n\t"
    "v_and_or_b32 %12, v0, %19, %12\n\t"
    "v_lshrrev_b32 %12, 1, %12\n\t"
    "v_and_or_b32 %12, v1, %19, %12\n\t"
    "v_lshrrev_b32 %12, 1, %12\n\t"
    "v_and_or_b32 %12, v7, %19, %12\n\t"
    "v_lshrrev_b32 %12, 1, %12\n\t"
    "v_and_or_b32 %12, v8, %19, %12\n\t"
    "v_pk_sub_u16 v32, %18, v32 clamp\n\t"
    "v_and_b32 v33, 0xf0f0f0f0, v33\n\t"
    "v_or3_b32 %13, %13, v32, v33\n\t"
    : "=&v"(pk[22][0]), "=&v"(pk[22][1]), "=&v"(pk[22][2]), "=&v"(pk[22][3]), "=&v"(pk[22][4]), "=&v"(pk[22][5]), "=&v"(pk[22][6]), "=&v"(pk[22][7]), "=&v"(pk[22][8]), "=&v"(pk[22][9]), "=&v"(pk[22][10]), "=&v"(pk[22][11]), "=&v"(pk[22][12]), "+v"(badv)
    : "v"(laddr), "s"(base2k), "s"(0x06040200u), "s"(0x07050301u), "s"(base2k + 0x00ff00ffu), "s"(0x80808080u), "v"(koff), "s"(kbase), "s"(ring)
    : "memory", "scc", "v0", "v1", "v2", "v3", "v4", "v5", "v6", "v7", "v8", "v9", "v10", "v11", "v12", "v13", "v14", "v15", "v16", "v17", "v18", "v19", "v20", "v21", "v22", "v23", "v24", "v25", "v26", "v27", "v28", "v29", "v30", "v31", "v32", "v33");
  asm volatile("" : "+v"(gur[0]), "+v"(gur[1]), "+v"(gur[2]), "+v"(gur[3]));
  asm volatile("" : "+v"(wor[0]), "+v"(wor[1]), "+v"(wor[2]), "+v"(wor[3]), "+v"(wor[4]), "+v"(wor[5]), "+v"(wor[6]), "+v"(wor[7]), "+v"(wor[8]), "+v"(wor[9]), "+v"(wor[10]), "+v"(wor[11]), "+v"(wor[12]), "+v"(wor[13]), "+v"(wor[14]), "+v"(wor[15]));
  asm volatile("" : "+v"(wdr[0]), "+v"(wdr[1]), "+v"(wdr[2]), "+v"(wdr[3]), "+v"(wdr[4]), "+v"(wdr[5]), "+v"(wdr[6]), "+v"(wdr[7]));
  repack_weights();
  pace(tp, 1769);
  asm volatile(
    "s_waitcnt vmcnt(14)\n\t"
    "ds_read_b128 v[16:19], %14 offset:28672\n\t"
    "ds_read_b128 v[20:23], %14 offset:29696\n\t"
    "ds_read_b128 v[24:27], %14 offset:30720\n\t"
    "ds_read_b128 v[28:31], %14 offset:31744\n\t"
    "v_xor_b32 v15, 212992, %20\n\t"
    "s_add_u32 m0, %22, 12288\n\t"
    "s_nop 0\n\t"
    "global_load_lds_dwordx4 v15, %21 nt\n\t"
    "global_load_lds_dwordx4 v15, %21 offset:1024 nt\n\t"
    "global_load_lds_dwordx4 v15, %21 offset:2048 nt\n\t"
    "global_load_lds_dwordx4 v15, %21 offset:3072 nt\n\t"
    "v_mov_b32 v32, -1\n\t"
    "v_mov_b32 v33, 0\n\t"
    "s_waitcnt lgkmcnt(2)\n\t"
    "v_perm_b32 %0, v17, v16, %16\n\t"
    "v_perm_b32 %3, v21, v20, %16\n\t"
    "v_perm_b32 v0, v17, v16, %17\n\t"
    "v_perm_b32 v7, v21, v20, %17\n\t"
    "v_perm_b32 %1, v19, v18, %16\n\t"
    "v_perm_b32 %4, v23, v22, %16\n\t"
    "v_perm_b32 v1, v19, v18, %17\n\t"
    "v_perm_b32 v8, v23, v22, %17\n\t"
    "v_and_b32 v16, 0x7fff7fff, v16\n\t"
    "v_and_b32 v20, 0x7fff7fff, v20\n\t"
    "v_pk_add_u16 v2, v16, -1\n\t"
    "v_pk_add_u16 v9, v20, -1\n\t"
    "v_pk_sub_u16 v16, v16, %15 clamp\n\t"
    "v_pk_sub_u16 v20, v20, %15 clamp\n\t"
    "v_pk_min_u16 v32, v32, v2\n\t"
    "v_pk_min_u16 v32, v32, v9\n\t"
    "v_and_b32 v17, 0x7fff7fff, v17\n\t"
    "v_and_b32 v21, 0x7fff7fff, v21\n\t"
    "v_pk_add_u16 v3, v17, -1\n\t"
    "v_pk_add_u16 v10, v21, -1\n\t"
    "v_pk_sub_u16 v17, v17, %15 clamp\n\t"
    "v_pk_sub_u16 v21, v21, %15 clamp\n\t"
    "v_pk_min_u16 v32, v32, v3\n\t"
    "v_pk_min_u16 v32, v32, v10\n\t"
    "v_and_b32 v18, 0x7fff7fff, v18\n\t"
    "v_and_b32 v22, 0x7fff7fff, v22\n\t"
    "v_pk_add_u16 v2, v18, -1\n\t"
    "v_pk_add_u16 v9, v22, -1\n\t"
    "v_pk_sub_u16 v18, v18, %15 clamp\n\t"
    "v_pk_sub_u16 v22, v22, %15 clamp\n\t"
    "v_pk_min_u16 v32, v32, v2\n\t"
    "v_pk_min_u16 v32, v32, v9\n\t"
    "v_and_b32 v19, 0x7fff7fff, v19\n\t"
    "v_and_b32 v23, 0x7fff7fff, v23\n\t"
    "v_pk_add_u16 v3, v19, -1\n\t"
    "v_pk_add_u16 v10, v23, -1\n\t"
    "v_pk_sub_u16 v19, v19, %15 clamp\n\t"
    "v_pk_sub_u16 v23, v23, %15 clamp\n\t"
    "v_pk_min_u16 v32, v32, v3\n\t"
    "v_pk_min_u16 v32, v32, v10\n\t"
    "v_perm_b32 v2, v17, v16, %17\n\t"
    "v_perm_b32 v9, v21, v20, %17\n\t"
    "v_perm_b32 v3, v19, v18, %17\n\t"
    "v_perm_b32 v10, v23, v22, %17\n\t"
    "v_or3_b32 v33, v33, v2, v3\n\t"
    "v_or3_b32 v33, v33, v9, v10\n\t"
    "v_lshl_or_b32 %2, v3, 4, v2\n\t"
    "v_lshl_or_b32 %5, v10, 4, v9\n\t"
    "v_and_b32 %12, %19, v0\n\t"
    "v_lshrrev_b32 %12, 1, %12\n\t"
    "v_and_or_b32 %12, v1, %19, %12\n\t"
    "v_lshrrev_b32 %12, 1, %12\n\t"
    "v_and_or_b32 %12, v7, %19, %12\n\t"
    "v_lshrrev_b32 %12, 1, %12\n\t"
    "v_and_or_b32 %12, v8, %19, %12\n\t"
    "s_waitcnt lgkmcnt(0)\n\t"
    "v_perm_b32 %6, v25, v24, %16\n\t"
    "v_perm_b32 %9, v29, v28, %16\n\t"
    "v_perm_b32 v0, v25, v24, %17\n\t"
    "v_perm_b32 v7, v29, v28, %17\n\t"
    "v_perm_b32 %7, v27, v26, %16\n\t"
    "v_perm_b32 %10, v31, v30, %16\n\t"
    "v_perm_b32 v1, v27, v26, %17\n\t"
    "v_perm_b32 v8, v31, v30, %17\n\t"
    "v_and_b32 v24, 0x7fff7fff, v24\n\t"
    "v_and_b32 v28, 0x7fff7fff, v28\n\t"
    "v_pk_add_u16 v2, v24, -1\n\t"
    "v_pk_add_u16 v9, v28, -1\n\t"
    "v_pk_sub_u16 v24, v24, %15 clamp\n\t"
    "v_pk_sub_u16 v28, v28, %15 clamp\n\t"
    "v_pk_min_u16 v32, v32, v2\n\t"
    "v_pk_min_u16 v32, v32, v9\n\t"
    "v_and_b32 v25, 0x7fff7fff, v25\n\t"
    "v_and_b32 v29, 0x7fff7fff, v29\n\t"
    "v_pk_add_u16 v3, v25, -1\n\t"
    "v_pk_add_u16 v10, v29, -1\n\t"
    "v_pk_sub_u16 v25, v25, %15 clamp\n\t"
    "v_pk_sub_u16 v29, v29, %15 clamp\n\t"
    "v_pk_min_u16 v32, v32, v3\n\t"
    "v_pk_min_u16 v32, v32, v10\n\t"
    "v_and_b32 v26, 0x7fff7fff, v26\n\t"
    "v_and_b32 v30, 0x7fff7fff, v30\n\t"
    "v_pk_add_u16 v2, v26, -1\n\t"
    "v_pk_add_u16 v9, v30, -1\n\t"
    "v_pk_sub_u16 v26, v26, %15 clamp\n\t"
    "v_pk_sub_u16 v30, v30, %15 clamp\n\t"
    "v_pk_min_u16 v32, v32, v2\n\t"
    "v_pk_min_u16 v32, v32, v9\n\t"
    "v_and_b32 v27, 0x7fff7fff, v27\n\t"
    "v_and_b32 v31, 0x7fff7fff, v31\n\t"
    "v_pk_add_u16 v3, v27, -1\n\t"
    "v_pk_add_u16 v10, v31, -1\n\t"
    "v_pk_sub_u16 v27, v27, %15 clamp\n\t"
    "v_pk_sub_u16 v31, v31, %15 clamp\n\t"
    "v_pk_min_u16 v32, v32, v3\n\t"
    "v_pk_min_u16 v32, v32, v10\n\t"
    "v_perm_b32 v2, v25, v24, %17\n\t"
    "v_perm_b32 v9, v29, v28, %17\n\t"
    "v_perm_b32 v3, v27, v26, %17\n\t"
    "v_perm_b32 v10, v31, v30, %17\n\t"
    "v_or3_b32 v33, v33, v2, v3\n\t"
    "v_or3_b32 v33, v33, v9, v10\n\t"
    "v_lshl_or_b32 %8, v3, 4, v2\n\t"
    "v_lshl_or_b32 %11, v10, 4, v9\n\t"
    "v_lshrrev_b32 %12, 1, %12\n\t"
    "v_and_or_b32 %12, v0, %19, %12\n\t"
    "v_lshrrev_b32 %12, 1, %12\n\t"
    "v_and_or_b32 %12, v1, %19, %12\n\t"
    "v_lshrrev_b32 %12, 1, %12\n\t"
    "v_and_or_b32 %12, v7, %19, %12\n\t"
    "v_lshrrev_b32 %12, 1, %12\n\t"
    "v_and_or_b32 %12, v8, %19, %12\n\t"
    "v_pk_sub_u16 v32, %18, v32 clamp\n\t"
    "v_and_b32 v33, 0xf0f0f0f0, v33\n\t"
    "v_or3_b32 %13, %13, v32, v33\n\t"
    : "=&v"(pk[23][0]), "=&v"(pk[23][1]), "=&v"(pk[23][2]), "=&v"(pk[23][3]), "=&v"(pk[23][4]), "=&v"(pk[23][5]), "=&v"(pk[23][6]), "=&v"(pk[23][7]), "=&v"(pk[23][8]), "=&v"(pk[23][9]), "=&v"(pk[23][10]), "=&v"(pk[23][11]), "=&v"(pk[23][12]), "+v"(badv)
    : "v"(laddr), "s"(base2v), "s"(0x06040200u), "s"(0x07050301u), "s"(base2v + 0x00ff00ffu), "s"(0x80808080u), "v"(vofr), "s"(vbase), "s"(ring)
    : "memory", "scc", "v0", "v1", "v2", "v3", "v4", "v5", "v6", "v7", "v8", "v9", "v10", "v11", "v12", "v13", "v14", "v15", "v16", "v17", "v18", "v19", "v20", "v21", "v22", "v23", "v24", "v25", "v26", "v27", "v28", "v29", "v30", "v31", "v32", "v33");
  badmask |= (__any(badv != 0) ? 1u : 0u) << 11; badv = 0;
  pace(tp, 1830);
  asm volatile(
    "s_waitcnt vmcnt(14)\n\t"
    "ds_read_b128 v[16:19], %14 offset:0\n\t"
    "ds_read_b128 v[20:23], %14 offset:1024\n\t"
    "ds_read_b128 v[24:27], %14 offset:2048\n\t"
    "ds_read_b128 v[28:31], %14 offset:3072\n\t"
    "v_xor_b32 v15, 229376, %20\n\t"
    "s_add_u32 m0, %22, 16384\n\t"
    "s_nop 0\n\t"
    "global_load_lds_dwordx4 v15, %21 nt\n\t"
    "global_load_lds_dwordx4 v15, %21 offset:1024 nt\n\t"
    "global_load_lds_dwordx4 v15, %21 offset:2048 nt\n\t"
    "global_load_lds_dwordx4 v15, %21 offset:3072 nt\n\t"
    "v_mov_b32 v32, -1\n\t"
    "v_mov_b32 v33, 0\n\t"
    "s_waitcnt lgkmcnt(2)\n\t"
    "v_perm_b32 %0, v17, v16, %16\n\t"
    "v_perm_b32 %3, v21, v20, %16\n\t"
    "v_perm_b32 v0, v17, v16, %17\n\t"
    "v_perm_b32 v7, v21, v20, %17\n\t"
    "v_perm_b32 %1, v19, v18, %16\n\t"
    "v_perm_b32 %4, v23, v22, %16\n\t"
    "v_perm_b32 v1, v19, v18, %17\n\t"
    "v_perm_b32 v8, v23, v22, %17\n\t"
    "v_and_b32 v16, 0x7fff7fff, v16\n\t"
    "v_and_b32 v20, 0x7fff7fff, v20\n\t"
    "v_pk_add_u16 v2, v16, -1\n\t"
    "v_pk_add_u16 v9, v20, -1\n\t"
    "v_pk_sub_u16 v16, v16, %15 clamp\n\t"
    "v_pk_sub_u16 v20, v20, %15 clamp\n\t"
    "v_pk_min_u16 v32, v32, v2\n\t"
    "v_pk_min_u16 v32, v32, v9\n\t"
    "v_and_b32 v17, 0x7fff7fff, v17\n\t"
    "v_and_b32 v21, 0x7fff7fff, v21\n\t"
    "v_pk_add_u16 v3, v17, -1\n\t"
    "v_pk_add_u16 v10, v21, -1\n\t"
    "v_pk_sub_u16 v17, v17, %15 clamp\n\t"
    "v_pk_sub_u16 v21, v21, %15 clamp\n\t"
    "v_pk_min_u16 v32, v32, v3\n\t"
    "v_pk_min_u16 v32, v32, v10\n\t"
    "v_and_b32 v18, 0x7fff7fff, v18\n\t"
    "v_and_b32 v22, 0x7fff7fff, v22\n\t"
    "v_pk_add_u16 v2, v18, -1\n\t"
    "v_pk_add_u16 v9, v22, -1\n\t"
    "v_pk_sub_u16 v18, v18, %15 clamp\n\t"
    "v_pk_sub_u16 v22, v22, %15 clamp\n\t"
    "v_pk_min_u16 v32, v32, v2\n\t"
    "v_pk_min_u16 v32, v32, v9\n\t"
    "v_and_b32 v19, 0x7fff7fff, v19\n\t"
    "v_and_b32 v23, 0x7fff7fff, v23\n\t"
    "v_pk_add_u16 v3, v19, -1\n\t"
    "v_pk_add_u16 v10, v23, -1\n\t"
    "v_pk_sub_u16 v19, v19, %15 clamp\n\t"
    "v_pk_sub_u16 v23, v23, %15 clamp\n\t"
    "v_pk_min_u16 v32, v32, v3\n\t"
    "v_pk_min_u16 v32, v32, v10\n\t"
    "v_perm_b32 v2, v17, v16, %17\n\t"
    "v_perm_b32 v9, v21, v20, %17\n\t"
    "v_perm_b32 v3, v19, v18, %17\n\t"
    "v_perm_b32 v10, v23, v22, %17\n\t"
    "v_or3_b32 v33, v33, v2, v3\n\t"
    "v_or3_b32 v33, v33, v9, v10\n\t"
    "v_lshl_or_b32 %2, v3, 4, v2\n\t"
    "v_lshl_or_b32 %5, v10, 4, v9\n\t"
    "v_and_b32 %12, %19, v0\n\t"
    "v_lshrrev_b32 %12, 1, %12\n\t"
    "v_and_or_b32 %12, v1, %19, %12\n\t"
    "v_lshrrev_b32 %12, 1, %12\n\t"
    "v_and_or_b32 %12, v7, %19, %12\n\t"
    "v_lshrrev_b32 %12, 1, %12\n\t"
    "v_and_or_b32 %12, v8, %19, %12\n\t"
    "s_waitcnt lgkmcnt(0)\n\t"
    "v_perm_b32 %6, v25, v24, %16\n\t"
    "v_perm_b32 %9, v29, v28, %16\n\t"
    "v_perm_b32 v0, v25, v24, %17\n\t"
    "v_perm_b32 v7, v29, v28, %17\n\t"
    "v_perm_b32 %7, v27, v26, %16\n\t"
    "v_perm_b32 %10, v31, v30, %16\n\t"
    "v_perm_b32 v1, v27, v26, %17\n\t"
    "v_perm_b32 v8, v31, v30, %17\n\t"
    "v_and_b32 v24, 0x7fff7fff, v24\n\t"
    "v_and_b32 v28, 0x7fff7fff, v28\n\t"
    "v_pk_add_u16 v2, v24, -1\n\t"
    "v_pk_add_u16 v9, v28, -1\n\t"
    "v_pk_sub_u16 v24, v24, %15 clamp\n\t"
    "v_pk_sub_u16 v28, v28, %15 clamp\n\t"
    "v_pk_min_u16 v32, v32, v2\n\t"
    "v_pk_min_u16 v32, v32, v9\n\t"
    "v_and_b32 v25, 0x7fff7fff, v25\n\t"
    "v_and_b32 v29, 0x7fff7fff, v29\n\t"
    "v_pk_add_u16 v3, v25, -1\n\t"
    "v_pk_add_u16 v10, v29, -1\n\t"
    "v_pk_sub_u16 v25, v25, %15 clamp\n\t"
    "v_pk_sub_u16 v29, v29, %15 clamp\n\t"
    "v_pk_min_u16 v32, v32, v3\n\t"
    "v_pk_min_u16 v32, v32, v10\n\t"
    "v_and_b32 v26, 0x7fff7fff, v26\n\t"
    "v_and_b32 v30, 0x7fff7fff, v30\n\t"
    "v_pk_add_u16 v2, v26, -1\n\t"
    "v_pk_add_u16 v9, v30, -1\n\t"
    "v_pk_sub_u16 v26, v26, %15 clamp\n\t"
    "v_pk_sub_u16 v30, v30, %15 clamp\n\t"
    "v_pk_min_u16 v32, v32, v2\n\t"
    "v_pk_min_u16 v32, v32, v9\n\t"
    "v_and_b32 v27, 0x7fff7fff, v27\n\t"
    "v_and_b32 v31, 0x7fff7fff, v31\n\t"
    "v_pk_add_u16 v3, v27, -1\n\t"
    "v_pk_add_u16 v10, v31, -1\n\t"
    "v_pk_sub_u16 v27, v27, %15 clamp\n\t"
    "v_pk_sub_u16 v31, v31, %15 clamp\n\t"
    "v_pk_min_u16 v32, v32, v3\n\t"
    "v_pk_min_u16 v32, v32, v10\n\t"
    "v_perm_b32 v2, v25, v24, %17\n\t"
    "v_perm_b32 v9, v29, v28, %17\n\t"
    "v_perm_b32 v3, v27, v26, %17\n\t"
    "v_perm_b32 v10, v31, v30, %17\n\t"
    "v_or3_b32 v33, v33, v2, v3\n\t"
    "v_or3_b32 v33, v33, v9, v10\n\t"
    "v_lshl_or_b32 %8, v3, 4, v2\n\t"
    "v_lshl_or_b32 %11, v10, 4, v9\n\t"
    "v_lshrrev_b32 %12, 1, %12\n\t"
    "v_and_or_b32 %12, v0, %19, %12\n\t"
    "v_lshrrev_b32 %12, 1, %12\n\t"
    "v_and_or_b32 %12, v1, %19, %12\n\t"
    "v_lshrrev_b32 %12, 1, %12\n\t"
    "v_and_or_b32 %12, v7, %19, %12\n\t"
    "v_lshrrev_b32 %12, 1, %12\n\t"
    "v_and_or_b32 %12, v8, %19, %12\n\t"
    "v_pk_sub_u16 v32, %18, v32 clamp\n\t"
    "v_and_b32 v33, 0xf0f0f0f0, v33\n\t"
    "v_or3_b32 %13, %13, v32, v33\n\t"
    : "=&v"(pk[24][0]), "=&v"(pk[24][1]), "=&v"(pk[24][2]), "=&v"(pk[24][3]), "=&v"(pk[24][4]), "=&v"(pk[24][5]), "=&v"(pk[24][6]), "=&v"(pk[24][7]), "=&v"(pk[24][8]), "=&v"(pk[24][9]), "=&v"(pk[24][10]), "=&v"(pk[24][11]), "=&v"(pk[24][12]), "+v"(badv)
    : "v"(laddr), "s"(base2k), "s"(0x06040200u), "s"(0x07050301u), "s"(base2k + 0x00ff00ffu), "s"(0x80808080u), "v"(koff), "s"(kbase), "s"(ring)
    : "memory", "scc", "v0", "v1", "v2", "v3", "v4", "v5", "v6", "v7", "v8", "v9", "v10", "v11", "v12", "v13", "v14", "v15", "v16", "v17", "v18", "v19", "v20", "v21", "v22", "v23", "v24", "v25", "v26", "v27", "v28", "v29", "v30", "v31", "v32", "v33");
  asm volatile("s_waitcnt vmcnt(16)" :  : : "memory");
  lds_barrier();
  { u32x4 qg[8]; for (int j = 0; j < 8; ++j) qg[j] = *(LAS const u32x4*)((LAS const char*)obuf + j * 1024 + voff); qf = q_sum(qg); }
  pace(tp, 1891);
  asm volatile(
    "s_waitcnt vmcnt(12)\n\t"
    "ds_read_b128 v[16:19], %14 offset:4096\n\t"
    "ds_read_b128 v[20:23], %14 offset:5120\n\t"
    "ds_read_b128 v[24:27], %14 offset:6144\n\t"
    "ds_read_b128 v[28:31], %14 offset:7168\n\t"
    "v_xor_b32 v15, 229376, %20\n\t"
    "s_add_u32 m0, %22, 20480\n\t"
    "s_nop 0\n\t"
    "global_load_lds_dwordx4 v15, %21 nt\n\t"
    "global_load_lds_dwordx4 v15, %21 offset:1024 nt\n\t"
    "global_load_lds_dwordx4 v15, %21 offset:2048 nt\n\t"
    "global_load_lds_dwordx4 v15, %21 offset:3072 nt\n\t"
    "v_mov_b32 v32, -1\n\t"
    "v_mov_b32 v33, 0\n\t"
    "s_waitcnt lgkmcnt(2)\n\t"
    "v_perm_b32 %0, v17, v16, %16\n\t"
    "v_perm_b32 %3, v21, v20, %16\n\t"
    "v_perm_b32 v0, v17, v16, %17\n\t"
    "v_perm_b32 v7, v21, v20, %17\n\t"
    "v_perm_b32 %1, v19, v18, %16\n\t"
    "v_perm_b32 %4, v23, v22, %16\n\t"
    "v_perm_b32 v1, v19, v18, %17\n\t"
    "v_perm_b32 v8, v23, v22, %17\n\t"
    "v_and_b32 v16, 0x7fff7fff, v16\n\t"
    "v_and_b32 v20, 0x7fff7fff, v20\n\t"
    "v_pk_add_u16 v2, v16, -1\n\t"
    "v_pk_add_u16 v9, v20, -1\n\t"
    "v_pk_sub_u16 v16, v16, %15 clamp\n\t"
    "v_pk_sub_u16 v20, v20, %15 clamp\n\t"
    "v_pk_min_u16 v32, v32, v2\n\t"
    "v_pk_min_u16 v32, v32, v9\n\t"
    "v_and_b32 v17, 0x7fff7fff, v17\n\t"
    "v_and_b32 v21, 0x7fff7fff, v21\n\t"
    "v_pk_add_u16 v3, v17, -1\n\t"
    "v_pk_add_u16 v10, v21, -1\n\t"
    "v_pk_sub_u16 v17, v17, %15 clamp\n\t"
    "v_pk_sub_u16 v21, v21, %15 clamp\n\t"
    "v_pk_min_u16 v32, v32, v3\n\t"
    "v_pk_min_u16 v32, v32, v10\n\t"
    "v_and_b32 v18, 0x7fff7fff, v18\n\t"
    "v_and_b32 v22, 0x7fff7fff, v22\n\t"
    "v_pk_add_u16 v2, v18, -1\n\t"
    "v_pk_add_u16 v9, v22, -1\n\t"
    "v_pk_sub_u16 v18, v18, %15 clamp\n\t"
    "v_pk_sub_u16 v22, v22, %15 clamp\n\t"
    "v_pk_min_u16 v32, v32, v2\n\t"
    "v_pk_min_u16 v32, v32, v9\n\t"
    "v_and_b32 v19, 0x7fff7fff, v19\n\t"
    "v_and_b32 v23, 0x7fff7fff, v23\n\t"
    "v_pk_add_u16 v3, v19, -1\n\t"
    "v_pk_add_u16 v10, v23, -1\n\t"
    "v_pk_sub_u16 v19, v19, %15 clamp\n\t"
    "v_pk_sub_u16 v23, v23, %15 clamp\n\t"
    "v_pk_min_u16 v32, v32, v3\n\t"
    "v_pk_min_u16 v32, v32, v10\n\t"
    "v_perm_b32 v2, v17, v16, %17\n\t"
    "v_perm_b32 v9, v21, v20, %17\n\t"
    "v_perm_b32 v3, v19, v18, %17\n\t"
    "v_perm_b32 v10, v23, v22, %17\n\t"
    "v_or3_b32 v33, v33, v2, v3\n\t"
    "v_or3_b32 v33, v33, v9, v10\n\t"
    "v_lshl_or_b32 %2, v3, 4, v2\n\t"
    "v_lshl_or_b32 %5, v10, 4, v9\n\t"
    "v_and_b32 %12, %19, v0\n\t"
    "v_lshrrev_b32 %12, 1, %12\n\t"
    "v_and_or_b32 %12, v1, %19, %12\n\t"
    "v_lshrrev_b32 %12, 1, %12\n\t"
    "v_and_or_b32 %12, v7, %19, %12\n\t"
    "v_lshrrev_b32 %12, 1, %12\n\t"
    "v_and_or_b32 %12, v8, %19, %12\n\t"
    "s_waitcnt lgkmcnt(0)\n\t"
    "v_perm_b32 %6, v25, v24, %16\n\t"
    "v_perm_b32 %9, v29, v28, %16\n\t"
    "v_perm_b32 v0, v25, v24, %17\n\t"
    "v_perm_b32 v7, v29, v28, %17\n\t"
    "v_perm_b32 %7, v27, v26, %16\n\t"
    "v_perm_b32 %10, v31, v30, %16\n\t"
    "v_perm_b32 v1, v27, v26, %17\n\t"
    "v_perm_b32 v8, v31, v30, %17\n\t"
    "v_and_b32 v24, 0x7fff7fff, v24\n\t"
    "v_and_b32 v28, 0x7fff7fff, v28\n\t"
    "v_pk_add_u16 v2, v24, -1\n\t"
    "v_pk_add_u16 v9, v28, -1\n\t"
    "v_pk_sub_u16 v24, v24, %15 clamp\n\t"
    "v_pk_sub_u16 v28, v28, %15 clamp\n\t"
    "v_pk_min_u16 v32, v32, v2\n\t"
    "v_pk_min_u16 v32, v32, v9\n\t"
    "v_and_b32 v25, 0x7fff7fff, v25\n\t"
    "v_and_b32 v29, 0x7fff7fff, v29\n\t"
    "v_pk_add_u16 v3, v25, -1\n\t"
    "v_pk_add_u16 v10, v29, -1\n\t"
    "v_pk_sub_u16 v25, v25, %15 clamp\n\t"
    "v_pk_sub_u16 v29, v29, %15 clamp\n\t"
    "v_pk_min_u16 v32, v32, v3\n\t"
    "v_pk_min_u16 v32, v32, v10\n\t"
    "v_and_b32 v26, 0x7fff7fff, v26\n\t"
    "v_and_b32 v30, 0x7fff7fff, v30\n\t"
    "v_pk_add_u16 v2, v26, -1\n\t"
    "v_pk_add_u16 v9, v30, -1\n\t"
    "v_pk_sub_u16 v26, v26, %15 clamp\n\t"
    "v_pk_sub_u16 v30, v30, %15 clamp\n\t"
    "v_pk_min_u16 v32, v32, v2\n\t"
    "v_pk_min_u16 v32, v32, v9\n\t"
    "v_and_b32 v27, 0x7fff7fff, v27\n\t"
    "v_and_b32 v31, 0x7fff7fff, v31\n\t"
    "v_pk_add_u16 v3, v27, -1\n\t"
    "v_pk_add_u16 v10, v31, -1\n\t"
    "v_pk_sub_u16 v27, v27, %15 clamp\n\t"
    "v_pk_sub_u16 v31, v31, %15 clamp\n\t"
    "v_pk_min_u16 v32, v32, v3\n\t"
    "v_pk_min_u16 v32, v32, v10\n\t"
    "v_perm_b32 v2, v25, v24, %17\n\t"
    "v_perm_b32 v9, v29, v28, %17\n\t"
    "v_perm_b32 v3, v27, v26, %17\n\t"
    "v_perm_b32 v10, v31, v30, %17\n\t"
    "v_or3_b32 v33, v33, v2, v3\n\t"
    "v_or3_b32 v33, v33, v9, v10\n\t"
    "v_lshl_or_b32 %8, v3, 4, v2\n\t"
    "v_lshl_or_b32 %11, v10, 4, v9\n\t"
    "v_lshrrev_b32 %12, 1, %12\n\t"
    "v_and_or_b32 %12, v0, %19, %12\n\t"
    "v_lshrrev_b32 %12, 1, %12\n\t"
    "v_and_or_b32 %12, v1, %19, %12\n\t"
    "v_lshrrev_b32 %12, 1, %12\n\t"
    "v_and_or_b32 %12, v7, %19, %12\n\t"
    "v_lshrrev_b32 %12, 1, %12\n\t"
    "v_and_or_b32 %12, v8, %19, %12\n\t"
    "v_pk_sub_u16 v32, %18, v32 clamp\n\t"
    "v_and_b32 v33, 0xf0f0f0f0, v33\n\t"
    "v_or3_b32 %13, %13, v32, v33\n\t"
    : "=&v"(pk[25][0]), "=&v"(pk[25][1]), "=&v"(pk[25][2]), "=&v"(pk[25][3]), "=&v"(pk[25][4]), "=&v"(pk[25][5]), "=&v"(pk[25][6]), "=&v"(pk[25][7]), "=&v"(pk[25][8]), "=&v"(pk[25][9]), "=&v"(pk[25][10]), "=&v"(pk[25][11]), "=&v"(pk[25][12]), "+v"(badv)
    : "v"(laddr), "s"(base2v), "s"(0x06040200u), "s"(0x07050301u), "s"(base2v + 0x00ff00ffu), "s"(0x80808080u), "v"(vofr), "s"(vbase), "s"(ring)
    : "memory", "scc", "v0", "v1", "v2", "v3", "v4", "v5", "v6", "v7", "v8", "v9", "v10", "v11", "v12", "v13", "v14", "v15", "v16", "v17", "v18", "v19", "v20", "v21", "v22", "v23", "v24", "v25", "v26", "v27", "v28", "v29", "v30", "v31", "v32", "v33");
  badmask |= (__any(badv != 0) ? 1u : 0u) << 12; badv = 0;
  pace(tp, 1952);
  asm volatile(
    "s_waitcnt vmcnt(12)\n\t"
    "ds_read_b128 v[16:19], %14 offset:8192\n\t"
    "ds_read_b128 v[20:23], %14 offset:9216\n\t"
    "ds_read_b128 v[24:27], %14 offset:10240\n\t"
    "ds_read_b128 v[28:31], %14 offset:11264\n\t"
    "v_xor_b32 v15, 245760, %20\n\t"
    "s_add_u32 m0, %22, 24576\n\t"
    "s_nop 0\n\t"
    "global_load_lds_dwordx4 v15, %21 nt\n\t"
    "global_load_lds_dwordx4 v15, %21 offset:1024 nt\n\t"
    "global_load_lds_dwordx4 v15, %21 offset:2048 nt\n\t"
    "global_load_lds_dwordx4 v15, %21 offset:3072 nt\n\t"
    "v_mov_b32 v32, -1\n\t"
    "v_mov_b32 v33, 0\n\t"
    "s_waitcnt lgkmcnt(2)\n\t"
    "v_perm_b32 %0, v17, v16, %16\n\t"
    "v_perm_b32 %3, v21, v20, %16\n\t"
    "v_perm_b32 v0, v17, v16, %17\n\t"
    "v_perm_b32 v7, v21, v20, %17\n\t"
    "v_perm_b32 %1, v19, v18, %16\n\t"
    "v_perm_b32 %4, v23, v22, %16\n\t"
    "v_perm_b32 v1, v19, v18, %17\n\t"
    "v_perm_b32 v8, v23, v22, %17\n\t"
    "v_and_b32 v16, 0x7fff7fff, v16\n\t"
    "v_and_b32 v20, 0x7fff7fff, v20\n\t"
    "v_pk_add_u16 v2, v16, -1\n\t"
    "v_pk_add_u16 v9, v20, -1\n\t"
    "v_pk_sub_u16 v16, v16, %15 clamp\n\t"
    "v_pk_sub_u16 v20, v20, %15 clamp\n\t"
    "v_pk_min_u16 v32, v32, v2\n\t"
    "v_pk_min_u16 v32, v32, v9\n\t"
    "v_and_b32 v17, 0x7fff7fff, v17\n\t"
    "v_and_b32 v21, 0x7fff7fff, v21\n\t"
    "v_pk_add_u16 v3, v17, -1\n\t"
    "v_pk_add_u16 v10, v21, -1\n\t"
    "v_pk_sub_u16 v17, v17, %15 clamp\n\t"
    "v_pk_sub_u16 v21, v21, %15 clamp\n\t"
    "v_pk_min_u16 v32, v32, v3\n\t"
    "v_pk_min_u16 v32, v32, v10\n\t"
    "v_and_b32 v18, 0x7fff7fff, v18\n\t"
    "v_and_b32 v22, 0x7fff7fff, v22\n\t"
    "v_pk_add_u16 v2, v18, -1\n\t"
    "v_pk_add_u16 v9, v22, -1\n\t"
    "v_pk_sub_u16 v18, v18, %15 clamp\n\t"
    "v_pk_sub_u16 v22, v22, %15 clamp\n\t"
    "v_pk_min_u16 v32, v32, v2\n\t"
    "v_pk_min_u16 v32, v32, v9\n\t"
    "v_and_b32 v19, 0x7fff7fff, v19\n\t"
    "v_and_b32 v23, 0x7fff7fff, v23\n\t"
    "v_pk_add_u16 v3, v19, -1\n\t"
    "v_pk_add_u16 v10, v23, -1\n\t"
    "v_pk_sub_u16 v19, v19, %15 clamp\n\t"
    "v_pk_sub_u16 v23, v23, %15 clamp\n\t"
    "v_pk_min_u16 v32, v32, v3\n\t"
    "v_pk_min_u16 v32, v32, v10\n\t"
    "v_perm_b32 v2, v17, v16, %17\n\t"
    "v_perm_b32 v9, v21, v20, %17\n\t"
    "v_perm_b32 v3, v19, v18, %17\n\t"
    "v_perm_b32 v10, v23, v22, %17\n\t"
    "v_or3_b32 v33, v33, v2, v3\n\t"
    "v_or3_b32 v33, v33, v9, v10\n\t"
    "v_lshl_or_b32 %2, v3, 4, v2\n\t"
    "v_lshl_or_b32 %5, v10, 4, v9\n\t"
    "v_and_b32 %12, %19, v0\n\t"
    "v_lshrrev_b32 %12, 1, %12\n\t"
    "v_and_or_b32 %12, v1, %19, %12\n\t"
    "v_lshrrev_b32 %12, 1, %12\n\t"
    "v_and_or_b32 %12, v7, %19, %12\n\t"
    "v_lshrrev_b32 %12, 1, %12\n\t"
    "v_and_or_b32 %12, v8, %19, %12\n\t"
    "s_waitcnt lgkmcnt(0)\n\t"
    "v_perm_b32 %6, v25, v24, %16\n\t"
    "v_perm_b32 %9, v29, v28, %16\n\t"
    "v_perm_b32 v0, v25, v24, %17\n\t"
    "v_perm_b32 v7, v29, v28, %17\n\t"
    "v_perm_b32 %7, v27, v26, %16\n\t"
    "v_perm_b32 %10, v31, v30, %16\n\t"
    "v_perm_b32 v1, v27, v26, %17\n\t"
    "v_perm_b32 v8, v31, v30, %17\n\t"
    "v_and_b32 v24, 0x7fff7fff, v24\n\t"
    "v_and_b32 v28, 0x7fff7fff, v28\n\t"
    "v_pk_add_u16 v2, v24, -1\n\t"
    "v_pk_add_u16 v9, v28, -1\n\t"
    "v_pk_sub_u16 v24, v24, %15 clamp\n\t"
    "v_pk_sub_u16 v28, v28, %15 clamp\n\t"
    "v_pk_min_u16 v32, v32, v2\n\t"
    "v_pk_min_u16 v32, v32, v9\n\t"
    "v_and_b32 v25, 0x7fff7fff, v25\n\t"
    "v_and_b32 v29, 0x7fff7fff, v29\n\t"
    "v_pk_add_u16 v3, v25, -1\n\t"
    "v_pk_add_u16 v10, v29, -1\n\t"
    "v_pk_sub_u16 v25, v25, %15 clamp\n\t"
    "v_pk_sub_u16 v29, v29, %15 clamp\n\t"
    "v_pk_min_u16 v32, v32, v3\n\t"
    "v_pk_min_u16 v32, v32, v10\n\t"
    "v_and_b32 v26, 0x7fff7fff, v26\n\t"
    "v_and_b32 v30, 0x7fff7fff, v30\n\t"
    "v_pk_add_u16 v2, v26, -1\n\t"
    "v_pk_add_u16 v9, v30, -1\n\t"
    "v_pk_sub_u16 v26, v26, %15 clamp\n\t"
    "v_pk_sub_u16 v30, v30, %15 clamp\n\t"
    "v_pk_min_u16 v32, v32, v2\n\t"
    "v_pk_min_u16 v32, v32, v9\n\t"
    "v_and_b32 v27, 0x7fff7fff, v27\n\t"
    "v_and_b32 v31, 0x7fff7fff, v31\n\t"
    "v_pk_add_u16 v3, v27, -1\n\t"
    "v_pk_add_u16 v10, v31, -1\n\t"
    "v_pk_sub_u16 v27, v27, %15 clamp\n\t"
    "v_pk_sub_u16 v31, v31, %15 clamp\n\t"
    "v_pk_min_u16 v32, v32, v3\n\t"
    "v_pk_min_u16 v32, v32, v10\n\t"
    "v_perm_b32 v2, v25, v24, %17\n\t"
    "v_perm_b32 v9, v29, v28, %17\n\t"
    "v_perm_b32 v3, v27, v26, %17\n\t"
    "v_perm_b32 v10, v31, v30, %17\n\t"
    "v_or3_b32 v33, v33, v2, v3\n\t"
    "v_or3_b32 v33, v33, v9, v10\n\t"
    "v_lshl_or_b32 %8, v3, 4, v2\n\t"
    "v_lshl_or_b32 %11, v10, 4, v9\n\t"
    "v_lshrrev_b32 %12, 1, %12\n\t"
    "v_and_or_b32 %12, v0, %19, %12\n\t"
    "v_lshrrev_b32 %12, 1, %12\n\t"
    "v_and_or_b32 %12, v1, %19, %12\n\t"
    "v_lshrrev_b32 %12, 1, %12\n\t"
    "v_and_or_b32 %12, v7, %19, %12\n\t"
    "v_lshrrev_b32 %12, 1, %12\n\t"
    "v_and_or_b32 %12, v8, %19, %12\n\t"
    "v_pk_sub_u16 v32, %18, v32 clamp\n\t"
    "v_and_b32 v33, 0xf0f0f0f0, v33\n\t"
    "v_or3_b32 %13, %13, v32, v33\n\t"
    : "=&v"(pk[26][0]), "=&v"(pk[26][1]), "=&v"(pk[26][2]), "=&v"(pk[26][3]), "=&v"(pk[26][4]), "=&v"(pk[26][5]), "=&v"(pk[26][6]), "=&v"(pk[26][7]), "=&v"(pk[26][8]), "=&v"(pk[26][9]), "=&v"(pk[26][10]), "=&v"(pk[26][11]), "=&v"(pk[26][12]), "+v"(badv)
    : "v"(laddr), "s"(base2k), "s"(0x06040200u), "s"(0x07050301u), "s"(base2k + 0x00ff00ffu), "s"(0x80808080u), "v"(koff), "s"(kbase), "s"(ring)
    : "memory", "scc", "v0", "v1", "v2", "v3", "v4", "v5", "v6", "v7", "v8", "v9", "v10", "v11", "v12", "v13", "v14", "v15", "v16", "v17", "v18", "v19", "v20", "v21", "v22", "v23", "v24", "v25", "v26", "v27", "v28", "v29", "v30", "v31", "v32", "v33");
  lds_barrier();
  pace(tp, 2013);
  asm volatile(
    "s_waitcnt vmcnt(12)\n\t"
    "ds_read_b128 v[16:19], %14 offset:12288\n\t"
    "ds_read_b128 v[20:23], %14 offset:13312\n\t"
    "ds_read_b128 v[24:27], %14 offset:14336\n\t"
    "ds_read_b128 v[28:31], %14 offset:15360\n\t"
    "v_xor_b32 v15, 245760, %20\n\t"
    "s_add_u32 m0, %22, 28672\n\t"
    "s_nop 0\n\t"
    "global_load_lds_dwordx4 v15, %21 nt\n\t"
    "global_load_lds_dwordx4 v15, %21 offset:1024 nt\n\t"
    "global_load_lds_dwordx4 v15, %21 offset:2048 nt\n\t"
    "global_load_lds_dwordx4 v15, %21 offset:3072 nt\n\t"
    "v_mov_b32 v32, -1\n\t"
    "v_mov_b32 v33, 0\n\t"
    "s_waitcnt lgkmcnt(2)\n\t"
    "v_perm_b32 %0, v17, v16, %16\n\t"
    "v_perm_b32 %3, v21, v20, %16\n\t"
    "v_perm_b32 v0, v17, v16, %17\n\t"
    "v_perm_b32 v7, v21, v20, %17\n\t"
    "v_perm_b32 %1, v19, v18, %16\n\t"
    "v_perm_b32 %4, v23, v22, %16\n\t"
    "v_perm_b32 v1, v19, v18, %17\n\t"
    "v_perm_b32 v8, v23, v22, %17\n\t"
    "v_and_b32 v16, 0x7fff7fff, v16\n\t"
    "v_and_b32 v20, 0x7fff7fff, v20\n\t"
    "v_pk_add_u16 v2, v16, -1\n\t"
    "v_pk_add_u16 v9, v20, -1\n\t"
    "v_pk_sub_u16 v16, v16, %15 clamp\n\t"
    "v_pk_sub_u16 v20, v20, %15 clamp\n\t"
    "v_pk_min_u16 v32, v32, v2\n\t"
    "v_pk_min_u16 v32, v32, v9\n\t"
    "v_and_b32 v17, 0x7fff7fff, v17\n\t"
    "v_and_b32 v21, 0x7fff7fff, v21\n\t"
    "v_pk_add_u16 v3, v17, -1\n\t"
    "v_pk_add_u16 v10, v21, -1\n\t"
    "v_pk_sub_u16 v17, v17, %15 clamp\n\t"
    "v_pk_sub_u16 v21, v21, %15 clamp\n\t"
    "v_pk_min_u16 v32, v32, v3\n\t"
    "v_pk_min_u16 v32, v32, v10\n\t"
    "v_and_b32 v18, 0x7fff7fff, v18\n\t"
    "v_and_b32 v22, 0x7fff7fff, v22\n\t"
    "v_pk_add_u16 v2, v18, -1\n\t"
    "v_pk_add_u16 v9, v22, -1\n\t"
    "v_pk_sub_u16 v18, v18, %15 clamp\n\t"
    "v_pk_sub_u16 v22, v22, %15 clamp\n\t"
    "v_pk_min_u16 v32, v32, v2\n\t"
    "v_pk_min_u16 v32, v32, v9\n\t"
    "v_and_b32 v19, 0x7fff7fff, v19\n\t"
    "v_and_b32 v23, 0x7fff7fff, v23\n\t"
    "v_pk_add_u16 v3, v19, -1\n\t"
    "v_pk_add_u16 v10, v23, -1\n\t"
    "v_pk_sub_u16 v19, v19, %15 clamp\n\t"
    "v_pk_sub_u16 v23, v23, %15 clamp\n\t"
    "v_pk_min_u16 v32, v32, v3\n\t"
    "v_pk_min_u16 v32, v32, v10\n\t"
    "v_perm_b32 v2, v17, v16, %17\n\t"
    "v_perm_b32 v9, v21, v20, %17\n\t"
    "v_perm_b32 v3, v19, v18, %17\n\t"
    "v_perm_b32 v10, v23, v22, %17\n\t"
    "v_or3_b32 v33, v33, v2, v3\n\t"
    "v_or3_b32 v33, v33, v9, v10\n\t"
    "v_lshl_or_b32 %2, v3, 4, v2\n\t"
    "v_lshl_or_b32 %5, v10, 4, v9\n\t"
    "v_and_b32 %12, %19, v0\n\t"
    "v_lshrrev_b32 %12, 1, %12\n\t"
    "v_and_or_b32 %12, v1, %19, %12\n\t"
    "v_lshrrev_b32 %12, 1, %12\n\t"
    "v_and_or_b32 %12, v7, %19, %12\n\t"
    "v_lshrrev_b32 %12, 1, %12\n\t"
    "v_and_or_b32 %12, v8, %19, %12\n\t"
    "s_waitcnt lgkmcnt(0)\n\t"
    "v_perm_b32 %6, v25, v24, %16\n\t"
    "v_perm_b32 %9, v29, v28, %16\n\t"
    "v_perm_b32 v0, v25, v24, %17\n\t"
    "v_perm_b32 v7, v29, v28, %17\n\t"
    "v_perm_b32 %7, v27, v26, %16\n\t"
    "v_perm_b32 %10, v31, v30, %16\n\t"
    "v_perm_b32 v1, v27, v26, %17\n\t"
    "v_perm_b32 v8, v31, v30, %17\n\t"
    "v_and_b32 v24, 0x7fff7fff, v24\n\t"
    "v_and_b32 v28, 0x7fff7fff, v28\n\t"
    "v_pk_add_u16 v2, v24, -1\n\t"
    "v_pk_add_u16 v9, v28, -1\n\t"
    "v_pk_sub_u16 v24, v24, %15 clamp\n\t"
    "v_pk_sub_u16 v28, v28, %15 clamp\n\t"
    "v_pk_min_u16 v32, v32, v2\n\t"
    "v_pk_min_u16 v32, v32, v9\n\t"
    "v_and_b32 v25, 0x7fff7fff, v25\n\t"
    "v_and_b32 v29, 0x7fff7fff, v29\n\t"
    "v_pk_add_u16 v3, v25, -1\n\t"
    "v_pk_add_u16 v10, v29, -1\n\t"
    "v_pk_sub_u16 v25, v25, %15 clamp\n\t"
    "v_pk_sub_u16 v29, v29, %15 clamp\n\t"
    "v_pk_min_u16 v32, v32, v3\n\t"
    "v_pk_min_u16 v32, v32, v10\n\t"
    "v_and_b32 v26, 0x7fff7fff, v26\n\t"
    "v_and_b32 v30, 0x7fff7fff, v30\n\t"
    "v_pk_add_u16 v2, v26, -1\n\t"
    "v_pk_add_u16 v9, v30, -1\n\t"
    "v_pk_sub_u16 v26, v26, %15 clamp\n\t"
    "v_pk_sub_u16 v30, v30, %15 clamp\n\t"
    "v_pk_min_u16 v32, v32, v2\n\t"
    "v_pk_min_u16 v32, v32, v9\n\t"
    "v_and_b32 v27, 0x7fff7fff, v27\n\t"
    "v_and_b32 v31, 0x7fff7fff, v31\n\t"
    "v_pk_add_u16 v3, v27, -1\n\t"
    "v_pk_add_u16 v10, v31, -1\n\t"
    "v_pk_sub_u16 v27, v27, %15 clamp\n\t"
    "v_pk_sub_u16 v31, v31, %15 clamp\n\t"
    "v_pk_min_u16 v32, v32, v3\n\t"
    "v_pk_min_u16 v32, v32, v10\n\t"
    "v_perm_b32 v2, v25, v24, %17\n\t"
    "v_perm_b32 v9, v29, v28, %17\n\t"
    "v_perm_b32 v3, v27, v26, %17\n\t"
    "v_perm_b32 v10, v31, v30, %17\n\t"
    "v_or3_b32 v33, v33, v2, v3\n\t"
    "v_or3_b32 v33, v33, v9, v10\n\t"
    "v_lshl_or_b32 %8, v3, 4, v2\n\t"
    "v_lshl_or_b32 %11, v10, 4, v9\n\t"
    "v_lshrrev_b32 %12, 1, %12\n\t"
    "v_and_or_b32 %12, v0, %19, %12\n\t"
    "v_lshrrev_b32 %12, 1, %12\n\t"
    "v_and_or_b32 %12, v1, %19, %12\n\t"
    "v_lshrrev_b32 %12, 1, %12\n\t"
    "v_and_or_b32 %12, v7, %19, %12\n\t"
    "v_lshrrev_b32 %12, 1, %12\n\t"
    "v_and_or_b32 %12, v8, %19, %12\n\t"
    "v_pk_sub_u16 v32, %18, v32 clamp\n\t"
    "v_and_b32 v33, 0xf0f0f0f0, v33\n\t"
    "v_or3_b32 %13, %13, v32, v33\n\t"
    : "=&v"(pk[27][0]), "=&v"(pk[27][1]), "=&v"(pk[27][2]), "=&v"(pk[27][3]), "=&v"(pk[27][4]), "=&v"(pk[27][5]), "=&v"(pk[27][6]), "=&v"(pk[27][7]), "=&v"(pk[27][8]), "=&v"(pk[27][9]), "=&v"(pk[27][10]), "=&v"(pk[27][11]), "=&v"(pk[27][12]), "+v"(badv)
    : "v"(laddr), "s"(base2v), "s"(0x06040200u), "s"(0x07050301u), "s"(base2v + 0x00ff00ffu), "s"(0x80808080u), "v"(vofr), "s"(vbase), "s"(ring)
    : "memory", "scc", "v0", "v1", "v2", "v3", "v4", "v5", "v6", "v7", "v8", "v9", "v10", "v11", "v12", "v13", "v14", "v15", "v16", "v17", "v18", "v19", "v20", "v21", "v22", "v23", "v24", "v25", "v26", "v27", "v28", "v29", "v30", "v31", "v32", "v33");
  badmask |= (__any(badv != 0) ? 1u : 0u) << 13; badv = 0;
  pace(tp, 2074);
  asm volatile(
    "s_waitcnt vmcnt(12)\n\t"
    "ds_read_b128 v[16:19], %14 offset:16384\n\t"
    "ds_read_b128 v[20:23], %14 offset:17408\n\t"
    "ds_read_b128 v[24:27], %14 offset:18432\n\t"
    "ds_read_b128 v[28:31], %14 offset:19456\n\t"
    "v_xor_b32 v15, 262144, %20\n\t"
    "s_add_u32 m0, %22, 0\n\t"
    "s_nop 0\n\t"
    "global_load_lds_dwordx4 v15, %21 nt\n\t"
    "global_load_lds_dwordx4 v15, %21 offset:1024 nt\n\t"
    "global_load_lds_dwordx4 v15, %21 offset:2048 nt\n\t"
    "global_load_lds_dwordx4 v15, %21 offset:3072 nt\n\t"
    "v_mov_b32 v32, -1\n\t"
    "v_mov_b32 v33, 0\n\t"
    "s_waitcnt lgkmcnt(2)\n\t"
    "v_perm_b32 %0, v17, v16, %16\n\t"
    "v_perm_b32 %3, v21, v20, %16\n\t"
    "v_perm_b32 v0, v17, v16, %17\n\t"
    "v_perm_b32 v7, v21, v20, %17\n\t"
    "v_perm_b32 %1, v19, v18, %16\n\t"
    "v_perm_b32 %4, v23, v22, %16\n\t"
    "v_perm_b32 v1, v19, v18, %17\n\t"
    "v_perm_b32 v8, v23, v22, %17\n\t"
    "v_and_b32 v16, 0x7fff7fff, v16\n\t"
    "v_and_b32 v20, 0x7fff7fff, v20\n\t"
    "v_pk_add_u16 v2, v16, -1\n\t"
    "v_pk_add_u16 v9, v20, -1\n\t"
    "v_pk_sub_u16 v16, v16, %15 clamp\n\t"
    "v_pk_sub_u16 v20, v20, %15 clamp\n\t"
    "v_pk_min_u16 v32, v32, v2\n\t"
    "v_pk_min_u16 v32, v32, v9\n\t"
    "v_and_b32 v17, 0x7fff7fff, v17\n\t"
    "v_and_b32 v21, 0x7fff7fff, v21\n\t"
    "v_pk_add_u16 v3, v17, -1\n\t"
    "v_pk_add_u16 v10, v21, -1\n\t"
    "v_pk_sub_u16 v17, v17, %15 clamp\n\t"
    "v_pk_sub_u16 v21, v21, %15 clamp\n\t"
    "v_pk_min_u16 v32, v32, v3\n\t"
    "v_pk_min_u16 v32, v32, v10\n\t"
    "v_and_b32 v18, 0x7fff7fff, v18\n\t"
    "v_and_b32 v22, 0x7fff7fff, v22\n\t"
    "v_pk_add_u16 v2, v18, -1\n\t"
    "v_pk_add_u16 v9, v22, -1\n\t"
    "v_pk_sub_u16 v18, v18, %15 clamp\n\t"
    "v_pk_sub_u16 v22, v22, %15 clamp\n\t"
    "v_pk_min_u16 v32, v32, v2\n\t"
    "v_pk_min_u16 v32, v32, v9\n\t"
    "v_and_b32 v19, 0x7fff7fff, v19\n\t"
    "v_and_b32 v23, 0x7fff7fff, v23\n\t"
    "v_pk_add_u16 v3, v19, -1\n\t"
    "v_pk_add_u16 v10, v23, -1\n\t"
    "v_pk_sub_u16 v19, v19, %15 clamp\n\t"
    "v_pk_sub_u16 v23, v23, %15 clamp\n\t"
    "v_pk_min_u16 v32, v32, v3\n\t"
    "v_pk_min_u16 v32, v32, v10\n\t"
    "v_perm_b32 v2, v17, v16, %17\n\t"
    "v_perm_b32 v9, v21, v20, %17\n\t"
    "v_perm_b32 v3, v19, v18, %17\n\t"
    "v_perm_b32 v10, v23, v22, %17\n\t"
    "v_or3_b32 v33, v33, v2, v3\n\t"
    "v_or3_b32 v33, v33, v9, v10\n\t"
    "v_lshl_or_b32 %2, v3, 4, v2\n\t"
    "v_lshl_or_b32 %5, v10, 4, v9\n\t"
    "v_and_b32 %12, %19, v0\n\t"
    "v_lshrrev_b32 %12, 1, %12\n\t"
    "v_and_or_b32 %12, v1, %19, %12\n\t"
    "v_lshrrev_b32 %12, 1, %12\n\t"
    "v_and_or_b32 %12, v7, %19, %12\n\t"
    "v_lshrrev_b32 %12, 1, %12\n\t"
    "v_and_or_b32 %12, v8, %19, %12\n\t"
    "s_waitcnt lgkmcnt(0)\n\t"
    "v_perm_b32 %6, v25, v24, %16\n\t"
    "v_perm_b32 %9, v29, v28, %16\n\t"
    "v_perm_b32 v0, v25, v24, %17\n\t"
    "v_perm_b32 v7, v29, v28, %17\n\t"
    "v_perm_b32 %7, v27, v26, %16\n\t"
    "v_perm_b32 %10, v31, v30, %16\n\t"
    "v_perm_b32 v1, v27, v26, %17\n\t"
    "v_perm_b32 v8, v31, v30, %17\n\t"
    "v_and_b32 v24, 0x7fff7fff, v24\n\t"
    "v_and_b32 v28, 0x7fff7fff, v28\n\t"
    "v_pk_add_u16 v2, v24, -1\n\t"
    "v_pk_add_u16 v9, v28, -1\n\t"
    "v_pk_sub_u16 v24, v24, %15 clamp\n\t"
    "v_pk_sub_u16 v28, v28, %15 clamp\n\t"
    "v_pk_min_u16 v32, v32, v2\n\t"
    "v_pk_min_u16 v32, v32, v9\n\t"
    "v_and_b32 v25, 0x7fff7fff, v25\n\t"
    "v_and_b32 v29, 0x7fff7fff, v29\n\t"
    "v_pk_add_u16 v3, v25, -1\n\t"
    "v_pk_add_u16 v10, v29, -1\n\t"
    "v_pk_sub_u16 v25, v25, %15 clamp\n\t"
    "v_pk_sub_u16 v29, v29, %15 clamp\n\t"
    "v_pk_min_u16 v32, v32, v3\n\t"
    "v_pk_min_u16 v32, v32, v10\n\t"
    "v_and_b32 v26, 0x7fff7fff, v26\n\t"
    "v_and_b32 v30, 0x7fff7fff, v30\n\t"
    "v_pk_add_u16 v2, v26, -1\n\t"
    "v_pk_add_u16 v9, v30, -1\n\t"
    "v_pk_sub_u16 v26, v26, %15 clamp\n\t"
    "v_pk_sub_u16 v30, v30, %15 clamp\n\t"
    "v_pk_min_u16 v32, v32, v2\n\t"
    "v_pk_min_u16 v32, v32, v9\n\t"
    "v_and_b32 v27, 0x7fff7fff, v27\n\t"
    "v_and_b32 v31, 0x7fff7fff, v31\n\t"
    "v_pk_add_u16 v3, v27, -1\n\t"
    "v_pk_add_u16 v10, v31, -1\n\t"
    "v_pk_sub_u16 v27, v27, %15 clamp\n\t"
    "v_pk_sub_u16 v31, v31, %15 clamp\n\t"
    "v_pk_min_u16 v32, v32, v3\n\t"
    "v_pk_min_u16 v32, v32, v10\n\t"
    "v_perm_b32 v2, v25, v24, %17\n\t"
    "v_perm_b32 v9, v29, v28, %17\n\t"
    "v_perm_b32 v3, v27, v26, %17\n\t"
    "v_perm_b32 v10, v31, v30, %17\n\t"
    "v_or3_b32 v33, v33, v2, v3\n\t"
    "v_or3_b32 v33, v33, v9, v10\n\t"
    "v_lshl_or_b32 %8, v3, 4, v2\n\t"
    "v_lshl_or_b32 %11, v10, 4, v9\n\t"
    "v_lshrrev_b32 %12, 1, %12\n\t"
    "v_and_or_b32 %12, v0, %19, %12\n\t"
    "v_lshrrev_b32 %12, 1, %12\n\t"
    "v_and_or_b32 %12, v1, %19, %12\n\t"
    "v_lshrrev_b32 %12, 1, %12\n\t"
    "v_and_or_b32 %12, v7, %19, %12\n\t"
    "v_lshrrev_b32 %12, 1, %12\n\t"
    "v_and_or_b32 %12, v8, %19, %12\n\t"
    "v_pk_sub_u16 v32, %18, v32 clamp\n\t"
    "v_and_b32 v33, 0xf0f0f0f0, v33\n\t"
    "v_or3_b32 %13, %13, v32, v33\n\t"
    : "=&v"(pk[28][0]), "=&v"(pk[28][1]), "=&v"(pk[28][2]), "=&v"(pk[28][3]), "=&v"(pk[28][4]), "=&v"(pk[28][5]), "=&v"(pk[28][6]), "=&v"(pk[28][7]), "=&v"(pk[28][8]), "=&v"(pk[28][9]), "=&v"(pk[28][10]), "=&v"(pk[28][11]), "=&v"(pk[28][12]), "+v"(badv)
    : "v"(laddr), "s"(base2k), "s"(0x06040200u), "s"(0x07050301u), "s"(base2k + 0x00ff00ffu), "s"(0x80808080u), "v"(koff), "s"(kbase), "s"(ring)
    : "memory", "scc", "v0", "v1", "v2", "v3", "v4", "v5", "v6", "v7", "v8", "v9", "v10", "v11", "v12", "v13", "v14", "v15", "v16", "v17", "v18", "v19", "v20", "v21", "v22", "v23", "v24", "v25", "v26", "v27", "v28", "v29", "v30", "v31", "v32", "v33");
  pace(tp, 2135);
  asm volatile(
    "s_waitcnt vmcnt(12)\n\t"
    "ds_read_b128 v[16:19], %14 offset:20480\n\t"
    "ds_read_b128 v[20:23], %14 offset:21504\n\t"
    "ds_read_b128 v[24:27], %14 offset:22528\n\t"
    "ds_read_b128 v[28:31], %14 offset:23552\n\t"
    "v_xor_b32 v15, 262144, %20\n\t"
    "s_add_u32 m0, %22, 4096\n\t"
    "s_nop 0\n\t"
    "global_load_lds_dwordx4 v15, %21 nt\n\t"
    "global_load_lds_dwordx4 v15, %21 offset:1024 nt\n\t"
    "global_load_lds_dwordx4 v15, %21 offset:2048 nt\n\t"
    "global_load_lds_dwordx4 v15, %21 offset:3072 nt\n\t"
    "v_mov_b32 v32, -1\n\t"
    "v_mov_b32 v33, 0\n\t"
    "s_waitcnt lgkmcnt(2)\n\t"
    "v_perm_b32 %0, v17, v16, %16\n\t"
    "v_perm_b32 %3, v21, v20, %16\n\t"
    "v_perm_b32 v0, v17, v16, %17\n\t"
    "v_perm_b32 v7, v21, v20, %17\n\t"
    "v_perm_b32 %1, v19, v18, %16\n\t"
    "v_perm_b32 %4, v23, v22, %16\n\t"
    "v_perm_b32 v1, v19, v18, %17\n\t"
    "v_perm_b32 v8, v23, v22, %17\n\t"
    "v_and_b32 v16, 0x7fff7fff, v16\n\t"
    "v_and_b32 v20, 0x7fff7fff, v20\n\t"
    "v_pk_add_u16 v2, v16, -1\n\t"
    "v_pk_add_u16 v9, v20, -1\n\t"
    "v_pk_sub_u16 v16, v16, %15 clamp\n\t"
    "v_pk_sub_u16 v20, v20, %15 clamp\n\t"
    "v_pk_min_u16 v32, v32, v2\n\t"
    "v_pk_min_u16 v32, v32, v9\n\t"
    "v_and_b32 v17, 0x7fff7fff, v17\n\t"
    "v_and_b32 v21, 0x7fff7fff, v21\n\t"
    "v_pk_add_u16 v3, v17, -1\n\t"
    "v_pk_add_u16 v10, v21, -1\n\t"
    "v_pk_sub_u16 v17, v17, %15 clamp\n\t"
    "v_pk_sub_u16 v21, v21, %15 clamp\n\t"
    "v_pk_min_u16 v32, v32, v3\n\t"
    "v_pk_min_u16 v32, v32, v10\n\t"
    "v_and_b32 v18, 0x7fff7fff, v18\n\t"
    "v_and_b32 v22, 0x7fff7fff, v22\n\t"
    "v_pk_add_u16 v2, v18, -1\n\t"
    "v_pk_add_u16 v9, v22, -1\n\t"
    "v_pk_sub_u16 v18, v18, %15 clamp\n\t"
    "v_pk_sub_u16 v22, v22, %15 clamp\n\t"
    "v_pk_min_u16 v32, v32, v2\n\t"
    "v_pk_min_u16 v32, v32, v9\n\t"
    "v_and_b32 v19, 0x7fff7fff, v19\n\t"
    "v_and_b32 v23, 0x7fff7fff, v23\n\t"
    "v_pk_add_u16 v3, v19, -1\n\t"
    "v_pk_add_u16 v10, v23, -1\n\t"
    "v_pk_sub_u16 v19, v19, %15 clamp\n\t"
    "v_pk_sub_u16 v23, v23, %15 clamp\n\t"
    "v_pk_min_u16 v32, v32, v3\n\t"
    "v_pk_min_u16 v32, v32, v10\n\t"
    "v_perm_b32 v2, v17, v16, %17\n\t"
    "v_perm_b32 v9, v21, v20, %17\n\t"
    "v_perm_b32 v3, v19, v18, %17\n\t"
    "v_perm_b32 v10, v23, v22, %17\n\t"
    "v_or3_b32 v33, v33, v2, v3\n\t"
    "v_or3_b32 v33, v33, v9, v10\n\t"
    "v_lshl_or_b32 %2, v3, 4, v2\n\t"
    "v_lshl_or_b32 %5, v10, 4, v9\n\t"
    "v_and_b32 %12, %19, v0\n\t"
    "v_lshrrev_b32 %12, 1, %12\n\t"
    "v_and_or_b32 %12, v1, %19, %12\n\t"
    "v_lshrrev_b32 %12, 1, %12\n\t"
    "v_and_or_b32 %12, v7, %19, %12\n\t"
    "v_lshrrev_b32 %12, 1, %12\n\t"
    "v_and_or_b32 %12, v8, %19, %12\n\t"
    "s_waitcnt lgkmcnt(0)\n\t"
    "v_perm_b32 %6, v25, v24, %16\n\t"
    "v_perm_b32 %9, v29, v28, %16\n\t"
    "v_perm_b32 v0, v25, v24, %17\n\t"
    "v_perm_b32 v7, v29, v28, %17\n\t"
    "v_perm_b32 %7, v27, v26, %16\n\t"
    "v_perm_b32 %10, v31, v30, %16\n\t"
    "v_perm_b32 v1, v27, v26, %17\n\t"
    "v_perm_b32 v8, v31, v30, %17\n\t"
    "v_and_b32 v24, 0x7fff7fff, v24\n\t"
    "v_and_b32 v28, 0x7fff7fff, v28\n\t"
    "v_pk_add_u16 v2, v24, -1\n\t"
    "v_pk_add_u16 v9, v28, -1\n\t"
    "v_pk_sub_u16 v24, v24, %15 clamp\n\t"
    "v_pk_sub_u16 v28, v28, %15 clamp\n\t"
    "v_pk_min_u16 v32, v32, v2\n\t"
    "v_pk_min_u16 v32, v32, v9\n\t"
    "v_and_b32 v25, 0x7fff7fff, v25\n\t"
    "v_and_b32 v29, 0x7fff7fff, v29\n\t"
    "v_pk_add_u16 v3, v25, -1\n\t"
    "v_pk_add_u16 v10, v29, -1\n\t"
    "v_pk_sub_u16 v25, v25, %15 clamp\n\t"
    "v_pk_sub_u16 v29, v29, %15 clamp\n\t"
    "v_pk_min_u16 v32, v32, v3\n\t"
    "v_pk_min_u16 v32, v32, v10\n\t"
    "v_and_b32 v26, 0x7fff7fff, v26\n\t"
    "v_and_b32 v30, 0x7fff7fff, v30\n\t"
    "v_pk_add_u16 v2, v26, -1\n\t"
    "v_pk_add_u16 v9, v30, -1\n\t"
    "v_pk_sub_u16 v26, v26, %15 clamp\n\t"
    "v_pk_sub_u16 v30, v30, %15 clamp\n\t"
    "v_pk_min_u16 v32, v32, v2\n\t"
    "v_pk_min_u16 v32, v32, v9\n\t"
    "v_and_b32 v27, 0x7fff7fff, v27\n\t"
    "v_and_b32 v31, 0x7fff7fff, v31\n\t"
    "v_pk_add_u16 v3, v27, -1\n\t"
    "v_pk_add_u16 v10, v31, -1\n\t"
    "v_pk_sub_u16 v27, v27, %15 clamp\n\t"
    "v_pk_sub_u16 v31, v31, %15 clamp\n\t"
    "v_pk_min_u16 v32, v32, v3\n\t"
    "v_pk_min_u16 v32, v32, v10\n\t"
    "v_perm_b32 v2, v25, v24, %17\n\t"
    "v_perm_b32 v9, v29, v28, %17\n\t"
    "v_perm_b32 v3, v27, v26, %17\n\t"
    "v_perm_b32 v10, v31, v30, %17\n\t"
    "v_or3_b32 v33, v33, v2, v3\n\t"
    "v_or3_b32 v33, v33, v9, v10\n\t"
    "v_lshl_or_b32 %8, v3, 4, v2\n\t"
    "v_lshl_or_b32 %11, v10, 4, v9\n\t"
    "v_lshrrev_b32 %12, 1, %12\n\t"
    "v_and_or_b32 %12, v0, %19, %12\n\t"
    "v_lshrrev_b32 %12, 1, %12\n\t"
    "v_and_or_b32 %12, v1, %19, %12\n\t"
    "v_lshrrev_b32 %12, 1, %12\n\t"
    "v_and_or_b32 %12, v7, %19, %12\n\t"
    "v_lshrrev_b32 %12, 1, %12\n\t"
    "v_and_or_b32 %12, v8, %19, %12\n\t"
    "v_pk_sub_u16 v32, %18, v32 clamp\n\t"
    "v_and_b32 v33, 0xf0f0f0f0, v33\n\t"
    "v_or3_b32 %13, %13, v32, v33\n\t"
    : "=&v"(pk[29][0]), "=&v"(pk[29][1]), "=&v"(pk[29][2]), "=&v"(pk[29][3]), "=&v"(pk[29][4]), "=&v"(pk[29][5]), "=&v"(pk[29][6]), "=&v"(pk[29][7]), "=&v"(pk[29][8]), "=&v"(pk[29][9]), "=&v"(pk[29][10]), "=&v"(pk[29][11]), "=&v"(pk[29][12]), "+v"(badv)
    : "v"(laddr), "s"(base2v), "s"(0x06040200u), "s"(0x07050301u), "s"(base2v + 0x00ff00ffu), "s"(0x80808080u), "v"(vofr), "s"(vbase), "s"(ring)
    : "memory", "scc", "v0", "v1", "v2", "v3", "v4", "v5", "v6", "v7", "v8", "v9", "v10", "v11", "v12", "v13", "v14", "v15", "v16", "v17", "v18", "v19", "v20", "v21", "v22", "v23", "v24", "v25", "v26", "v27", "v28", "v29", "v30", "v31", "v32", "v33");
  badmask |= (__any(badv != 0) ? 1u : 0u) << 14; badv = 0;
  STAMP(3);

  const int q4 = lane & 3;
  const u32 mask0 = q4 == 0 ? 0xffffu : q4 == 1 ? 0xffff0000u : 0u, mask1 = q4 == 2 ? 0xffffu : q4 == 3 ? 0xffff0000u : 0u;
  const u64 modd = 0xaaaaaaaaaaaaaaaaull, mhi = 0xccccccccccccccccull;

#pragma unroll 1
  for (int pass = 0; pass < 2; ++pass) {
    const u32 tag = pass + 1;
    f32x4 acc0 = {0.f, 0.f, 0.f, 0.f}, acc1 = {0.f, 0.f, 0.f, 0.f};
    float m = -3.0e38f, l = 0.f, negmc = 0.f;
    auto raise_max = [&](float mx) {
      mx = fmaxf(mx, dppf<XOR1>(mx)); mx = fmaxf(mx, dppf<XOR2>(mx)); mx = rows_max(mx);
      float mn = fmaxf(m, mx);
      float alpha = __builtin_amdgcn_exp2f((m - mn) * CS);
      l *= alpha; m = mn; negmc = -mn * CS;
      int ai = __builtin_bit_cast(int, alpha), ar = ai;
      ar = __builtin_amdgcn_update_dpp(ar, ai, 0x150, 0x1, 0xf, false);
      ar = __builtin_amdgcn_update_dpp(ar, ai, 0x154, 0x2, 0xf, false);
      ar = __builtin_amdgcn_update_dpp(ar, ai, 0x158, 0x4, 0xf, false);
      ar = __builtin_amdgcn_update_dpp(ar, ai, 0x15c, 0x8, 0xf, false);
      float a = __builtin_bit_cast(float, ar);
      acc0 *= a; acc1 *= a;
    };
    auto fallback = [&]() {
      asm volatile("s_nop 7" : "+v"(acc0), "+v"(acc1));
#pragma unroll 1
    for (u32 mk = badmask; mk != 0; mk &= mk - 1) {
      const u32 bo = (u32)__builtin_ctz(mk) * 16384u;
      u32x4 t4[4]; float X1[1];
      asm volatile("global_load_dwordx4 %0, %4, %5\n\tglobal_load_dwordx4 %1, %4, %5 offset:1024\n\tglobal_load_dwordx4 %2, %4, %5 offset:2048\n\t"
                   "global_load_dwordx4 %3, %4, %5 offset:3072\n\ts_waitcnt vmcnt(0)"
                   : "=&v"(t4[0]), "=&v"(t4[1]), "=&v"(t4[2]), "=&v"(t4[3]) : "v"(koff ^ bo), "s"(kbase) : "memory");
      qk_one(X1, t4, qf, modd, mhi);
      if (__any(X1[0] > m)) raise_max(X1[0]);
      float pv = __builtin_amdgcn_exp2f(fmaf(X1[0], CS, negmc)); l += pv;
      u32 pq[1] = {pk_bf16(pv, pv)};
      asm volatile("global_load_dwordx4 %0, %4, %5\n\tglobal_load_dwordx4 %1, %4, %5 offset:1024\n\tglobal_load_dwordx4 %2, %4, %5 offset:2048\n\t"
                   "global_load_dwordx4 %3, %4, %5 offset:3072\n\ts_waitcnt vmcnt(0)"
                   : "=&v"(t4[0]), "=&v"(t4[1]), "=&v"(t4[2]), "=&v"(t4[3]) : "v"(vofr ^ bo), "s"(vbase) : "memory");
      pv_one(acc0, acc1, pq, t4, mask0, mask1);
    }
    };
    u32x4 qs;
#pragma unroll
    for (int i = 0; i < 4; ++i) qs[i] = pk_bf16(bf_lo(qf[i]) * 0x1p64f, bf_hi(qf[i]) * 0x1p64f);
#define SJOB(B) do { u32 bm_ = badmask; asm volatile("" : "+s"(bm_)); if (__builtin_expect(!((bm_ >> (B)) & 1u), 1)) { float X1; sk##B(X1, pk, qs, modd, mhi); X1 *= sck; \
      if (__builtin_expect(__any(X1 > m), 0)) raise_max(X1); \
      float pv = __builtin_amdgcn_exp2f(fmaf(X1, CS, negmc)); l += pv; pv *= scv; \
      sv##B(acc0, acc1, pk, pk_bf16(pv, pv), mask0, mask1); } } while (0)
#define UJOB(WK, WV, SLOT, MD0, BO0, LO0, MD1, BO1, LO1, DUEK, DUEV) do { \
      u32 ps_ = pass; asm volatile("" : "+s"(ps_)); \
      const u32 md_ = (u32)(MD0) + ps_ * (u32)((MD1) - (MD0)), bo_ = (u32)(BO0) + ps_ * (u32)((BO1) - (BO0)), m0_ = ring + (u32)(LO0) + ps_ * (u32)((LO1) - (LO0)); \
      pace(tp, DUEK); \
      float X1; uk<WK, SLOT>(X1, qf, modd, mhi, laddr, koff, kbase, m0_, bo_, md_); \
      if (__builtin_expect(__any(X1 > m), 0)) raise_max(X1); \
      float pv = __builtin_amdgcn_exp2f(fmaf(X1, CS, negmc)); l += pv; \
      pace(tp, DUEV); \
      uv<WV, SLOT + 1>(acc0, acc1, pk_bf16(pv, pv), mask0, mask1, laddr, laddr8, vofr, vbase, m0_ + 4096u, bo_, md_); } while (0)
    if (__builtin_expect(pass == 1 && badmask != 0, 0)) fallback();
    UJOB(12, 12, 6, 2, 278528, 8192, 3, 245760, 24576, 2196, 2257);
    SJOB(0);
    UJOB(12, 12, 0, 1, 294912, 16384, 3, 262144, 0, 2318, 2379);
    SJOB(1);
    UJOB(12, 12, 2, 2, 311296, 24576, 0, 0, 0, 2440, 2501);
    if (__builtin_expect(pass == 0 && badmask != 0, 0)) fallback();
    SJOB(2);
    UJOB(12, 12, 4, 1, 327680, 0, 0, 0, 0, 2562, 2623);
    SJOB(3);
    UJOB(12, 12, 6, 2, 344064, 8192, 1, 294912, 8192, 2684, 2745);
    SJOB(4);
    UJOB(12, 12, 0, 1, 360448, 16384, 1, 278528, 16384, 2806, 2867);
    SJOB(5);
    UJOB(12, 12, 2, 1, 376832, 24576, 1, 327680, 24576, 2928, 2989);
    SJOB(6);
    UJOB(12, 12, 4, 1, 393216, 0, 1, 360448, 0, 3050, 3111);
    SJOB(7);
    UJOB(12, 12, 6, 1, 409600, 8192, 1, 376832, 8192, 3172, 3233);
    SJOB(8);
    UJOB(12, 12, 0, 1, 425984, 16384, 1, 311296, 16384, 3294, 3355);
    SJOB(9);
    UJOB(12, 12, 2, 1, 442368, 24576, 1, 393216, 24576, 3416, 3477);
    SJOB(10);
    UJOB(12, 12, 4, 1, 458752, 0, 1, 409600, 0, 3538, 3599);
    SJOB(11);
    UJOB(12, 12, 6, 1, 475136, 8192, 1, 425984, 8192, 3660, 3721);
    SJOB(12);
    UJOB(12, 12, 0, 1, 491520, 16384, 1, 344064, 16384, 3782, 3843);
    code_prefetch((u32)pass);
    SJOB(13);
    UJOB(12, 12, 2, 1, 507904, 24576, 1, 442368, 24576, 3904, 3965);
    SJOB(14);
    UJOB(12, 8, 4, 0, 0, 0, 0, 0, 0, 0, 0);
    UJOB(4, 0, 6, 0, 0, 0, 0, 0, 0, 0, 0);
    asm volatile("" : "+v"(cpf));
    asm volatile("s_nop 7" : "+v"(acc0), "+v"(acc1));
    STAMP(6 + 12 * pass);
    u32 zz = 0; asm volatile("" : "+s"(zz));
    LAS float* obuf_ = obuf + zz; LAS float* mlbuf_ = mlbuf + zz; LAS float* red_q_ = red_q + zz; LAS float* red_o_ = red_o + zz; LAS float* red_g_ = red_g + zz;
    LAS float* red_d_ = red_d + zz; LAS float* cs_q_ = cs_q + zz; LAS float* red_h_ = red_h + zz; LAS float* cs_g_ = cs_g + zz; LAS u32x4* wq_lds_ = wq_lds + zz;
    int lane = lane_id();
    const int tid = w * 64 + lane, q4 = lane & 3;
    const u32 off_g1 = (OFF_G1 + zz) + ((((gam * 4 + w) * 8 + (lane >> 3)) * 32 + s) * 16 + 2 * (lane & 7)) * 8;
    const u32 off_q = (OFF_G6 + zz) + gam * 2048 + lane * 16;
    {
      float lh = l + dppf<XOR1>(l); lh += dppf<XOR2>(lh); lh = swap16_sum(lh, lh); lh = swap32_sum(lh, lh);
      if (lane < 16 && q4 == 0) *(LAS f32x2*)(mlbuf_ + (w * 4 + (lane >> 2)) * 2) = f32x2{m * CS, lh};
      LAS f32x4* od = (LAS f32x4*)(obuf_ + w * 512 + (lane >> 4) * 128 + (lane & 15) * 8);
      od[0] = acc0; od[1] = acc1;
      lds_barrier();
      float mw[4], lw[4]; f32x2 ow[4];
#pragma unroll
      for (int ww = 0; ww < 4; ++ww) { f32x2 t2 = *(LAS f32x2*)(mlbuf_ + (ww * 4 + w) * 2); mw[ww] = t2[0]; lw[ww] = t2[1]; ow[ww] = *(LAS f32x2*)(obuf_ + ww * 512 + w * 128 + 2 * lane); }
      float M = fmaxf(fmaxf(mw[0], mw[1]), fmaxf(mw[2], mw[3]));
      float L = 0.f, o0 = 0.f, o1 = 0.f;
#pragma unroll
      for (int ww = 0; ww < 4; ++ww) { float f = __builtin_amdgcn_exp2f(mw[ww] - M); L = fmaf(f, lw[ww], L); o0 = fmaf(f, ow[ww][0], o0); o1 = fmaf(f, ow[ww][1], o1); }
      const u32 ex = __float_as_uint((lane & 1) ? L : M);
      st16_sc1(off_g1, u32x4{__float_as_uint(o0), (ex & 0xffff0000u) | tag, __float_as_uint(o1), (ex << 16) | tag}, ws);
    }
    STAMP(7 + 12 * pass);
    {
      u32x4 g;
      delay(DLY1);
      for (u32 sp = 0; sp < SPIN_CAP; ++sp) {
        asm volatile("global_load_dwordx4 %0, %1, %2 sc1\n\ts_waitcnt vmcnt(0)" : "=&v"(g) : "v"((OFF_G1 + zz) + cl * 4096 + tid * 16), "s"(ws) : "memory");
        if (__all((((g[1] ^ tag) | (g[3] ^ tag)) & 0xffffu) == 0)) break;
      }
      const float e = __uint_as_float((g[1] & 0xffff0000u) | (g[3] >> 16)), ms = dppf<0xA0>(e);
      float Mw = fmaxf(ms, dppf<ROR8>(ms)); Mw = rows_max(Mw);
      const float f = __builtin_amdgcn_exp2f(ms - Mw);
      float a = f * __uint_as_float(g[0]), b = f * __uint_as_float(g[2]), L = f * e;
      a += dppf<ROR8>(a); b += dppf<ROR8>(b); L += dppf<ROR8>(L);
      float ab = swap16_sum(a, b); L = swap16_sum(L, L);
      ab = swap32_sum(ab, L);
      if (lane == 48) ab = Mw;
      if ((lane & 8) == 0 && (lane < 32 || lane == 33 || lane == 48)) red_h_[w * 20 + (lane < 32 ? (lane & 7) * 2 + (lane >> 4) : lane == 33 ? 17 : 16)] = ab;
      lds_barrier();
      f32x2 ml[4], ov[4];
#pragma unroll
      for (int ww = 0; ww < 4; ++ww) { ml[ww] = *(LAS f32x2*)(red_h_ + ww * 20 + 16); ov[ww] = *(LAS f32x2*)(red_h_ + ww * 20 + 2 * (lane & 7)); }
      const float M = fmaxf(fmaxf(ml[0][0], ml[1][0]), fmaxf(ml[2][0], ml[3][0]));
      float oa = 0.f, ob = 0.f, Lt = 0.f;
#pragma unroll
      for (int ww = 0; ww < 4; ++ww) { float fw = __builtin_amdgcn_exp2f(ml[ww][0] - M); oa = fmaf(fw, ov[ww][0], oa); ob = fmaf(fw, ov[ww][1], ob); Lt = fmaf(fw, ml[ww][1], Lt); }
      const float inv = __builtin_amdgcn_rcpf(Lt);
      u32 op = pk_bf16(oa * inv, ob * inv);
      float y0 = 0.f, y1 = 0.f;
#pragma unroll
      for (int j = 0; j < 8; ++j) { u32 o2r = __builtin_amdgcn_readlane(op, j); y0 = dot2(wo_p[2 * j], o2r, y0); y1 = dot2(wo_p[2 * j + 1], o2r, y1); }
      st8_sc1((OFF_G2 + zz) + ((tid >> 1) * 256 + c) * 16 + (tid & 1) * 8, pack24(y0, y1, tag), ws);
    }
    STAMP(8 + 12 * pass);
    if (dest) {
      delay(DLY2);
      reduce_inbox((OFF_G2 + zz) + c * 4096, 256, ws, tag, w, lane, red_o_, xo0, xo1);
      if (tid < 3) st8_sc1(tid < 2 ? (OFF_G3 + zz) + c * XS + tid * 8 : (OFF_SX + zz) + c * 128, u32x2{tid == 0 ? xo0 : tid == 1 ? xo1 : 2 * tag - 1, tag}, ws);
      delay(DLY3);
    } else if (ffn_on) poll_word((OFF_SX + zz) + (c - 128) * 128, ws, 2 * tag - 1, w);
    STAMP(9 + 12 * pass);
    if (ffn_on) {
      const f32x2 cg = sum4(cs_g_ + 2 * q4, 16), cu = sum4(cs_g_ + 8 + 2 * q4, 16);
      u32 xp = poll_pair((OFF_G3 + zz) + (tid >> 1) * XS + (tid & 1) * 8, ws, tag);
      gemv_a(xp, gu_p, sh, w, lane, red_g_);
      lds_barrier();
      float mean, rstd; ln_stats(red_g_, sh, mean, rstd);
      const f32x2 tg = sum4(red_g_ + 2 * q4, 20), tu = sum4(red_g_ + 8 + 2 * q4, 20);
      float g0 = rstd * fmaf(-mean, cg[0], tg[0]), g1 = rstd * fmaf(-mean, cg[1], tg[1]);
      float u0 = rstd * fmaf(-mean, cu[0], tu[0]), u1 = rstd * fmaf(-mean, cu[1], tu[1]);
      g0 = rbf(g0); g1 = rbf(g1); u0 = rbf(u0); u1 = rbf(u1);
      g0 = g0 > 0.f ? g0 : g0 * 0.f; g1 = g1 > 0.f ? g1 : g1 * 0.f;
      u32 hp = pk_bf16(g0 * u0, g1 * u1);
      float y0 = 0.f, y1 = 0.f;
#pragma unroll
      for (int j = 0; j < 4; ++j) { u32 h2 = __builtin_amdgcn_readlane(hp, j); y0 = dot2(wd_p[2 * j], h2, y0); y1 = dot2(wd_p[2 * j + 1], h2, y1); }
      st8_sc1((OFF_G4 + zz) + ((tid >> 1) * 256 + tau) * 16 + (tid & 1) * 8, pack24(y0, y1, tag), ws);
    }
    STAMP(10 + 12 * pass);
    if (dest) {
      delay(DLY4);
      reduce_inbox((OFF_G4 + zz) + c * 4096, 224, ws, tag, w, lane, red_d_, xo0, xo1);
      if (pass == 1) { if (tid == 0) *(u32x2*)(p.out + c * 8) = u32x2{xo0, xo1}; }
      else if (tid < 3) st8_sc1(tid < 2 ? (OFF_G5 + zz) + c * XS + tid * 8 : (OFF_SX + zz) + c * 128, u32x2{tid == 0 ? xo0 : tid == 1 ? xo1 : 2u, 1u}, ws);
    }
    STAMP(11 + 12 * pass);
    if (pass == 1) { STAMP_FLUSH(); break; }
    {
      u32 wq_p[16];
#pragma unroll
      for (int j = 0; j < 4; ++j) { u32x4 t4 = wq_lds_[j * 256 + tid]; wq_p[4 * j] = t4[0]; wq_p[4 * j + 1] = t4[1]; wq_p[4 * j + 2] = t4[2]; wq_p[4 * j + 3] = t4[3]; }
      const f32x2 cq = sum4(cs_q_ + 2 * (lane & 31), 64);
      if (dest) delay(DLY5); else poll_word((OFF_SX + zz) + (c - 128) * 128, ws, 2u, w);
      u32 xs, xg;
      poll_pair2(xs, xg, (OFF_G5 + zz) + (tid >> 1) * XS + (tid & 1) * 8, (OFF_G5 + zz) + (32 * rb + (tid >> 3)) * XS + ((tid >> 2) & 1) * 8, ws, 1u);
      qproj_a(xs, xg, wq_p, sh, w, lane, red_q_);
      lds_barrier();
      qproj_b(w, lane, red_q_, cq, sh, (OFF_G6 + zz) + q_slot(rb, cb, lane & 31), 2u, ws);
    }
    STAMP(12);
    {
      u32x4 g[8];
      delay(DLY6);
      for (u32 sp = 0; sp < SPIN_CAP; ++sp) { q_load(g, off_q, ws); if (__all(q_ok(g, 2u))) break; }
      qf = q_sum(g);
    }
    STAMP(13);
  }
}

extern "C" void kernel_launch(void* const* d_in, const int* in_sizes, int n_in, void* d_out, int out_size, void* d_ws, size_t ws_size, hipStream_t stream) {
  Params p{(const char*)d_in[0], (const char*)d_in[1], (const char*)d_in[2], (const char*)d_in[3], (const char*)d_in[4],
           (const char*)d_in[6], (const char*)d_in[7], (const char*)d_in[8], (char*)d_out, (char*)d_ws};
  mega<<<256, 256, 0, stream>>>(p);
}
```
